```python
import torch

def _softmax(x, axis=-1):
    return torch.softmax(x, dim=axis)
def _gelu(x):
    return torch.nn.functional.gelu(x, approximate="tanh")
def _silu(x):
    return torch.nn.functional.silu(x)
def _sigmoid(x):
    return torch.sigmoid(x)
def _layernorm(x, axis=-1, eps=1e-5):
    m = x.mean(dim=axis, keepdim=True)
    v = x.var(dim=axis, keepdim=True, unbiased=False)
    return (x - m) / torch.sqrt(v + eps)
def _scatter_add(tgt, idx, src, axis=0):
    return tgt.clone().index_add_(axis, idx, src)
def _segment_sum(src, seg, n):
    out = torch.zeros((n,) + src.shape[1:], dtype=src.dtype)
    return out.index_add_(0, seg, src)
def _pad(x, pw):
    flat = tuple(v for pair in reversed(pw) for v in pair)
    return torch.nn.functional.pad(x, flat)

kernel_name = 'sequential_preLN__linear_kernel_hedgehog__mixer_token_mlp__prompt'

def setup_inputs():

    _w = (lambda a: (lambda t: t.to(torch.bfloat16) if t.is_floating_point() else t)(torch.as_tensor(a)))
    def _A_setup():
        B, S, D, H = 1, 2048, 576, 32
        Dh = D // H
        return {
            "x": _w((lambda *s: torch.randn(*s, dtype=torch.bfloat16) * 0.05)(B, S, D)),
            "wqkv": _w((lambda *s: torch.randn(*s, dtype=torch.bfloat16) * 0.2)(D, 3 * D)),
            "w_phi": _w((lambda *s: torch.randn(*s, dtype=torch.bfloat16) * 0.2)(H, Dh, Dh) * (Dh ** -0.5)),
            "wo": _w((lambda *s: torch.randn(*s, dtype=torch.bfloat16) * 0.05)(D, D)),
        }
    _A_in = _A_setup()

    def _B_setup():
        B, S, D, F = 1, 2048, 576, 4608
        return {
            "x": _w((lambda *s: torch.randn(*s, dtype=torch.bfloat16) * 0.05)(B, S, D)),
            "w1": _w((lambda *s: torch.randn(*s, dtype=torch.bfloat16) * 0.4)(S, F) * (S ** -0.5)),
            "b1": _w((lambda *s: torch.randn(*s, dtype=torch.bfloat16) * 12.8)(F) * 0.02),
            "w2": _w((lambda *s: torch.randn(*s, dtype=torch.bfloat16) * 0.4)(F, S) * (F ** -0.5)),
            "b2": _w((lambda *s: torch.randn(*s, dtype=torch.bfloat16) * 6.4)(S) * 0.02),
        }
    _B_in = _B_setup()

    return {
        "A_x": _A_in["x"],
        "A_wqkv": _A_in["wqkv"],
        "A_w_phi": _A_in["w_phi"],
        "A_wo": _A_in["wo"],
        "B_x": _B_in["x"],
        "B_w1": _B_in["w1"],
        "B_b1": _B_in["b1"],
        "B_w2": _B_in["w2"],
        "B_b2": _B_in["b2"],
    }

def reference(A_x, A_wqkv, A_w_phi, A_wo, B_x, B_w1, B_b1, B_w2, B_b2):
    def _A_ref(x, wqkv, w_phi, wo):
        B, S, D = x.shape
        H = 32; Dh = D // H
        qkv = (lambda x, *d: x.permute(*d))((x @ wqkv).reshape(B, S, 3, H, Dh), 2, 0, 3, 1, 4)
        q, k, v = qkv[0], qkv[1], qkv[2]
        pq = torch.einsum("bhsd,hdf->bhsf", q, w_phi)
        pk = torch.einsum("bhsd,hdf->bhsf", k, w_phi)
        phi_q = torch.concatenate([_softmax(pq, axis=-1), _softmax(-pq, axis=-1)], axis=-1)
        phi_k = torch.concatenate([_softmax(pk, axis=-1), _softmax(-pk, axis=-1)], axis=-1)
        kv = torch.einsum("bhsf,bhsd->bhfd", phi_k, v)
        z  = torch.einsum("bhsf->bhf", phi_k)
        num = torch.einsum("bhqf,bhfd->bhqd", phi_q, kv)
        den = torch.einsum("bhqf,bhf->bhq", phi_q, z)[..., None] + 1e-6
        o = num / den
        return (lambda x, *d: x.permute(*d))(o, 0, 2, 1, 3).reshape(B, S, D) @ wo

    def _B_ref(x, w1, b1, w2, b2):
        B, S, D = x.shape
        h = _layernorm(x, axis=-1)
        u = torch.einsum("bsd,sf->bdf", h, w1) + b1
        u = _gelu(u)
        y = torch.einsum("bdf,fs->bds", u, w2) + b2
        return (lambda x, *d: x.permute(*d))(y, 0, 2, 1)

    x = A_x
    h = x + _A_ref(_layernorm(x, axis=-1), A_wqkv, A_w_phi, A_wo)
    h2 = h + _B_ref(_layernorm(h, axis=-1), B_w1, B_b1, B_w2, B_b2)
    return h2
```

```cpp
#include <hip/hip_runtime.h>
#include <hip/hip_bf16.h>
typedef __hip_bfloat16 bf16;
constexpr int S=2048, D=576, H=32, DH=18, F=4608;
__device__ __forceinline__ float b2f(bf16 v){return __bfloat162float(v);}
__device__ __forceinline__ bf16 f2b(float v){return __float2bfloat16(v);}

__global__ void k_ln(const bf16* x, float* xn){
  int s=blockIdx.x; __shared__ float red[2];
  __shared__ float buf[D];
  for(int i=threadIdx.x;i<D;i+=64) buf[i]=b2f(x[s*D+i]);
  __syncthreads();
  if(threadIdx.x==0){float m=0;for(int i=0;i<D;i++)m+=buf[i];m/=D;float v=0;for(int i=0;i<D;i++){float d=buf[i]-m;v+=d*d;}v/=D;red[0]=m;red[1]=rsqrtf(v+1e-5f);}
  __syncthreads();
  for(int i=threadIdx.x;i<D;i+=64) xn[s*D+i]=b2f(f2b((buf[i]-red[0])*red[1]));
}
__global__ void k_qkv(const float* xn,const bf16* w,float* qkv){
  int n=blockIdx.x*64+threadIdx.x; int s=blockIdx.y;
  float a=0; for(int k=0;k<D;k++) a+=xn[s*D+k]*b2f(w[k*3*D+n]);
  qkv[s*3*D+n]=b2f(f2b(a));
}
__global__ void k_phi(const float* qkv,const bf16* wphi,float* phi){
  int idx=blockIdx.x*64+threadIdx.x;
  int h=idx%H; int s=(idx/H)%S; int t=idx/(H*S);
  float p[DH];
  for(int f=0;f<DH;f++){float a=0;for(int d=0;d<DH;d++)a+=qkv[s*3*D+t*D+h*DH+d]*b2f(wphi[h*DH*DH+d*DH+f]);p[f]=a;}
  float mx=-1e30f,mn=1e30f;for(int f=0;f<DH;f++){mx=fmaxf(mx,p[f]);mn=fminf(mn,p[f]);}
  float s1=0,s2=0;for(int f=0;f<DH;f++){s1+=expf(p[f]-mx);s2+=expf(mn-p[f]);}
  float* o=phi+(size_t)idx*36;
  for(int f=0;f<DH;f++){o[f]=expf(p[f]-mx)/s1;o[DH+f]=expf(mn-p[f])/s2;}
}
__global__ void k_kv(const float* qkv,const float* phi,float* kv){
  int h=blockIdx.x; int i=threadIdx.x; if(i>=36*19) return; int f=i/19,d=i%19;
  float a=0; for(int s=0;s<S;s++){float pk=phi[((size_t)(S+s)*H+h)*36+f]; float v=d<18?qkv[s*3*D+2*D+h*DH+d]:1.f; a+=pk*v;}
  kv[h*36*19+i]=a;
}
__global__ void k_o(const float* phi,const float* kv,float* o){
  int idx=blockIdx.x*64+threadIdx.x;
  int c=idx%D,s=idx/D,h=c/DH,d=c%DH;
  float num=0,den=0;for(int f=0;f<36;f++){float pq=phi[((size_t)s*H+h)*36+f];num+=pq*kv[h*36*19+f*19+d];den+=pq*kv[h*36*19+f*19+18];}
  o[idx]=b2f(f2b(num/(den+1e-6f)));
}
__global__ void k_h(const float* o,const bf16* wo,const bf16* x,bf16* h){
  int n=blockIdx.x*64+threadIdx.x; int s=blockIdx.y;
  float a=0; for(int k=0;k<D;k++) a+=o[s*D+k]*b2f(wo[k*D+n]);
  h[s*D+n]=f2b(b2f(x[s*D+n])+a);
}
__global__ void k_ln2(const bf16* x, float* xn){
  int s=blockIdx.x; __shared__ float red[2];
  __shared__ float buf[D];
  for(int i=threadIdx.x;i<D;i+=64) buf[i]=b2f(x[s*D+i]);
  __syncthreads();
  if(threadIdx.x==0){float m=0;for(int i=0;i<D;i++)m+=buf[i];m/=D;float v=0;for(int i=0;i<D;i++){float d=buf[i]-m;v+=d*d;}v/=D;red[0]=m;
    float r=rsqrtf(v+1e-5f); float v2=v*r*r; red[1]=r*rsqrtf(v2+1e-5f);}
  __syncthreads();
  for(int i=threadIdx.x;i<D;i+=64) xn[s*D+i]=b2f(f2b((buf[i]-red[0])*red[1]));
}
__global__ void k_u(const float* hn,const bf16* w1,const bf16* b1,float* u){
  int f=blockIdx.x*64+threadIdx.x; int d=blockIdx.y;
  float a=0; for(int s=0;s<S;s++) a+=hn[s*D+d]*b2f(w1[(size_t)s*F+f]);
  a+=b2f(b1[f]);
  float g=0.5f*a*(1.f+tanhf(0.7978845608f*(a+0.044715f*a*a*a)));
  u[d*F+f]=b2f(f2b(g));
}
__global__ void k_y(const float* u,const bf16* w2,const bf16* b2,const bf16* h,bf16* out){
  int s=blockIdx.x*64+threadIdx.x; int d=blockIdx.y;
  float a=0; for(int f=0;f<F;f++) a+=u[d*F+f]*b2f(w2[(size_t)f*S+s]);
  a+=b2f(b2[s]);
  out[s*D+d]=f2b(b2f(h[s*D+d])+a);
}
extern "C" void kernel_launch(void* const* d_in, const int* in_sizes, int n_in, void* d_out, int out_size, void* d_ws, size_t ws_size, hipStream_t stream){
  const bf16* x=(const bf16*)d_in[0]; const bf16* wqkv=(const bf16*)d_in[1]; const bf16* wphi=(const bf16*)d_in[2]; const bf16* wo=(const bf16*)d_in[3];
  const bf16* w1=(const bf16*)d_in[5]; const bf16* b1=(const bf16*)d_in[6]; const bf16* w2=(const bf16*)d_in[7]; const bf16* b2=(const bf16*)d_in[8];
  float* ws=(float*)d_ws;
  float* xn=ws; float* qkv=xn+S*D; float* phi=qkv+S*3*D; float* kv=phi+(size_t)2*S*H*36; float* o=kv+H*36*19+64; float* u=o+S*D; bf16* h=(bf16*)(u+D*F);
  k_ln<<<S,64,0,stream>>>(x,xn);
  k_qkv<<<dim3(3*D/64,S),64,0,stream>>>(xn,wqkv,qkv);
  k_phi<<<2*S*H/64,64,0,stream>>>(qkv,wphi,phi);
  k_kv<<<H,704,0,stream>>>(qkv,phi,kv);
  k_o<<<S*D/64,64,0,stream>>>(phi,kv,o);
  k_h<<<dim3(D/64,S),64,0,stream>>>(o,wo,x,h);
  k_ln2<<<S,64,0,stream>>>(h,xn);
  k_u<<<dim3(F/64,D),64,0,stream>>>(xn,w1,b1,u);
  k_y<<<dim3(S/64,D),64,0,stream>>>(u,w2,b2,h,(bf16*)d_out);
}
```

```cpp
#include <hip/hip_runtime.h>
#include <hip/hip_bf16.h>

typedef unsigned u32;
typedef unsigned long long u64;
typedef u32 u32x2 __attribute__((ext_vector_type(2)));
typedef u32 u32x4 __attribute__((ext_vector_type(4)));
typedef float f32x2 __attribute__((ext_vector_type(2)));
typedef float f32x4 __attribute__((ext_vector_type(4)));
typedef short s16x4 __attribute__((ext_vector_type(4)));
typedef __bf16 bf16x2 __attribute__((ext_vector_type(2)));
typedef __bf16 bf16x8 __attribute__((ext_vector_type(8)));
#define LAS __attribute__((address_space(3)))
#define DEVINL __device__ __forceinline__

namespace {
constexpr int S = 2048, D = 576, F = 4608;
constexpr int NWG = 256, NT = 512;
constexpr int LDS_BYTES = 163840;
constexpr u32 SPIN_CAP = 1u << 16;

constexpr u32 W_EPOCH = 0;
constexpr u32 W_FLAGB = 4096;
constexpr u32 W_FLAGD = W_FLAGB + 256 * 128;
constexpr u32 W_FLAGE = W_FLAGD + 256 * 128;
constexpr u32 W_KVFIN = W_FLAGE + 256 * 128;
constexpr u32 W_STATS = W_KVFIN + 8 * 32 * 384;
constexpr u32 W_KVPART = W_STATS + 32 * 4 * 8 * 256;
constexpr u32 W_O = W_KVPART + 8 * 32 * 32 * 384;
constexpr u32 W_H = W_O + 128 * 18 * 1024;
constexpr u32 W_HN = W_H + S * D * 2;
constexpr u32 W_U = W_HN + 36 * 64 * 1024;
constexpr u32 W_YP = W_U + 36 * 144 * 1024;
constexpr u32 W_END = W_YP + 64 * 4 * 3 * 3 * 8 * 1024;
constexpr u32 W_DBG = W_END;

constexpr u32 SLOT = 27648;
constexpr u32 L_XN = 3 * SLOT;
constexpr u32 L_PHI = 4 * SLOT;
constexpr u32 L_V = L_PHI + 64 * 320;
constexpr u32 L_XT = L_PHI;
constexpr u32 L_KV = L_V + 64 * 144;
constexpr u32 L_MISC = L_XN + 73728;
constexpr u32 L_WPHI = L_MISC;
constexpr u32 L_ZERO = L_MISC + 3072;
constexpr u32 L_STAT = L_MISC + 3200;
constexpr u32 L_HT = 0;

struct Params {
  const char* x; const char* wqkv; const char* wphi; const char* wo;
  const char* w1; const char* b1; const char* w2; const char* b2;
  char* out; char* ws;
};

DEVINL f32x4 mfma32(u32x4 a, u32x4 b, f32x4 c) {
  return __builtin_amdgcn_mfma_f32_16x16x32_bf16(__builtin_bit_cast(bf16x8, a), __builtin_bit_cast(bf16x8, b), c, 0, 0, 0);
}
DEVINL f32x4 mfma16(u32x2 a, u32x2 b, f32x4 c) {
  return mfma32(u32x4{a.x, a.y, 0u, 0u}, u32x4{b.x, b.y, 0u, 0u}, c);
}
DEVINL u32 pack2(float lo, float hi) {
  f32x2 p = {lo, hi};
  return __builtin_bit_cast(u32, __builtin_convertvector(p, bf16x2));
}
DEVINL u32x2 pack4(f32x4 v) { return u32x2{pack2(v.x, v.y), pack2(v.z, v.w)}; }
DEVINL float bflo(u32 v) { return __builtin_bit_cast(float, v << 16); }
DEVINL float bfhi(u32 v) { return __builtin_bit_cast(float, v & 0xffff0000u); }
DEVINL float asf(u32 v) { return __builtin_bit_cast(float, v); }
DEVINL u32 asu(float v) { return __builtin_bit_cast(u32, v); }
DEVINL float dot2(u32 a, u32 b, float c) {
  return __builtin_amdgcn_fdot2_f32_bf16(__builtin_bit_cast(bf16x2, a), __builtin_bit_cast(bf16x2, b), c, false);
}
DEVINL u32x2 lds_tr(u32 addr) {
  return __builtin_bit_cast(u32x2, __builtin_amdgcn_ds_read_tr16_b64_v4i16((LAS s16x4*)addr));
}
DEVINL u32x4 lds_tr2(u32 a0, u32 a1) {
  u32x2 lo = lds_tr(a0), hi = lds_tr(a1);
  return u32x4{lo.x, lo.y, hi.x, hi.y};
}
DEVINL u32x4 lds_r128(u32 addr) { return *(LAS u32x4*)addr; }
DEVINL u32x2 lds_r64(u32 addr) { return *(LAS u32x2*)addr; }
DEVINL void lds_w128(u32 addr, u32x4 v) { *(LAS u32x4*)addr = v; }
DEVINL void lds_w64(u32 addr, u32x2 v) { *(LAS u32x2*)addr = v; }
DEVINL float shx(float v, int m) { return __shfl_xor(v, m, 64); }

DEVINL void dma(const char* base, u32 voff, u32 lds) {
  asm volatile("s_mov_b32 m0, %2\n\ts_nop 0\n\tglobal_load_lds_dwordx4 %0, %1" ::"v"(voff), "s"(base), "s"(lds) : "memory");
}
DEVINL void dma_sc1(const char* base, u32 voff, u32 lds) {
  asm volatile("s_mov_b32 m0, %2\n\ts_nop 0\n\tglobal_load_lds_dwordx4 %0, %1 sc1" ::"v"(voff), "s"(base), "s"(lds) : "memory");
}
template <int NL> DEVINL void dma_n(const char* base, u32 voff, u32 lds) {
  asm volatile("s_mov_b32 m0, %2\n\ts_mov_b32 exec_hi, %3\n\tglobal_load_lds_dwordx4 %0, %1\n\ts_mov_b32 exec_hi, -1" ::"v"(voff), "s"(base), "s"(lds), "n"((1u << (NL - 32)) - 1u) : "memory");
}
template <int NL> DEVINL void dma_n_sc1(const char* base, u32 voff, u32 lds) {
  asm volatile("s_mov_b32 m0, %2\n\ts_mov_b32 exec_hi, %3\n\tglobal_load_lds_dwordx4 %0, %1 sc1\n\ts_mov_b32 exec_hi, -1" ::"v"(voff), "s"(base), "s"(lds), "n"((1u << (NL - 32)) - 1u) : "memory");
}
DEVINL void st128_sc1(char* base, u32 voff, u32x4 v) {
  asm volatile("global_store_dwordx4 %0, %1, %2 sc1\n\ts_nop 1" ::"v"(voff), "v"(v), "s"(base) : "memory");
}
DEVINL void st64_sc1(char* base, u32 voff, u32x2 v) {
  asm volatile("global_store_dwordx2 %0, %1, %2 sc1" ::"v"(voff), "v"(v), "s"(base) : "memory");
}
DEVINL void st64(char* base, u32 voff, u32x2 v) {
  asm volatile("global_store_dwordx2 %0, %1, %2" ::"v"(voff), "v"(v), "s"(base) : "memory");
}
DEVINL void st32_sc1(char* base, u32 voff, u32 v) {
  asm volatile("global_store_dword %0, %1, %2 sc1" ::"v"(voff), "v"(v), "s"(base) : "memory");
}
DEVINL void ld128(u32x4& d, const char* base, u32 voff) {
  asm volatile("global_load_dwordx4 %0, %1, %2" : "=v"(d) : "v"(voff), "s"(base) : "memory");
}
DEVINL void ld128_sc1(u32x4& d, const char* base, u32 voff) {
  asm volatile("global_load_dwordx4 %0, %1, %2 sc1" : "=v"(d) : "v"(voff), "s"(base) : "memory");
}
DEVINL void ld64(u32x2& d, const char* base, u32 voff) {
  asm volatile("global_load_dwordx2 %0, %1, %2" : "=v"(d) : "v"(voff), "s"(base) : "memory");
}
DEVINL void ld64_sc1(u32x2& d, const char* base, u32 voff) {
  asm volatile("global_load_dwordx2 %0, %1, %2 sc1" : "=v"(d) : "v"(voff), "s"(base) : "memory");
}
DEVINL void ld16(u32& d, const char* base, u32 voff) {
  asm volatile("global_load_ushort %0, %1, %2" : "=v"(d) : "v"(voff), "s"(base) : "memory");
}
template <int N> DEVINL void waitv() { asm volatile("s_waitcnt vmcnt(%0)" ::"n"(N) : "memory"); }
template <typename T> DEVINL void pin(T& v) { asm volatile("" : "+v"(v)); }
template <int N> DEVINL void bar_v() { asm volatile("s_waitcnt vmcnt(%0) lgkmcnt(0)\n\ts_barrier" ::"n"(N) : "memory"); }
DEVINL void bar() { asm volatile("s_waitcnt lgkmcnt(0)\n\ts_barrier" ::: "memory"); }

DEVINL void poll_flags(const char* base, u32 voff, u32 tag) {
  for (u32 spins = 0; spins < SPIN_CAP; ++spins) {
    u32 v;
    asm volatile("global_load_dword %0, %1, %2 sc1\n\ts_waitcnt vmcnt(0)" : "=v"(v) : "v"(voff), "s"(base) : "memory");
    if (__all(v == tag)) break;
  }
}

#define STAMP(i)

DEVINL void feature_map(const u32x4 (&wa)[5], const u32x2 (&wb)[5], f32x4 t0, f32x4 t1, f32x4 t2, u32 (&ph)[10]) {
  u32x2 p0 = pack4(t0), p1 = pack4(t1), p2 = pack4(t2);
  u32x4 b01 = {p0.x, p0.y, p1.x, p1.y};
  float p[20];
#pragma unroll
  for (int t = 0; t < 5; ++t) {
    f32x4 a = {0.f, 0.f, 0.f, 0.f};
    a = mfma32(wa[t], b01, a);
    a = mfma16(wb[t], p2, a);
    p[4 * t] = a.x; p[4 * t + 1] = a.y; p[4 * t + 2] = a.z; p[4 * t + 3] = a.w;
  }
  float m = p[0];
#pragma unroll
  for (int i = 1; i < 18; ++i) m = fmaxf(m, p[i]);
  const float L2E = 1.44269504088896341f;
  float nm = -m * L2E, sum = 0.f;
#pragma unroll
  for (int i = 0; i < 18; ++i) { p[i] = __builtin_amdgcn_exp2f(__builtin_fmaf(p[i], L2E, nm)); sum += p[i]; }
  float inv = __builtin_amdgcn_rcpf(sum);
#pragma unroll
  for (int i = 0; i < 9; ++i) ph[i] = pack2(p[2 * i] * inv, p[2 * i + 1] * inv);
  ph[9] = 0u;
}

DEVINL float gelu_tanh(float x) {
  const float C = -2.f * 0.7978845608028654f * 1.44269504088896341f;
  float y = x * __builtin_fmaf(x * x, 0.044715f * C, C);
  float e = __builtin_amdgcn_exp2f(y);
  return x * __builtin_amdgcn_rcpf(1.f + e);
}

__global__ __launch_bounds__(NT, 2) void fused_kernel(Params P) {
  extern __shared__ __attribute__((aligned(16))) char smem[];
  STAMP(0);
  const u32 tid = threadIdx.x, lane = tid & 63;
  const u32 w = __builtin_amdgcn_readfirstlane(tid >> 6);
  const u32 bid = blockIdx.x, xcd = bid & 7, idx = bid >> 3;
  const u32 l15 = lane & 15, g = lane >> 4, q4 = l15 >> 2, p4 = lane & 3;
  char* const ws = P.ws;

  const u32 cg = (xcd & 1) * 4 + (idx & 3), rb = (xcd >> 1) * 8 + (idx >> 2);
  const u32 rot = ((idx >> 2) % 3);
  u32 cdummy;
  {
    u64 pc; u32 len;
    asm volatile("s_getpc_b64 %0\n.Lfk_code_begin:\n\ts_mov_b32 %1, .Lfk_code_end-.Lfk_code_begin" : "=s"(pc), "=s"(len));
    const u32 co = min(((idx * 8 + w) * 2 + (lane & 1)) * 128u, len);
    asm volatile("global_load_dword %0, %1, %2" : "=v"(cdummy) : "v"(co), "s"(pc) : "memory");
  }
  if (w < 3) dma_n<54>(P.wphi + cg * 2592 + w * 864, lane * 16, L_WPHI + w * 864);
  u32x4 xr[9];
  {
    const u32 xo = (rb * 64 + w * 8 + (lane >> 3)) * 1152 + (lane & 7) * 16;
#pragma unroll
    for (int i = 0; i < 9; ++i) ld128(xr[i], P.x + i * 128, xo);
  }
  const u32 r3 = lane / 18, c18 = lane % 18, seg = c18 / 9, ch9 = c18 % 9;
  u32 wrow[4];
#pragma unroll
  for (int i = 0; i < 4; ++i) {
    u32 r = 12 * w + 3 * i + r3;
    wrow[i] = (r & ~12u) | ((r & 4u) << 1) | ((r & 8u) >> 1);
  }
  u32 voff[4];
#pragma unroll
  for (int i = 0; i < 4; ++i) voff[i] = wrow[i] * 3456 + seg * 1152 + ch9 * 16;
  const char* wkv = P.wqkv + 1152 + cg * 144;
  auto issue_kv = [&](int i, u32 slot) {
    u32 st = 2 * rot + i; if (st >= 6) st -= 6;
    const char* b = wkv + st * (96 * 3456);
#pragma unroll
    for (int j = 0; j < 4; ++j) dma_n<54>(b, voff[j], slot * SLOT + (w * 4 + j) * 864);
  };
  auto issue_q = [&](int i, u32 slot) {
    u32 st = rot + i; if (st >= 3) st -= 3;
    const char* b = P.wqkv + cg * 144 + st * (192 * 3456);
#pragma unroll
    for (int j = 0; j < 4; ++j) dma_n<54>(b, voff[j] + seg * (96 * 3456 - 1152), slot * SLOT + (w * 4 + j) * 864);
  };
  issue_kv(0, 0); issue_kv(1, 1); issue_kv(2, 2);
  u32 tag;
  asm volatile("s_load_dword %0, %1, 0x0\n\ts_waitcnt lgkmcnt(0)" : "=s"(tag) : "s"(ws) : "memory");
  tag += 1;
  if (tid < 16) *(LAS u32*)(L_ZERO + tid * 4) = 0u;

  asm volatile("s_waitcnt vmcnt(12)" : "+v"(xr[0]), "+v"(xr[1]), "+v"(xr[2]), "+v"(xr[3]), "+v"(xr[4]), "+v"(xr[5]), "+v"(xr[6]), "+v"(xr[7]), "+v"(xr[8]), "+v"(cdummy)::"memory");
  STAMP(1);
  {
    float s = 0.f;
#pragma unroll
    for (int i = 0; i < 9; ++i)
#pragma unroll
      for (int j = 0; j < 4; ++j) s = dot2(xr[i][j], 0x3f803f80u, s);
    s += shx(s, 1); s += shx(s, 2); s += shx(s, 4);
    const float mean = s * (1.f / 576.f);
    float c[72], v = 0.f;
#pragma unroll
    for (int i = 0; i < 9; ++i)
#pragma unroll
      for (int j = 0; j < 4; ++j) {
        float a = bflo(xr[i][j]) - mean, b = bfhi(xr[i][j]) - mean;
        c[i * 8 + 2 * j] = a; c[i * 8 + 2 * j + 1] = b;
        v = __builtin_fmaf(a, a, v); v = __builtin_fmaf(b, b, v);
      }
    v += shx(v, 1); v += shx(v, 2); v += shx(v, 4);
    const float rs = __builtin_amdgcn_rsqf(v * (1.f / 576.f) + 1e-5f);
    const u32 row16 = 8 * (w & 1) + (lane >> 3);
    const u32 a0 = L_XN + ((w >> 1) * 18 + ((lane & 7) >> 2)) * 1024 + (lane & 3) * 256 + ((row16 ^ (lane & 7)) * 16);
#pragma unroll
    for (int i = 0; i < 9; ++i) {
      u32x4 o;
#pragma unroll
      for (int j = 0; j < 4; ++j) o[j] = pack2(c[i * 8 + 2 * j] * rs, c[i * 8 + 2 * j + 1] * rs);
      lds_w128(a0 + i * 2048, o);
    }
  }
  bar();
  const u32 mt = w >> 1, hp = w & 1;
  u32x4 xf[18];
  {
#pragma unroll
    for (int i = 0; i < 18; ++i) {
      u32 ks = 6 * rot + i; if (ks >= 18) ks -= 18;
      xf[i] = lds_r128(L_XN + (mt * 18 + ks) * 1024 + g * 256 + ((l15 ^ ((i & 1) * 4 + g)) * 16));
    }
  }
  u32x4 wa[5]; u32x2 wb[5];
  {
    const u32 hb = q4 >> 1, sg = (q4 & 1) ? 0x80008000u : 0u;
    u32 ad[12];
#pragma unroll
    for (int j = 0; j < 12; ++j) {
      u32 ic = j < 4 ? 4 * g + j : (j < 8 ? 12 + 4 * g + j : 24 + j);
      bool ok = (ic >= 18 ? 1u : 0u) == hb && (j < 8 || g == 0);
      ad[j] = ok ? L_WPHI + (((hp * 2 + hb) * 18 + (ic - 18 * hb)) * 18 + p4) * 2 : L_ZERO;
    }
#pragma unroll
    for (int t = 0; t < 5; ++t) {
      u32 e[6];
#pragma unroll
      for (int j = 0; j < 6; ++j) {
        u32 lo = *(LAS unsigned short*)(ad[2 * j] + 8 * t), hi = *(LAS unsigned short*)(ad[2 * j + 1] + 8 * t);
        e[j] = (lo | (hi << 16)) ^ sg;
      }
      wa[t] = u32x4{e[0], e[1], e[2], e[3]};
      wb[t] = u32x2{e[4], e[5]};
    }
  }
  bar_v<8>();
  STAMP(2);
  issue_kv(3, 3); issue_kv(4, 4);
  const u32 trrow = (16 * (g >> 1) + 4 * (g & 1) + q4) * 288;
  const u32 trA = trrow + hp * 72 + p4 * 8;
  const u32 trM = trrow + hp * 72 + 64 + (p4 & 1) * 144;
  f32x4 acc[5];
#pragma unroll
  for (int i = 0; i < 5; ++i) acc[i] = f32x4{0.f, 0.f, 0.f, 0.f};
  auto kv_stage = [&](int i, u32 slot) {
#pragma unroll
    for (int kk = 0; kk < 3; ++kk) {
      const u32 b = slot * SLOT + kk * (32 * 288);
      const u32x4 xb = xf[3 * i + kk];
      acc[0] = mfma32(lds_tr2(b + trA, b + trA + 2304), xb, acc[0]);
      acc[1] = mfma32(lds_tr2(b + trA + 32, b + trA + 32 + 2304), xb, acc[1]);
      acc[2] = mfma32(lds_tr2(b + trA + 144, b + trA + 144 + 2304), xb, acc[2]);
      acc[3] = mfma32(lds_tr2(b + trA + 176, b + trA + 176 + 2304), xb, acc[3]);
      acc[4] = mfma32(lds_tr2(b + trM, b + trM + 2304), xb, acc[4]);
    }
  };
  kv_stage(0, 0);
  bar_v<12>(); issue_kv(5, 0); kv_stage(1, 1);
  bar_v<12>(); issue_q(0, 1); kv_stage(2, 2);
  bar_v<12>(); issue_q(1, 2); kv_stage(3, 3);
  bar_v<12>(); issue_q(2, 3); kv_stage(4, 4);
  bar_v<12>(); kv_stage(5, 0);
  STAMP(3);
  {
    u32 ph[10];
    feature_map(wa, wb, acc[0], acc[1], acc[4], ph);
    const u32 row = mt * 16 + l15;
    const u32 pa = L_PHI + row * 320 + (hp * 4 + g) * 40;
#pragma unroll
    for (int i = 0; i < 5; ++i) lds_w64(pa + 8 * i, u32x2{ph[2 * i], ph[2 * i + 1]});
    const u32 va = L_V + row * 144 + hp * 72 + g * 8;
    lds_w64(va, pack4(acc[2]));
    lds_w64(va + 32, pack4(acc[3]));
    if (g == 1) lds_w64(va + 56, pack4(acc[4]));
  }
  bar();
  {
    const u32 hl = w >> 1, vt = w & 1;
    const u32 srow = 8 * g + q4;
    const u32 vcol = (hl >> 1) * 72 + (vt ? 32 : ((hl & 1) ? 40 : 0) + p4 * 8);
    f32x4 kv[3];
#pragma unroll
    for (int i = 0; i < 3; ++i) kv[i] = f32x4{0.f, 0.f, 0.f, 0.f};
#pragma unroll
    for (int ks = 0; ks < 2; ++ks) {
      const u32 vb = L_V + (32 * ks + srow) * 144 + vcol;
      u32x4 vf = lds_tr2(vb, vb + 4 * 144);
      if (vt && l15 >= 4) vf = u32x4{0x3f803f80u, 0x3f803f80u, 0x3f803f80u, 0x3f803f80u};
      const u32 pb = L_PHI + (32 * ks + srow) * 320 + hl * 80;
      kv[0] = mfma32(lds_tr2(pb + p4 * 8, pb + p4 * 8 + 1280), vf, kv[0]);
      kv[1] = mfma32(lds_tr2(pb + 32 + p4 * 8, pb + 32 + p4 * 8 + 1280), vf, kv[1]);
      kv[2] = mfma32(lds_tr2(pb + 64 + (p4 & 1) * 8, pb + 64 + (p4 & 1) * 8 + 1280), vf, kv[2]);
    }
    const u32 colp = l15 == 4 ? 2u : l15 - 2 * (hl & 1);
#pragma unroll
    for (int i = 0; i < 3; ++i) {
      const u32 sidx = hl * 192 + (vt ? 160 + colp * 10 + 4 * i + g : 64 * i + lane);
      const bool ok = vt ? ((l15 == 4 || colp < 2) && (i < 2 || g < 2 || l15 == 4)) : (i < 2 || g < 2);
      const u32 owner = sidx / 24, slot = sidx % 24;
      u32x2 pk = pack4(kv[i]);
      if (ok) st128_sc1(ws + W_KVPART, ((cg * 32 + owner) * 32 + rb) * 384 + slot * 16, u32x4{pk.x, tag, pk.y, tag});
    }
  }
  STAMP(4);
  auto issue_wo = [&](int part) {
#pragma unroll
    for (int j = 0; j < 4; ++j) {
      u32 kap = part * 96 + wrow[j] + seg * 288;
      u32 row = kap < 512 ? 9 * (kap >> 3) + (kap & 7) : 9 * (kap - 512) + 8;
      dma_n<54>(P.wo + cg * 144, row * 1152 + ch9 * 16, part * SLOT + (w * 4 + j) * 864);
    }
  };
  issue_wo(0);
  bar_v<7>();
#pragma unroll
  for (int j = 0; j < 2; ++j)
    dma_n<36>(P.x + cg * 144 + (rb * 64 + w * 8 + j * 4) * 1152, (lane / 9) * 1152 + (lane % 9) * 16, L_XT + (w * 2 + j) * 576);
#pragma unroll
  for (int i = 0; i < 3; ++i) acc[i] = f32x4{0.f, 0.f, 0.f, 0.f};
  {
    const u32 trQ = trrow + hp * 72 + 64;
#pragma unroll
    for (int i = 0; i < 18; ++i) {
      const u32 b = (1 + i / 6) * SLOT + (i % 3) * (32 * 288) + ((i % 6) / 3) * 144;
      acc[0] = mfma32(lds_tr2(b + trA, b + trA + 2304), xf[i], acc[0]);
      acc[1] = mfma32(lds_tr2(b + trA + 32, b + trA + 32 + 2304), xf[i], acc[1]);
      acc[2] = mfma32(lds_tr2(b + trQ, b + trQ + 2304), xf[i], acc[2]);
    }
  }
  bar();
  STAMP(5);
  if (w == 7) {
    const u32 ln = lane < 48 ? lane : lane - 48;
    const u32 off = (cg * 32 + rb) * (32 * 384) + ln * 16;
    f32x4 sum;
    for (u32 spins = 0; spins < SPIN_CAP; ++spins) {
      u32x4 v[16];
#pragma unroll
      for (int i = 0; i < 16; ++i) ld128_sc1(v[i], ws + W_KVPART + i * 768, off);
      waitv<0>();
      bool ok = true;
      sum = f32x4{0.f, 0.f, 0.f, 0.f};
#pragma unroll
      for (int i = 0; i < 16; ++i) {
        pin(v[i]);
        ok = ok && v[i].y == tag && v[i].w == tag;
        sum += f32x4{bflo(v[i].x), bfhi(v[i].x), bflo(v[i].z), bfhi(v[i].z)};
      }
      if (__all(ok)) break;
    }
    f32x4 oth;
#pragma unroll
    for (int j = 0; j < 4; ++j) oth[j] = __shfl(sum[j], lane < 24 ? lane + 24 : lane, 64);
    sum += oth;
    u32x2 pk = pack4(sum);
    if (lane < 24) st128_sc1(ws + W_KVFIN, (cg * 32 + rb) * 384 + lane * 16, u32x4{pk.x, tag, pk.y, tag});
  }
  issue_wo(1); issue_wo(2);
  u32 pq[10];
  feature_map(wa, wb, acc[0], acc[1], acc[2], pq);
  STAMP(6);
  {
    u32 la[2];
#pragma unroll
    for (int a = 0; a < 2; ++a) {
      const u32 r = (w & 1) * 96 + a * 64 + (a ? (lane & 31) : lane), hl = w >> 1;
      u32 cc, piece;
      if (r < 160) { cc = (r & 15) + 2 * (hl & 1); piece = (r >> 6) * 4 + ((r >> 4) & 3); }
      else { u32 rr = r - 160, cp = rr / 10; piece = rr % 10; cc = cp >= 2 ? 18 : ((hl & 1) ? cp : 16 + cp); if (cp == 3) piece = 10; }
      la[a] = piece < 10 ? L_KV + (hl * 19 + cc) * 96 + (piece / 5) * 48 + (piece % 5) * 8 : L_KV + 40;
    }
    const u32 o0 = cg * (32 * 384) + (w * 96 + lane) * 16, o1 = cg * (32 * 384) + (w * 96 + 64 + (lane & 31)) * 16;
    u32x4 v0, v1;
    for (u32 spins = 0; spins < SPIN_CAP; ++spins) {
      ld128_sc1(v0, ws + W_KVFIN, o0);
      ld128_sc1(v1, ws + W_KVFIN, o1);
      waitv<0>();
      pin(v0); pin(v1);
      if (__all(v0.y == tag && v0.w == tag && v1.y == tag && v1.w == tag)) break;
    }
    lds_w64(la[0], u32x2{v0.x, v0.z});
    lds_w64(la[1], u32x2{v1.x, v1.z});
  }
  bar();
  STAMP(7);
  {
    f32x4 nu[3];
    const bool same = (q4 >> 1) == (g >> 1);
#pragma unroll
    for (int t = 0; t < 3; ++t) {
      const u32 slot = 4 * t + p4;
      const u32 cc = slot < 9 ? 9 * (q4 & 1) + slot : 18;
      const u32 a = same ? L_KV + ((hp * 2 + (g >> 1)) * 19 + cc) * 96 + (g & 1) * 48 : L_ZERO;
      f32x4 c = {0.f, 0.f, 0.f, 0.f};
      c = mfma32(lds_r128(a), u32x4{pq[0], pq[1], pq[2], pq[3]}, c);
      c = mfma32(lds_r128(a + 16), u32x4{pq[4], pq[5], pq[6], pq[7]}, c);
      c = mfma16(lds_r64(a + 32), u32x2{pq[8], pq[9]}, c);
      nu[t] = c;
    }
    const float inv = __builtin_amdgcn_rcpf(nu[2].y + 1e-6f);
    u32x2 a = pack4(nu[0] * inv), b = pack4(nu[1] * inv);
    const u32 mtg = rb * 4 + mt;
    st128_sc1(ws + W_O, (mtg * 18 + cg * 2 + hp) * 1024 + lane * 16, u32x4{a.x, a.y, b.x, b.y});
    const float e0 = nu[2].x * inv;
    const float e1 = __shfl(e0, l15 + 16, 64), e2 = __shfl(e0, l15 + 32, 64), e3 = __shfl(e0, l15 + 48, 64);
    if (g == 0) st64_sc1(ws + W_O, (mtg * 18 + 16 + (cg >> 2)) * 1024 + (cg & 3) * 256 + l15 * 16 + hp * 8, u32x2{pack2(e0, e1), pack2(e2, e3)});
  }
  const u32 mp = w >> 2, nt = w & 3;
  const bool extra = (w == 0 || w == 5);
  const u32 col0 = nt * 16 + (cg & 1) * 8;
  const u32 colx = (cg & 1) ? ((p4 & 1) * 4) : 64 + (p4 & 1) * 4;
  f32x4 ha[2], hx[2];
  {
#pragma unroll
    for (int a = 0; a < 2; ++a) {
      const u32 rowb = L_XT + (32 * mp + 16 * a + 4 * g + q4) * 144;
      u32x2 t = lds_tr(rowb + (col0 + p4 * 4) * 2);
      ha[a] = f32x4{bflo(t.x), bfhi(t.x), bflo(t.y), bfhi(t.y)};
      u32x2 t2 = lds_tr(rowb + colx * 2);
      hx[a] = f32x4{bflo(t2.x), bfhi(t2.x), bflo(t2.y), bfhi(t2.y)};
    }
  }
  bar_v<0>();
  STAMP(8);
  if (w == 0) {
    if (lane == 0) st32_sc1(ws + W_FLAGB, (rb * 8 + cg) * 128, tag);
    poll_flags(ws + W_FLAGB, (rb * 8 + (lane & 7)) * 128, tag);
  }
  bar();
  STAMP(9);
#pragma unroll
  for (int j = 0; j < 9; ++j) {
    const u32 pc = j * 8 + w, ks = pc >> 2, m = pc & 3;
    dma_sc1(ws + W_O + ((rb * 4 + m) * 18 + ks) * 1024, lane * 16, L_XN + (m * 18 + ks) * 1024);
  }
  {
    const u32 ob = L_XN + mp * (2 * 18 * 1024) + lane * 16;
    const u32 wbm = trrow + (col0 + p4 * 4) * 2, wbx = trrow + colx * 2;
#pragma unroll
    for (int part = 0; part < 3; ++part) {
      if (part == 0) bar_v<6>(); else if (part == 1) bar_v<3>(); else bar_v<0>();
#pragma unroll
      for (int kk = 0; kk < 6; ++kk) {
        const int ks = part * 6 + kk;
        const u32 b = (ks % 9) * (32 * 288) + (ks / 9) * 144;
        const u32x4 o0 = lds_r128(ob + ks * 1024), o1 = lds_r128(ob + (18 + ks) * 1024);
        const u32x4 wf = lds_tr2(b + wbm, b + wbm + 2304);
        ha[0] = mfma32(o0, wf, ha[0]);
        ha[1] = mfma32(o1, wf, ha[1]);
        if (extra) {
          const u32x4 wx = lds_tr2(b + wbx, b + wbx + 2304);
          hx[0] = mfma32(o0, wx, hx[0]);
          hx[1] = mfma32(o1, wx, hx[1]);
        }
      }
    }
  }
  bar();
  STAMP(10);
  {
#pragma unroll
    for (int a = 0; a < 2; ++a) {
      lds_w64(L_HT + (col0 + l15) * 144 + (32 * mp + 16 * a + 4 * g) * 2, pack4(ha[a]));
      if (extra && l15 < 8) lds_w64(L_HT + (((cg & 1) ? 0 : 64) + l15) * 144 + (32 * mp + 16 * a + 4 * g) * 2, pack4(hx[a]));
    }
  }
  bar();
  if (w < 4) {
    float s1 = 0.f, s2 = 0.f;
    u32x2 hr[5];
#pragma unroll
    for (int n = 0; n < 5; ++n) {
      const u32 piece = n < 4 ? 4 * n + g : 16 + (g & 1);
      hr[n] = lds_tr(L_HT + (4 * piece + q4) * 144 + (16 * w + 4 * p4) * 2);
      if (n == 4 && g >= 2) hr[n] = u32x2{0u, 0u};
      s1 = dot2(hr[n].x, 0x3f803f80u, s1); s1 = dot2(hr[n].y, 0x3f803f80u, s1);
      s2 = dot2(hr[n].x, hr[n].x, s2); s2 = dot2(hr[n].y, hr[n].y, s2);
    }
    s1 += shx(s1, 16); s2 += shx(s2, 16);
    s1 += shx(s1, 32); s2 += shx(s2, 32);
    const u32 sb = (rb * 4 + w) * 2048;
    if (g == 0) st128_sc1(ws + W_STATS, sb + cg * 256 + l15 * 16, u32x4{__builtin_bit_cast(u32, s1), tag, __builtin_bit_cast(u32, s2), tag});
    const u32 ho = (rb * 64 + w * 16 + l15) * 1152 + cg * 144 + g * 8;
#pragma unroll
    for (int n = 0; n < 5; ++n)
      if (n < 4 || g < 2) st64_sc1(ws + W_H, ho + n * 32, hr[n]);
    u32x4 v0, v1;
    for (u32 spins = 0; spins < SPIN_CAP; ++spins) {
      ld128_sc1(v0, ws + W_STATS, sb + lane * 16);
      ld128_sc1(v1, ws + W_STATS, sb + 1024 + lane * 16);
      waitv<0>();
      pin(v0); pin(v1);
      if (__all(v0.y == tag && v0.w == tag && v1.y == tag && v1.w == tag)) break;
    }
    s1 = asf(v0.x) + asf(v1.x);
    s2 = asf(v0.z) + asf(v1.z);
    s1 += shx(s1, 16); s2 += shx(s2, 16);
    s1 += shx(s1, 32); s2 += shx(s2, 32);
    const float mean = s1 * (1.f / 576.f);
    const float var = fmaxf(s2 * (1.f / 576.f) - mean * mean, 0.f);
    const float r1 = __builtin_amdgcn_rsqf(var + 1e-5f);
    const float r2 = r1 * __builtin_amdgcn_rsqf(var * r1 * r1 + 1e-5f);
    if (g == 0) lds_w64(L_STAT + (w * 16 + l15) * 8, u32x2{__builtin_bit_cast(u32, r2), __builtin_bit_cast(u32, -mean * r2)});
  }
  bar();
  STAMP(11);
  {
    u32x4 pm, px;
#pragma unroll
    for (int a = 0; a < 2; ++a) {
      const f32x4 s0 = __builtin_bit_cast(f32x4, lds_r128(L_STAT + (32 * mp + 16 * a + 4 * g) * 8));
      const f32x4 s1 = __builtin_bit_cast(f32x4, lds_r128(L_STAT + (32 * mp + 16 * a + 4 * g) * 8 + 16));
      pm[2 * a] = pack2(__builtin_fmaf(ha[a].x, s0.x, s0.y), __builtin_fmaf(ha[a].y, s0.z, s0.w));
      pm[2 * a + 1] = pack2(__builtin_fmaf(ha[a].z, s1.x, s1.y), __builtin_fmaf(ha[a].w, s1.z, s1.w));
      px[2 * a] = pack2(__builtin_fmaf(hx[a].x, s0.x, s0.y), __builtin_fmaf(hx[a].y, s0.z, s0.w));
      px[2 * a + 1] = pack2(__builtin_fmaf(hx[a].z, s1.x, s1.y), __builtin_fmaf(hx[a].w, s1.z, s1.w));
    }
    const u32 dt = 9 * (cg >> 1) + (cg & 1) * 5 + nt, ksg = rb * 2 + mp;
    st128_sc1(ws + W_HN, (dt * 64 + ksg) * 1024 + lane * 16, pm);
    if (extra && l15 < 8) st128_sc1(ws + W_HN, ((9 * (cg >> 1) + 4) * 64 + ksg) * 1024 + g * 256 + ((cg & 1) * 8 + l15) * 16, px);
  }
  bar_v<0>();
  if (tid == 0) st32_sc1(ws + W_FLAGD, (cg * 32 + rb) * 128, tag);
  STAMP(12);

  const u32 fp = w & 3, kh = w >> 2;
  const u32 wrow4 = w * 4 + g;
  const u32 wcol = ((((l15 >> 1) ^ (wrow4 & 7)) << 1) | (l15 & 1)) * 16;
  const u32 trw0 = (4 * g + q4) * 256 + (((2 * fp) ^ (4 * (g & 1) + q4)) * 32) + p4 * 8, trw1 = trw0 ^ 32;
  const u32 Mq = idx & 3, Nq = (xcd & 1) * 8 + (idx >> 2), kq = xcd >> 1;
  const char* w2b = P.w2 + (size_t)kq * 1152 * 4096 + Nq * 256;
  const u32 w2off = wrow4 * 4096 + wcol;
  auto issue_w2 = [&](int ps, u32 slot) {
    dma(w2b + ps * (32 * 4096), w2off, slot * 16384 + w * 1024);
    dma(w2b + (18 + ps) * (32 * 4096), w2off, slot * 16384 + 8192 + w * 1024);
  };
  if (idx < 27) {
    const u32 tile = xcd * 27 + idx, ni = tile / 6, mi = tile % 6;
    constexpr u32 ST = 28672;
    const char* w1b = P.w1 + ni * 256;
    const u32 w1off = wrow4 * 9216 + wcol;
    const char* hb = ws + W_HN + mi * (6 * 64 * 1024);
    u32 aoff[2];
#pragma unroll
    for (int j = 0; j < 2; ++j) {
      const u32 byte = (w * 2 + j) * 768 + lane * 16, fr = byte >> 10;
      aoff[j] = ((fr % 6) * 64 + (fr / 6) * 32) * 1024 + (byte & 1023);
    }
    auto issue_w = [&](int st) {
      const u32 sl = (st % 5) * ST;
      dma(w1b + (size_t)st * (32 * 9216), w1off, sl + w * 1024);
      dma(w1b + (size_t)(32 + st) * (32 * 9216), w1off, sl + 8192 + w * 1024);
    };
    auto issue_a = [&](int st) {
      const u32 sl = (st % 5) * ST + 16384;
      dma_n_sc1<48>(hb + st * 1024, aoff[0], sl + (w * 2) * 768);
      dma_n_sc1<48>(hb + st * 1024, aoff[1], sl + (w * 2 + 1) * 768);
    };
    u32x2 bias[2];
    ld64(bias[0], P.b1 + ni * 256, fp * 64 + g * 8);
    ld64(bias[1], P.b1 + ni * 256, fp * 64 + 32 + g * 8);
#pragma unroll
    for (int st = 0; st < 5; ++st) issue_w(st);
    if (w == 0) {
      const u32 cj = (mi * 96) / 72 + (lane >> 5);
      poll_flags(ws + W_FLAGD, (cj * 32 + (lane & 31)) * 128, tag);
    }
    bar();
    STAMP(13);
#pragma unroll
    for (int st = 0; st < 5; ++st) issue_a(st);
    f32x4 c[6][2];
#pragma unroll
    for (int i = 0; i < 6; ++i) { c[i][0] = f32x4{0.f, 0.f, 0.f, 0.f}; c[i][1] = c[i][0]; }
    u32x4 fa[2][6], fw[2][2];
    const u32 ard = 16384 + kh * 6144 + lane * 16, wrd = kh * 8192;
    auto read_frags = [&](int st) {
      const u32 sl = (st % 5) * ST;
      fw[st & 1][0] = lds_tr2(sl + wrd + trw0, sl + wrd + trw0 + 4096);
      fw[st & 1][1] = lds_tr2(sl + wrd + trw1, sl + wrd + trw1 + 4096);
#pragma unroll
      for (int i = 0; i < 6; ++i) fa[st & 1][i] = lds_r128(sl + ard + i * 1024);
    };
    bar_v<8>();
    read_frags(0);
#pragma unroll
    for (int st = 0; st < 32; ++st) {
      if (st < 31) {
        if (st == 0) bar_v<6>(); else if (st == 1) bar_v<8>(); else if (st == 2) bar_v<10>();
        else if (st <= 27) bar_v<12>(); else if (st == 28) bar_v<8>(); else if (st == 29) bar_v<4>(); else bar_v<0>();
        if (st + 5 < 32) { issue_w(st + 5); issue_a(st + 5); }
        read_frags(st + 1);
      }
#pragma unroll
      for (int i = 0; i < 6; ++i) {
        c[i][0] = mfma32(fw[st & 1][0], fa[st & 1][i], c[i][0]);
        c[i][1] = mfma32(fw[st & 1][1], fa[st & 1][i], c[i][1]);
      }
      __builtin_amdgcn_sched_barrier(0);
    }
    bar();
    STAMP(14);
#pragma unroll
    for (int ps = 0; ps < 7; ++ps) issue_w2(ps, ps);
    const u32 ex = 7 * 16384;
#pragma unroll
    for (int i = 0; i < 3; ++i)
#pragma unroll
      for (int j = 0; j < 2; ++j) {
        const f32x4 snd = kh ? c[i][j] : c[3 + i][j];
        lds_w128(ex + (w * 6 + i * 2 + j) * 1024 + lane * 16, __builtin_bit_cast(u32x4, snd));
      }
    bar();
    pin(bias[0]); pin(bias[1]);
    const f32x4 b0 = {bflo(bias[0].x), bfhi(bias[0].x), bflo(bias[0].y), bfhi(bias[0].y)};
    const f32x4 b1v = {bflo(bias[1].x), bfhi(bias[1].x), bflo(bias[1].y), bfhi(bias[1].y)};
#pragma unroll
    for (int i = 0; i < 3; ++i) {
      f32x4 v0 = (kh ? c[3 + i][0] : c[i][0]) + __builtin_bit_cast(f32x4, lds_r128(ex + ((w ^ 4) * 6 + i * 2) * 1024 + lane * 16)) + b0;
      f32x4 v1 = (kh ? c[3 + i][1] : c[i][1]) + __builtin_bit_cast(f32x4, lds_r128(ex + ((w ^ 4) * 6 + i * 2 + 1) * 1024 + lane * 16)) + b1v;
      u32x4 o = {pack2(gelu_tanh(v0.x), gelu_tanh(v0.y)), pack2(gelu_tanh(v0.z), gelu_tanh(v0.w)),
                 pack2(gelu_tanh(v1.x), gelu_tanh(v1.y)), pack2(gelu_tanh(v1.z), gelu_tanh(v1.w))};
      st128_sc1(ws + W_U, ((mi * 6 + kh * 3 + i) * 144 + ni * 4 + fp) * 1024 + lane * 16, o);
    }
    bar_v<0>();
    if (tid == 0) st32_sc1(ws + W_FLAGE, tile * 128, tag);
  } else {
#pragma unroll
    for (int ps = 0; ps < 7; ++ps) issue_w2(ps, ps);
    bar_v<0>();
  }
  STAMP(15);

  {
    u32x4 wf[18][2];
    const u32 wrd = kh * 8192;
    auto grab = [&](int ps, u32 slot) {
      const u32 sl = slot * 16384 + wrd;
      wf[ps][0] = lds_tr2(sl + trw0, sl + trw0 + 4096);
      wf[ps][1] = lds_tr2(sl + trw1, sl + trw1 + 4096);
    };
#pragma unroll
    for (int ps = 7; ps < 10; ++ps) issue_w2(ps, ps);
#pragma unroll
    for (int ps = 0; ps < 7; ++ps) grab(ps, ps);
    bar();
#pragma unroll
    for (int ps = 10; ps < 17; ++ps) issue_w2(ps, ps - 10);
    bar_v<14>();
#pragma unroll
    for (int ps = 7; ps < 10; ++ps) grab(ps, ps);
    bar();
    issue_w2(17, 7);
    if (w == 0) {
      const u32 ln = lane < 18 ? lane : 0;
      const u32 t = (kq * 9 + (ln >> 1)) * 6 + (Mq * 144) / 96 + (ln & 1);
      poll_flags(ws + W_FLAGE, t * 128, tag);
    }
    bar_v<0>();
#pragma unroll
    for (int ps = 10; ps < 18; ++ps) grab(ps, ps - 10);
    bar();
    STAMP(16);
    auto dtl = [&](int i) -> u32 { return i == 0 ? 8u : ((2 * (kq + 1) + (i - 1)) & 7u); };
    const char* ub = ws + W_U + (Mq * 9 * 144 + kq * 36) * 1024;
    auto issue_u = [&](int i) {
      const char* b = ub + dtl(i) * (144 * 1024);
#pragma unroll
      for (int j = 0; j < 6; ++j) dma_n_sc1<48>(b + (j * 8 + w) * 768, lane * 16, (i & 3) * 36864 + (j * 8 + w) * 768);
    };
    const u32 st8 = 2 * fp + kh;
    const u32 srow = Nq * 128 + st8 * 16 + l15;
    const u32 oo = srow * 1152 + Mq * 288 + g * 8;
    u32x2 hres[3]; u32 bb;
    ld64_sc1(hres[0], ws + W_H, oo + kq * 64);
    ld64_sc1(hres[1], ws + W_H, oo + kq * 64 + 32);
    ld64_sc1(hres[2], ws + W_H, oo + 256);
    ld16(bb, P.b2, srow * 2);
    issue_u(0); issue_u(1); issue_u(2);
    const u32 grp = Mq * 16 + Nq;
    const u32 exb = 147456;
    f32x4 keep = {0.f, 0.f, 0.f, 0.f}, own[3];
    auto finish = [&](int i) {
      const f32x4 v = keep + __builtin_bit_cast(f32x4, lds_r128(exb + (i & 1) * 8192 + (w ^ 4) * 1024 + lane * 16));
      if (i >= 7) { own[i - 7] = v; return; }
      if (i == 0) own[2] = v;
      const u32 owner = i == 0 ? fp : ((kq + 1 + ((i - 1) >> 1)) & 3u);
      const u32 slot = i == 0 ? 2 : ((i - 1) & 1);
      const u32 src = (kq - owner - 1) & 3u;
      const u32x2 pk = pack4(v);
      const u32 off = src < 3 ? ((((grp * 4 + owner) * 3 + src) * 3 + slot) * 8 + st8) * 1024 : ((((grp * 4 + owner) * 3) * 3 + 2) * 8 + (st8 ^ 2)) * 1024;
      st128_sc1(ws + W_YP, off + lane * 16, u32x4{pk.x, tag, pk.y, tag});
    };
#pragma unroll
    for (int i = 0; i < 9; ++i) {
      if (i == 0) bar_v<12>(); else if (i == 1) bar_v<12>(); else if (i <= 6) bar_v<13>(); else if (i == 7) bar_v<7>(); else bar_v<1>();
      if (i + 3 < 9) issue_u(i + 3);
      if (i > 0) finish(i - 1);
      f32x4 c0 = {0.f, 0.f, 0.f, 0.f}, c1 = c0;
      const u32 ar = (i & 3) * 36864 + kh * 18432 + lane * 16;
#pragma unroll
      for (int ks = 0; ks < 18; ++ks) {
        const u32x4 a = lds_r128(ar + ks * 1024);
        c0 = mfma32(a, wf[ks][0], c0);
        c1 = mfma32(a, wf[ks][1], c1);
      }
      keep = kh ? c1 : c0;
      lds_w128(exb + (i & 1) * 8192 + w * 1024 + lane * 16, __builtin_bit_cast(u32x4, kh ? c0 : c1));
    }
    bar();
    finish(8);
    STAMP(17);
    const bool has8 = fp == kq;
    u32x4 pv[9];
    const u32 yo = ((grp * 4 + kq) * 9 * 8 + st8) * 1024 + lane * 16;
    for (u32 spins = 0; spins < SPIN_CAP; ++spins) {
#pragma unroll
      for (int s = 0; s < 3; ++s)
#pragma unroll
        for (int t = 0; t < 3; ++t) ld128_sc1(pv[s * 3 + t], ws + W_YP, yo + (s * 3 + ((t < 2 || has8) ? t : 0)) * 8192);
      waitv<0>();
      bool ok = true;
#pragma unroll
      for (int j = 0; j < 9; ++j) { pin(pv[j]); ok = ok && pv[j].y == tag && pv[j].w == tag; }
      if (__all(ok)) break;
    }
    pin(hres[0]); pin(hres[1]); pin(hres[2]); pin(bb);
    const float bias = bflo(bb);
#pragma unroll
    for (int t = 0; t < 3; ++t) {
      f32x4 v = own[t];
#pragma unroll
      for (int s = 0; s < 3; ++s) v += f32x4{bflo(pv[s * 3 + t].x), bfhi(pv[s * 3 + t].x), bflo(pv[s * 3 + t].z), bfhi(pv[s * 3 + t].z)};
      v += f32x4{bflo(hres[t].x), bfhi(hres[t].x), bflo(hres[t].y), bfhi(hres[t].y)} + bias;
      if (t < 2) st64(P.out, oo + kq * 64 + t * 32, pack4(v));
      else if (has8) st64(P.out, oo + 256, pack4(v));
    }
  }
  asm volatile(".Lfk_code_end:");
  if (bid == 0 && tid == 0) st32_sc1(ws + W_EPOCH, 0u, tag);
}
}

extern "C" void kernel_launch(void* const* d_in, const int* in_sizes, int n_in, void* d_out, int out_size, void* d_ws,
                              size_t ws_size, hipStream_t stream) {
  Params p;
  p.x = (const char*)d_in[0]; p.wqkv = (const char*)d_in[1]; p.wphi = (const char*)d_in[2]; p.wo = (const char*)d_in[3];
  p.w1 = (const char*)d_in[5]; p.b1 = (const char*)d_in[6]; p.w2 = (const char*)d_in[7]; p.b2 = (const char*)d_in[8];
  p.out = (char*)d_out; p.ws = (char*)d_ws;
  (void)hipFuncSetAttribute((const void*)fused_kernel, hipFuncAttributeMaxDynamicSharedMemorySize, LDS_BYTES);
  fused_kernel<<<NWG, NT, LDS_BYTES, stream>>>(p);
}
```

```cpp
#include <hip/hip_runtime.h>
#include <hip/hip_bf16.h>

typedef unsigned u32;
typedef unsigned long long u64;
typedef u32 u32x2 __attribute__((ext_vector_type(2)));
typedef u32 u32x4 __attribute__((ext_vector_type(4)));
typedef float f32x2 __attribute__((ext_vector_type(2)));
typedef float f32x4 __attribute__((ext_vector_type(4)));
typedef float f32x16 __attribute__((ext_vector_type(16)));
typedef short s16x4 __attribute__((ext_vector_type(4)));
typedef __bf16 bf16x2 __attribute__((ext_vector_type(2)));
typedef __bf16 bf16x8 __attribute__((ext_vector_type(8)));
#define LAS __attribute__((address_space(3)))
#define DEVINL __device__ __forceinline__

namespace {
constexpr int S = 2048, D = 576, F = 4608;
constexpr int NWG = 256, NT = 512;
constexpr int LDS_BYTES = 163840;
constexpr u32 SPIN_CAP = 1u << 16;

constexpr u32 W_EPOCH = 0;
constexpr u32 W_FLAGB = 4096;
constexpr u32 W_FLAGD = W_FLAGB + 1024 * 128;
constexpr u32 W_FLAGE = W_FLAGD + 256 * 128;
constexpr u32 W_FLAGH = W_FLAGE + 18 * 36 * 128;
constexpr u32 W_KVFIN = W_FLAGH + 256 * 128;
constexpr u32 W_STATS = W_KVFIN + 8 * 96 * 128;
constexpr u32 W_KVPART = W_STATS + 32 * 4 * 8 * 256;
constexpr u32 W_O = W_KVPART + 8 * 96 * 32 * 128;
constexpr u32 W_H = W_O + 128 * 18 * 1024;
constexpr u32 W_HN = W_H + S * D * 2;
constexpr u32 W_U = W_HN + 18 * 128 * 1024;
constexpr u32 W_UP = W_U + 18 * 288 * 1024;
constexpr u32 W_YP = W_UP + 18 * 288 * 2048;
constexpr u32 W_YS = W_YP + 64 * 4 * 3 * 8 * 2048;
constexpr u32 W_END = W_YS + 32 * 8 * 8 * 2048;
constexpr u32 W_DBG = W_END;

constexpr u32 SLOT = 27648;
constexpr u32 L_XN = 3 * SLOT;
constexpr u32 L_PHI = 4 * SLOT;
constexpr u32 L_V = L_PHI + 64 * 328;
constexpr u32 L_KV = 147456;
constexpr u32 L_MISC = L_XN + 73728;
constexpr u32 L_WPHI = L_MISC;
constexpr u32 L_ZERO = L_MISC + 5760;
constexpr u32 L_CNT = L_MISC + 5824;
constexpr u32 L_HI = 5 * SLOT;
constexpr u32 L_HN = L_HI + 10240;

struct Params {
  const char* x; const char* wqkv; const char* wphi; const char* wo;
  const char* w1; const char* b1; const char* w2; const char* b2;
  char* out; char* ws;
};

DEVINL f32x4 mfma32(u32x4 a, u32x4 b, f32x4 c) {
  return __builtin_amdgcn_mfma_f32_16x16x32_bf16(__builtin_bit_cast(bf16x8, a), __builtin_bit_cast(bf16x8, b), c, 0, 0, 0);
}
DEVINL f32x16 mfmaB(u32x4 a, u32x4 b, f32x16 c) {
  return __builtin_amdgcn_mfma_f32_32x32x16_bf16(__builtin_bit_cast(bf16x8, a), __builtin_bit_cast(bf16x8, b), c, 0, 0, 0);
}
template <int Q> DEVINL f32x4 quad(const f32x16& v) { return f32x4{v[4 * Q], v[4 * Q + 1], v[4 * Q + 2], v[4 * Q + 3]}; }
DEVINL f32x4 mfma16(u32x2 a, u32x2 b, f32x4 c) {
  return mfma32(u32x4{a.x, a.y, 0u, 0u}, u32x4{b.x, b.y, 0u, 0u}, c);
}
DEVINL u32 pack2(float lo, float hi) {
  f32x2 p = {lo, hi};
  return __builtin_bit_cast(u32, __builtin_convertvector(p, bf16x2));
}
DEVINL u32x2 pack4(f32x4 v) { return u32x2{pack2(v.x, v.y), pack2(v.z, v.w)}; }
DEVINL float bflo(u32 v) { return __builtin_bit_cast(float, v << 16); }
DEVINL float bfhi(u32 v) { return __builtin_bit_cast(float, v & 0xffff0000u); }
DEVINL float asf(u32 v) { return __builtin_bit_cast(float, v); }
DEVINL u32 asu(float v) { return __builtin_bit_cast(u32, v); }
DEVINL f32x4 unpk(u32 a, u32 b) { return f32x4{bflo(a), bfhi(a), bflo(b), bfhi(b)}; }
DEVINL float dot2(u32 a, u32 b, float c) {
  return __builtin_amdgcn_fdot2_f32_bf16(__builtin_bit_cast(bf16x2, a), __builtin_bit_cast(bf16x2, b), c, false);
}
DEVINL u32x2 lds_tr(u32 addr) {
  return __builtin_bit_cast(u32x2, __builtin_amdgcn_ds_read_tr16_b64_v4i16((LAS s16x4*)addr));
}
DEVINL u32x4 lds_tr2(u32 a0, u32 a1) {
  u32x2 lo = lds_tr(a0), hi = lds_tr(a1);
  return u32x4{lo.x, lo.y, hi.x, hi.y};
}
DEVINL u32x4 lds_r128(u32 addr) { return *(LAS u32x4*)addr; }
DEVINL u32x2 lds_r64(u32 addr) { return *(LAS u32x2*)addr; }
DEVINL void lds_w128(u32 addr, u32x4 v) { *(LAS u32x4*)addr = v; }
DEVINL void lds_w64(u32 addr, u32x2 v) { *(LAS u32x2*)addr = v; }
template <int CTRL> DEVINL float dpp(float v) { return asf(__builtin_amdgcn_update_dpp(0u, asu(v), CTRL, 0xf, 0xf, true)); }
DEVINL float sum8(float v) { v += dpp<0xB1>(v); v += dpp<0x4E>(v); return v + dpp<0x141>(v); }
DEVINL float sum_g(float v) {
  auto a = __builtin_amdgcn_permlane16_swap(asu(v), asu(v), false, false);
  v = asf(a[0]) + asf(a[1]);
  auto b = __builtin_amdgcn_permlane32_swap(asu(v), asu(v), false, false);
  return asf(b[0]) + asf(b[1]);
}
#define SB() __builtin_amdgcn_sched_barrier(0)
#define PRIO_K1 2
#define PRIO_K2 2
#define PRIO(v)
DEVINL u32 fresh_lane() { u32 l; asm volatile("v_mbcnt_lo_u32_b32 %0, -1, 0\n\tv_mbcnt_hi_u32_b32 %0, -1, %0" : "=v"(l)); __builtin_assume(l < 64); return l; }
#define LANEVARS const u32 lane = fresh_lane(), l15 = lane & 15, g = lane >> 4, q4 = l15 >> 2, p4 = lane & 3; \
  const u32 trrow = (16 * (g >> 1) + 4 * (g & 1) + q4) * 288; (void)l15; (void)g; (void)q4; (void)p4; (void)trrow
struct LAddr {
  u32 b[3];
  DEVINL explicit LAddr(u32 a) { b[0] = a; b[1] = a + 65536u; b[2] = a + 131072u; asm volatile("" : "+v"(b[0]), "+v"(b[1]), "+v"(b[2])); }
  DEVINL u32 operator()(u32 c) const { return b[c >> 16] + (c & 0xffffu); }
};

#define POL_W ""
#define POL_T ""
#define POL_S ""
#define PFMASK 0xff
#define B1N 4
#define B3WAIT 0
#define POL_1 " nt"
#define ROT1 1
#define POL_X ""
template <int NL, bool SC1> DEVINL void dma(const char* base, u32 voff, u32 lbase, u32 loff) {
  if constexpr (NL == 64) {
    if constexpr (SC1) asm volatile("s_add_u32 m0, %2, %3\n\ts_nop 0\n\tglobal_load_lds_dwordx4 %0, %1 sc1" ::"v"(voff), "s"(base), "s"(lbase), "n"(loff) : "memory", "scc");
    else asm volatile("s_add_u32 m0, %2, %3\n\ts_nop 0\n\tglobal_load_lds_dwordx4 %0, %1" POL_W ::"v"(voff), "s"(base), "s"(lbase), "n"(loff) : "memory", "scc");
  } else {
    if constexpr (SC1) asm volatile("s_add_u32 m0, %2, %3\n\ts_mov_b32 exec_hi, %4\n\tglobal_load_lds_dwordx4 %0, %1 sc1\n\ts_mov_b32 exec_hi, -1" ::"v"(voff), "s"(base), "s"(lbase), "n"(loff), "n"((1u << (NL - 32)) - 1u) : "memory", "scc");
    else asm volatile("s_add_u32 m0, %2, %3\n\ts_mov_b32 exec_hi, %4\n\tglobal_load_lds_dwordx4 %0, %1" POL_X "\n\ts_mov_b32 exec_hi, -1" ::"v"(voff), "s"(base), "s"(lbase), "n"(loff), "n"((1u << (NL - 32)) - 1u) : "memory", "scc");
  }
}
DEVINL void dma_w1(const char* base, u32 voff, u32 lds) {
  asm volatile("s_mov_b32 m0, %2\n\ts_nop 0\n\tglobal_load_lds_dwordx4 %0, %1" POL_1 ::"v"(voff), "s"(base), "s"(lds) : "memory");
}
DEVINL void st128_sc1(char* base, u32 voff, u32x4 v) {
  asm volatile("global_store_dwordx4 %0, %1, %2 sc1\n\ts_nop 1" ::"v"(voff), "v"(v), "s"(base) : "memory");
}
DEVINL void st128_sc1_m(char* base, u32 voff, u32x4 v, u64 mask) {
  asm volatile("s_mov_b64 exec, %3\n\tglobal_store_dwordx4 %0, %1, %2 sc1\n\ts_mov_b64 exec, -1\n\ts_nop 0" ::"v"(voff), "v"(v), "s"(base), "s"(mask) : "memory");
}
DEVINL void st64_sc1(char* base, u32 voff, u32x2 v) {
  asm volatile("global_store_dwordx2 %0, %1, %2 sc1" ::"v"(voff), "v"(v), "s"(base) : "memory");
}
DEVINL void st64(char* base, u32 voff, u32x2 v) {
  asm volatile("global_store_dwordx2 %0, %1, %2" ::"v"(voff), "v"(v), "s"(base) : "memory");
}
DEVINL void st32_sc1(char* base, u32 voff, u32 v) {
  asm volatile("global_store_dword %0, %1, %2 sc1" ::"v"(voff), "v"(v), "s"(base) : "memory");
}
DEVINL void st_flag(char* addr, u32 tag) {
  u32 z, t;
  asm volatile("v_mov_b32 %0, 0\n\tv_mov_b32 %1, %3\n\ts_mov_b64 exec, 1\n\tglobal_store_dword %0, %1, %2 sc1\n\ts_mov_b64 exec, -1" : "=&v"(z), "=&v"(t) : "s"(addr), "s"(tag) : "memory");
}
DEVINL void ld128(u32x4& d, const char* base, u32 voff, int imm) {
  asm volatile("global_load_dwordx4 %0, %1, %2 offset:%3" POL_X : "=v"(d) : "v"(voff), "s"(base), "n"(imm) : "memory");
}
DEVINL void ld128_sc1(u32x4& d, const char* base, u32 voff, int imm = 0) {
  asm volatile("global_load_dwordx4 %0, %1, %2 offset:%3 sc1" : "=v"(d) : "v"(voff), "s"(base), "n"(imm) : "memory");
}
DEVINL void ld64(u32x2& d, const char* base, u32 voff, int imm = 0) {
  asm volatile("global_load_dwordx2 %0, %1, %2 offset:%3" : "=v"(d) : "v"(voff), "s"(base), "n"(imm) : "memory");
}
DEVINL void ld32(u32& d, const char* base, u32 voff) {
  asm volatile("global_load_dword %0, %1, %2" POL_T : "=v"(d) : "v"(voff), "s"(base) : "memory");
}
DEVINL void touch(u32& d, const char* base, u32 voff) {
  asm volatile("global_load_dword %0, %1, %2" POL_T : "+v"(d) : "v"(voff), "s"(base) : "memory");
}
DEVINL void ld64_sc1(u32x2& d, const char* base, u32 voff) {
  asm volatile("global_load_dwordx2 %0, %1, %2 sc1" : "=v"(d) : "v"(voff), "s"(base) : "memory");
}
DEVINL void ld16(u32& d, const char* base, u32 voff) {
  asm volatile("global_load_ushort %0, %1, %2" : "=v"(d) : "v"(voff), "s"(base) : "memory");
}
template <int N> DEVINL void waitv() { asm volatile("s_waitcnt vmcnt(%0)" ::"n"(N) : "memory"); }
template <typename T> DEVINL void pin(T& v) { asm volatile("" : "+v"(v)); }
template <int N> DEVINL void bar_v() { asm volatile("s_waitcnt vmcnt(%0) lgkmcnt(0)\n\ts_barrier" ::"n"(N) : "memory"); }
template <int N> DEVINL void bar_vo() { asm volatile("s_waitcnt vmcnt(%0)\n\ts_barrier" ::"n"(N) : "memory"); }
DEVINL void bar() { asm volatile("s_waitcnt lgkmcnt(0)\n\ts_barrier" ::: "memory"); }

DEVINL void cnt_add(u32 c) {
  u32 a, o;
  asm volatile("v_mov_b32 %0, %2\n\tv_mov_b32 %1, 1\n\ts_mov_b64 exec, 1\n\tds_add_u32 %0, %1\n\ts_mov_b64 exec, -1" : "=&v"(a), "=&v"(o) : "n"(L_CNT + 4 * c) : "memory");
}
DEVINL void cnt_wait(u32 c) {
  for (u32 spins = 0; spins < (SPIN_CAP << 4); ++spins) {
    u32 v;
    asm volatile("v_mov_b32 %0, %1\n\tds_read_b32 %0, %0\n\ts_waitcnt lgkmcnt(0)" : "=v"(v) : "n"(L_CNT + 4 * c) : "memory");
    if (__builtin_amdgcn_readfirstlane(v) >= 4u) break;
    __builtin_amdgcn_s_sleep(1);
  }
}
template <int N> DEVINL void sync4(u32 c) {
  asm volatile("s_waitcnt vmcnt(%0) lgkmcnt(0)" ::"n"(N) : "memory");
  cnt_add(c);
  cnt_wait(c);
}
DEVINL void poll_flags(const char* base, u32 voff, u32 tag) {
  for (u32 spins = 0; spins < SPIN_CAP; ++spins) {
    u32 v;
    asm volatile("global_load_dword %0, %1, %2 sc1\n\ts_waitcnt vmcnt(0)" : "=v"(v) : "v"(voff), "s"(base) : "memory");
    if (__all(v == tag)) break;
  }
}

#define STAMP(i)

#define FS(i)
DEVINL void feature_map2(const u32x4 (&wa)[5], const u32x2 (&wb)[5], const f32x4 (&t0)[2], const f32x4 (&t1)[2], const f32x4 (&t2)[2], u32 (&ph)[2][9]) {
  float p[2][20];
#pragma unroll
  for (int m = 0; m < 2; ++m) {
    const u32x2 p0 = pack4(t0[m]), p1 = pack4(t1[m]), p2 = pack4(t2[m]);
    const u32x4 b01 = {p0.x, p0.y, p1.x, p1.y};
    f32x4 a[5];
#pragma unroll
    for (int t = 0; t < 5; ++t) a[t] = mfma32(wa[t], b01, f32x4{0.f, 0.f, 0.f, 0.f});
#pragma unroll
    for (int t = 0; t < 5; ++t) {
      a[t] = mfma16(wb[t], p2, a[t]);
      p[m][4 * t] = a[t].x; p[m][4 * t + 1] = a[t].y; p[m][4 * t + 2] = a[t].z; p[m][4 * t + 3] = a[t].w;
    }
  }
  const float L2E = 1.44269504088896341f;
#pragma unroll
  for (int m = 0; m < 2; ++m) {
    float mx = p[m][0];
#pragma unroll
    for (int i = 1; i < 18; ++i) mx = fmaxf(mx, p[m][i]);
    const float nm = -mx * L2E;
    f32x2 sum = {0.f, 0.f};
#pragma unroll
    for (int i = 0; i < 18; ++i) p[m][i] = __builtin_amdgcn_exp2f(__builtin_fmaf(p[m][i], L2E, nm));
#pragma unroll
    for (int i = 0; i < 9; ++i) sum += f32x2{p[m][2 * i], p[m][2 * i + 1]};
    const float inv = __builtin_amdgcn_rcpf(sum.x + sum.y);
#pragma unroll
    for (int i = 0; i < 9; ++i) ph[m][i] = pack2(p[m][2 * i] * inv, p[m][2 * i + 1] * inv);
  }
}

DEVINL float gelu_tanh(float x) {
  const float C = -2.f * 0.7978845608028654f * 1.44269504088896341f;
  float y = x * __builtin_fmaf(x * x, 0.044715f * C, C);
  float e = __builtin_amdgcn_exp2f(y);
  return x * __builtin_amdgcn_rcpf(1.f + e);
}

__global__ __launch_bounds__(NT, 2) void fused_kernel(Params P) {
  extern __shared__ __attribute__((aligned(16))) char smem[];
  STAMP(0); FS(0);
  const u32 w = __builtin_amdgcn_readfirstlane(threadIdx.x >> 6);
  const u32 bid = blockIdx.x, xcd = bid & 7, idx = bid >> 3;
  char* const ws = P.ws;
  u64 pc; u32 len;
  asm volatile("s_getpc_b64 %0\n.Lfk_code_begin:\n\ts_mov_b32 %1, .Lfk_code_end-.Lfk_code_begin\n\ts_max_u32 %1, %1, .Lfk_copy_end-.Lfk_code_begin" : "=s"(pc), "=s"(len)::"scc");

  const u32 cg = (xcd & 1) * 4 + (idx & 3), rb = (xcd >> 1) * 8 + (idx >> 2);
  const u32 rot = (idx >> 2) % 6;
  const u32 w3 = w & 3, kh = w >> 2;
  const u32 kq = xcd >> 1, kp = xcd & 1;
  const bool g1 = idx < 27;
  const u32 ni = kq * 9 + (g1 ? idx / 3 : 0), mi = idx % 3;
  u32 tag, tag0;
  u32x4 wf[36];
  u32x4 xr[9];
  const u32 rgp = w ^ ((cg & 1) * 4);
  auto load_x = [&]() __attribute__((always_inline)) {
    LANEVARS;
    const u32 xo = (rb * 64 + rgp * 8 + (lane >> 3)) * 1152 + (lane & 7) * 16;
#pragma unroll
    for (int i = 0; i < 9; ++i) ld128(xr[i], P.x, xo, i * 128);
    asm volatile("s_load_dword %0, %1, 0x0" : "=s"(tag0) : "s"(ws) : "memory");
    FS(1);
  };
  auto layer_norm = [&]() __attribute__((always_inline)) {
    LANEVARS;
    f32x2 c[36];
    f32x2 s1 = {0.f, 0.f}, s2 = {0.f, 0.f};
#pragma unroll
    for (int i = 0; i < 9; ++i) {
#pragma unroll
      for (int j = 0; j < 4; ++j) {
        const f32x2 v = {bflo(xr[i][j]), bfhi(xr[i][j])};
        c[i * 4 + j] = v;
        s1 += v; s2 += v * v;
      }
    }
    const float mean = sum8(s1.x + s1.y) * (1.f / 576.f);
    const float var = fmaxf(sum8(s2.x + s2.y) * (1.f / 576.f) - mean * mean, 0.f);
    const float rs = __builtin_amdgcn_rsqf(var + 1e-5f), nb = -mean * rs;
    const u32 a0 = L_XN + ((rgp >> 1) * 18 + ((lane & 7) >> 2)) * 1024 + (lane & 3) * 256 + (((8 * (rgp & 1) + (lane >> 3)) ^ (lane & 7)) * 16);
#pragma unroll
    for (int i = 0; i < 9; ++i) {
      u32x4 o;
#pragma unroll
      for (int j = 0; j < 4; ++j) { const f32x2 v = c[i * 4 + j] * rs + nb; o[j] = pack2(v.x, v.y); }
      lds_w128(a0 + i * 2048, o);
    }
  };
#define WAIT_X(N) asm volatile("s_waitcnt vmcnt(" #N ")" : "+v"(xr[0]), "+v"(xr[1]), "+v"(xr[2]), "+v"(xr[3]), "+v"(xr[4]), "+v"(xr[5]), "+v"(xr[6]), "+v"(xr[7]), "+v"(xr[8])::"memory")
  auto unit_at = [](int u) -> u32 { return u < 6 ? 2 * SLOT + u * 8192 : (u < 9 ? (u - 6) * 8192 : (u < 12 ? SLOT + (u - 9) * 8192 : 2 * SLOT + (u - 6) * 8192)); };
  auto grab1 = [&](const LAddr& t, int ks, int u) __attribute__((always_inline)) {
    const u32 sl = unit_at(u);
    wf[2 * ks] = lds_tr2(t(sl), t(sl + 1024));
    wf[2 * ks + 1] = lds_tr2(t(sl + 4096), t(sl + 4096 + 1024));
  };
  u32x4 hrow[3];
  const u32 wl = w3 * 3456;
  const char* wq = P.wqkv + cg * 144;
  auto stage = [&](u32 n) __attribute__((always_inline)) -> u32 { const u32 st = rot + n; return (st >= 6 ? st - 6 : st) * (96 * 3456); };
#define RING_DEF \
  const u32 r3 = lane / 18, c18 = lane % 18, seg = c18 / 9, ch9 = c18 % 9; \
  u32 wrow[4], voff[4]; \
  _Pragma("unroll") for (int i = 0; i < 4; ++i) { \
    const u32 r = 12 * w3 + 3 * i + r3; \
    wrow[i] = (r & ~12u) | ((r & 4u) << 1) | ((r & 8u) >> 1); \
    voff[i] = wrow[i] * 3456 + seg * 1152 + ch9 * 16; \
    pin(voff[i]); \
  } \
  auto issue1 = [&](int n, int j0, int j1) __attribute__((always_inline)) {   \
    constexpr u32 slot[9] = {0, 1, 2, 3, 4, 0, 1, 2, 3}; \
    const char* b = n < 6 ? wq + 1152 + stage(n) : wq; \
    const u32 qo = n < 6 ? 0u : (seg ? stage(2 * (n - 6) + 1) - 1152 : stage(2 * (n - 6))); \
    _Pragma("unroll") for (int j = j0; j < j1; ++j) \
      dma<54, false>(b + (j >> 2) * (48 * 3456), voff[j & 3] + qo, wl, slot[n] * SLOT + (j >> 2) * 13824 + (j & 3) * 864); \
  }

  if (__builtin_expect(w < 4, 1)) {
    const u32 mp = w >> 1, hp = w & 1;
    load_x();
    {
      LANEVARS;
      RING_DEF;
      WAIT_X(0);
      STAMP(1); FS(2);
      layer_norm();
      FS(3);
      issue1(1, 0, 8);
    }
    u32 ta0, ta1, xa[2];
    {
      LANEVARS;
      if (lane < 32) *(LAS u32*)(L_ZERO + lane * 4) = 0u;
      ta0 = trrow + hp * 72 + p4 * 8; ta1 = trrow + hp * 72 + 64 + (p4 & 1) * 144;
#pragma unroll
      for (int i = 0; i < 2; ++i) { xa[i] = L_XN + mp * 36864 + g * 256 + ((l15 ^ (((i + rot) & 1) * 4 + g)) * 16); pin(xa[i]); }
    }
    const LAddr trA(ta0);
    FS(4);
    asm volatile("s_waitcnt lgkmcnt(0)\n\ts_barrier\n\ts_add_u32 %0, %1, 1" : "=s"(tag) : "s"(tag0) : "memory", "scc");
    STAMP(2); FS(5);
    u32x4 xf[2][18];
    auto rdx = [&](int n) __attribute__((always_inline)) {
      u32 st = rot + n / 3; if (st >= 6) st -= 6;
      const u32 ad = xa[(n / 3 + n % 3) & 1] + st * 3072;
      xf[0][n] = lds_r128(ad + (n % 3) * 1024);
      xf[1][n] = lds_r128(ad + (n % 3) * 1024 + 18432);
    };
    f32x4 acc[5][2];
#pragma unroll
    for (int i = 0; i < 5; ++i) { acc[i][0] = f32x4{0.f, 0.f, 0.f, 0.f}; acc[i][1] = acc[i][0]; }
    u32x4 wa[5]; u32x2 wb[5];
    {
      const LAddr trM(ta1);
      u32x4 F[6];
      u32 ad[3];
      auto rd = [&](int u) __attribute__((always_inline)) {
        constexpr u32 toff[4] = {0, 32, 144, 176};
        const int n = u / 5, i = u % 5;
        const u32 b = ((n / 3) % 5) * SLOT + (n % 3) * 9216;
        F[u % 6] = i < 4 ? lds_tr2(trA(b + toff[i & 3]), trA(b + toff[i & 3] + 2304)) : lds_tr2(trM(b), trM(b + 2304));
      };
      rdx(0);
#pragma unroll
      for (int u = 0; u < 5; ++u) rd(u);
      rdx(1);
      SB();
#pragma unroll
      for (int n = 0; n < 18; ++n) {
        if (n == 14) {
          LANEVARS;
#pragma unroll
          for (int a = 0; a < 3; ++a) {
            const u32 ic = a < 2 ? 16 * a + 4 * g + q4 : 32 + q4;
            const bool ok = (ic >= 18 ? 1u : 0u) == (p4 >> 1) && (a < 2 || g == 0);
            ad[a] = ok ? L_WPHI + (p4 & 1) * 2880 + (hp * 36 + ic) * 40 : L_ZERO;
            pin(ad[a]);
          }
        }
        if (n % 3 == 2 && n < 17) { FS(6 + 2 * (n / 3)); bar_v<0>(); FS(7 + 2 * (n / 3)); }
#pragma unroll
        for (int i = 0; i < 5; ++i) {
          const int u = n * 5 + i;
          if (u + 5 < 90) rd(u + 5);
          else { wa[i] = lds_tr2(ad[0] + 8 * i, ad[1] + 8 * i); wb[i] = lds_tr(ad[2] + 8 * i); }
          if (u < 8) { rdx(2 + 2 * u); rdx(3 + 2 * u); }
          acc[i][0] = mfma32(F[u % 6], xf[0][n], acc[i][0]);
          acc[i][1] = mfma32(F[u % 6], xf[1][n], acc[i][1]);
          SB();
        }
      }
    }
    STAMP(3); FS(16);
    {
      LANEVARS;
      const u32 va = L_V + (mp * 32 + l15) * 152 + hp * 72 + g * 8;
#pragma unroll
      for (int m = 0; m < 2; ++m) {
        lds_w64(va + m * (16 * 152), pack4(acc[2][m]));
        lds_w64(va + m * (16 * 152) + 32, pack4(acc[3][m]));
        if (g == 1) lds_w64(va + m * (16 * 152) + 56, pack4(acc[4][m]));
      }
      u32 ph[2][9];
      feature_map2(wa, wb, acc[0], acc[1], acc[4], ph);
      const u32 pa = L_PHI + (mp * 32 + l15) * 328 + (hp * 4 + g) * 40;
#pragma unroll
      for (int m = 0; m < 2; ++m)
#pragma unroll
        for (int i = 0; i < 5; ++i) lds_w64(pa + m * (16 * 328) + 8 * i, u32x2{ph[m][2 * i], i < 4 ? ph[m][2 * i + 1] : 0u});
    }
    FS(17);
    bar();
    FS(18);
    bar();
    STAMP(4); FS(20);
#pragma unroll
    for (int i = 0; i < 3; ++i) { acc[i][0] = f32x4{0.f, 0.f, 0.f, 0.f}; acc[i][1] = acc[i][0]; }
    {
      u32 tq;
      { LANEVARS; tq = trrow + hp * 72 + 64; }
      const LAddr trQ(tq);
      u32x4 F[6];
      auto rd = [&](int u) __attribute__((always_inline)) {
        const int n = u / 3, i = u % 3;
        const u32 b = (1 + n / 6) * SLOT + (n % 3) * 9216 + ((n % 6) / 3) * 144;
        F[u % 6] = i < 2 ? lds_tr2(trA(b + 32 * i), trA(b + 32 * i + 2304)) : lds_tr2(trQ(b), trQ(b + 2304));
      };
#pragma unroll
      for (int u = 0; u < 5; ++u) rd(u);
      SB();
#pragma unroll
      for (int u = 0; u < 54; ++u) {
        if (u + 5 < 54) rd(u + 5);
        acc[u % 3][0] = mfma32(F[u % 6], xf[0][u / 3], acc[u % 3][0]);
        acc[u % 3][1] = mfma32(F[u % 6], xf[1][u / 3], acc[u % 3][1]);
        SB();
      }
    }
    STAMP(5); FS(21);
    u32 pq[2][9];
    feature_map2(wa, wb, acc[0], acc[1], acc[2], pq);
    LANEVARS;
    u32 oa[3];
    {
      const bool same = (q4 >> 1) == (g >> 1);
#pragma unroll
      for (int t = 0; t < 3; ++t) {
        const u32 slot = 4 * t + p4;
        const u32 cc = slot < 9 ? 9 * (q4 & 1) + slot : 18;
        oa[t] = same ? L_KV + ((hp * 2 + (g >> 1)) * 19 + cc) * 96 + (g & 1) * 48 : L_ZERO;
        pin(oa[t]);
      }
    }
    const char* w1b = P.w1 + (size_t)(kp * 1024 + w * 4) * 9216 + ni * 256;
    u32 w1off = g * 9216 + (((q4 ^ g) << 2) | p4) * 16, lane16 = lane * 16;
    const LAddr t1w((8 * (g >> 1) + q4) * 256 + (w ^ q4) * 64 + (g & 1) * 32 + p4 * 8);
    auto issue_w1 = [&](int k0, int u0, int n) __attribute__((always_inline)) {
#pragma unroll
      for (int j = 0; j < n; ++j) {
        u32 r = j + ROT1 * ((mi * n) / 3); if (r >= (u32)n) r -= n;
        const u32 u = u0 + r;
        const u32 lds = (u < 6 ? 2 * SLOT + u * 8192 : (u < 9 ? (u - 6) * 8192 : (u < 12 ? SLOT + (u - 9) * 8192 : 2 * SLOT + (u - 6) * 8192))) + w * 1024;
        const char* b = w1b + (size_t)(k0 + r) * (32 * 9216);
        dma_w1(b, w1off, lds);
        dma_w1(b + 16 * 9216, w1off, lds + 4096);
      }
    };
    pin(w1off); pin(lane16);
#pragma unroll
    for (int m = 0; m < 2; ++m)
#pragma unroll
      for (int i = 0; i < 9; ++i) pin(pq[m][i]);
    STAMP(6); FS(22);
    bar();
    STAMP(7); FS(23);
    {
      u32x4 k0[3], k1[3]; u32x2 k2[3];
#pragma unroll
      for (int t = 2; t >= 0; --t) { k0[t] = lds_r128(oa[t]); k1[t] = lds_r128(oa[t] + 16); k2[t] = lds_r64(oa[t] + 32); }
      SB();
      f32x4 nu[2][3];
#pragma unroll
      for (int t = 2; t >= 0; --t)
#pragma unroll
        for (int m = 0; m < 2; ++m) {
          f32x4 c = mfma32(k0[t], u32x4{pq[m][0], pq[m][1], pq[m][2], pq[m][3]}, f32x4{0.f, 0.f, 0.f, 0.f});
          c = mfma32(k1[t], u32x4{pq[m][4], pq[m][5], pq[m][6], pq[m][7]}, c);
          nu[m][t] = mfma16(k2[t], u32x2{pq[m][8], 0u}, c);
        }
      char* ob = ws + W_O + (rb * 4 + mp * 2) * 18432;
#pragma unroll
      for (int m = 0; m < 2; ++m) {
        const float inv = __builtin_amdgcn_rcpf(nu[m][2].y + 1e-6f);
        const u32x2 a = pack4(nu[m][0] * inv), b = pack4(nu[m][1] * inv);
        st128_sc1(ob + m * 18432 + (cg * 2 + hp) * 1024, lane16, u32x4{a.x, a.y, b.x, b.y});
        const u32 e0 = asu(nu[m][2].x * inv);
        auto t16 = __builtin_amdgcn_permlane16_swap(e0, e0, false, false);
        const u32 e01 = pack2(asf(t16[0]), asf(t16[1]));
        auto t32 = __builtin_amdgcn_permlane32_swap(e01, e01, false, false);
        asm volatile("s_mov_b64 exec, 0xffff\n\tglobal_store_dwordx2 %0, %1, %2 sc1\n\ts_mov_b64 exec, -1" ::"v"(lane16), "v"(u32x2{t32[0], t32[1]}), "s"(ob + m * 18432 + 16384 + cg * 256 + hp * 8) : "memory");
      }
    }
    FS(24);
    cnt_add(9);
    waitv<0>();
    st_flag(ws + W_FLAGB + ((rb * 2 + mp) * 16 + cg * 2 + hp) * 128, tag);
    STAMP(8); FS(25);
    cnt_wait(6);
    FS(42);
    issue_w1(0, 0, 9);
    sync4<0>(0);
#pragma unroll
    for (int ks = 0; ks < 9; ++ks) grab1(t1w, ks, ks);
    sync4<63>(1);
    FS(44);
    issue_w1(9, 0, 9);
    sync4<0>(2);
#pragma unroll
    for (int ks = 9; ks < 16; ++ks) grab1(t1w, ks, ks - 9);
    sync4<63>(3);
    FS(43);
    cnt_wait(7);
    issue_w1(18, 0, 7);
    issue_w1(25, 9, 7);
    FS(38);
    bar_vo<0>();
#pragma unroll
    for (int j = 0; j < 3; ++j) { const u32 it = min(j * 64 + lane, 143u); hrow[j] = lds_r128(L_HI + (w * 16 + it / 9) * 160 + (it % 9) * 16); }
#pragma unroll
    for (int j = 0; j < 3; ++j) {
      const u32 it = min(j * 64 + lane, 143u);
      st128_sc1(ws + W_H + (rb * 64 + w * 16) * 1152 + cg * 144, (it / 9) * 1152 + (it % 9) * 16, hrow[j]);
    }
    FS(40);
    bar();
  } else {
    LANEVARS;
    const u32 lane16 = lane * 16;
    u32 pfd = 0;
    asm volatile("global_load_dword %0, %1, %2" : "+v"(pfd) : "v"(min(((idx * 4 + w3) * 4 + p4) * 128u, len)), "s"(pc) : "memory");
    load_x();
    RING_DEF;
    u32 wp[3];
    {
      const u32 so = (min(lane, 59u) / 10) * 36 + min(lane % 10, 8u) * 4;
#pragma unroll
      for (int j = 0; j < 3; ++j) ld32(wp[j], P.wphi + cg * 2592 + w3 * 648 + j * 216, so);
    }
    issue1(0, 0, 8);
    WAIT_X(8);
    FS(2);
    pin(wp[0]); pin(wp[1]); pin(wp[2]);
    layer_norm();
    if (lane < 60) {
#pragma unroll
      for (int j = 0; j < 3; ++j) {
        *(LAS u32*)(L_WPHI + w3 * 720 + j * 240 + lane * 4) = wp[j];
        *(LAS u32*)(L_WPHI + 2880 + w3 * 720 + j * 240 + lane * 4) = wp[j] ^ 0x80008000u;
      }
    }
    FS(4);
    asm volatile("s_waitcnt vmcnt(0) lgkmcnt(0)\n\ts_barrier\n\ts_add_u32 %0, %1, 1" : "=s"(tag) : "s"(tag0) : "memory", "scc");
    issue1(2, 0, 8);
    FS(6);
    bar_vo<8>();
    issue1(3, 0, 8);
    FS(8);
    bar_vo<8>();
    issue1(4, 0, 8);
    FS(10);
    bar_vo<8>();
    issue1(5, 0, 8);
    FS(12);
    bar_vo<8>();
    {
      const u32 ln = min((idx * 4 + w3) * 23 + min(lane, 22u), 2879u);
      touch(pfd, P.wo + (xcd & 1) * 512, (ln / 5) * 1152 + (ln % 5) * 128);
    }
    issue1(6, 0, 4);
    FS(14);
    bar_vo<5>();
    issue1(6, 4, 8);
    issue1(7, 0, 8);
    const u32 srow = 8 * g + q4;
    u32 e_vb0 = L_V + srow * 152 + (w3 >> 1) * 72 + ((w3 & 1) ? 40 : 0) + p4 * 8, e_vb1 = L_V + srow * 152 + (w3 >> 1) * 72 + 32;
    u32 e_pb = L_PHI + srow * 328 + w3 * 80 + p4 * 8, e_pb2 = L_PHI + srow * 328 + w3 * 80 + 64 + (p4 & 1) * 8;
    u32 e_s0 = (lane >> 3) * 4096 + (lane & 7) * 16, e_s1[3];
    const u32 colp = l15 == 4 ? 2u : l15 - 2 * (w3 & 1);
#pragma unroll
    for (int i = 0; i < 3; ++i) {
      const u32 sidx = (160 + min(colp, 2u) * 10 + 4 * i + g);
      e_s1[i] = (sidx >> 3) * 4096 + (sidx & 7) * 16;
      pin(e_s1[i]);
    }
    const u64 e_m01 = __ballot(l15 == 4 || colp < 2), e_m2 = __ballot(l15 == 4 || (colp < 2 && g < 2));
    pin(e_vb0); pin(e_vb1); pin(e_pb); pin(e_pb2); pin(e_s0);
    issue1(8, 0, 8);
#pragma unroll
    for (int i = 0; i < 4; ++i) { voff[i] = (9 * (wrow[i] >> 3) + (wrow[i] & 7) + seg * 324) * 1152 + ch9 * 16; pin(voff[i]); }
    const char* wob = P.wo + cg * 144;
    FS(17);
    bar();
    FS(18);
    {
      u32x4 vf[2][2], pf_[2][3];
#pragma unroll
      for (int ks = 0; ks < 2; ++ks) {
        vf[ks][0] = lds_tr2(e_vb0 + ks * (32 * 152), e_vb0 + ks * (32 * 152) + 4 * 152);
        pf_[ks][0] = lds_tr2(e_pb + ks * (32 * 328), e_pb + ks * (32 * 328) + 4 * 328);
        pf_[ks][1] = lds_tr2(e_pb + ks * (32 * 328) + 32, e_pb + ks * (32 * 328) + 32 + 4 * 328);
        pf_[ks][2] = lds_tr2(e_pb2 + ks * (32 * 328), e_pb2 + ks * (32 * 328) + 4 * 328);
      }
#pragma unroll
      for (int ks = 0; ks < 2; ++ks) vf[ks][1] = lds_tr2(e_vb1 + ks * (32 * 152), e_vb1 + ks * (32 * 152) + 4 * 152);
      SB();
      f32x4 kv[2][3];
#pragma unroll
      for (int vt = 0; vt < 2; ++vt)
#pragma unroll
        for (int ks = 0; ks < 2; ++ks) {
          if (vt && l15 >= 4) vf[ks][1] = u32x4{0x3f803f80u, 0x3f803f80u, 0x3f803f80u, 0x3f803f80u};
#pragma unroll
          for (int i = 0; i < 3; ++i) kv[vt][i] = mfma32(pf_[ks][i], vf[ks][vt], ks ? kv[vt][i] : f32x4{0.f, 0.f, 0.f, 0.f});
        }
      char* kb = ws + W_KVPART + ((cg * 96 + w3 * 24) * 32 + rb) * 128;
#pragma unroll
      for (int i = 0; i < 3; ++i) {
        const u32x2 pk = pack4(kv[0][i]);
        st128_sc1_m(kb + i * 32768, e_s0, u32x4{pk.x, tag, pk.y, tag}, i < 2 ? ~0ull : 0xffffffffull);
      }
#pragma unroll
      for (int i = 0; i < 3; ++i) {
        const u32x2 pk = pack4(kv[1][i]);
        st128_sc1_m(kb, e_s1[i], u32x4{pk.x, tag, pk.y, tag}, i < 2 ? e_m01 : e_m2);
      }
    }
    FS(19);
    bar_v<6>();
#pragma unroll
    for (int j = 0; j < 8; ++j) dma<54, false>(wob + (j >> 2) * (54 * 1152), voff[j & 3], wl, (j >> 2) * 13824 + (j & 3) * 864);
    u32x2 xres[5];
    {
      const u32 xo = (rb * 64 + w3 * 16 + l15) * 1152 + cg * 144;
#pragma unroll
      for (int n = 0; n < 4; ++n) ld64(xres[n], P.x, xo + g * 8, n * 32);
      ld64(xres[4], P.x, xo + (g & 1) * 8, 128);
    }
    u32 la[3], vo2[8];
    {
#pragma unroll
      for (int a = 0; a < 3; ++a) {
        const u32 r = a * 64 + lane;
        const u32 rr = r - 160, cp = (rr * 205) >> 11;
        const u32 cc = r < 160 ? (r & 15) + 2 * (w3 & 1) : (cp >= 2 ? 18 : ((w3 & 1) ? cp : 16 + cp));
        const u32 piece = r < 160 ? (r >> 4) : rr - cp * 10;
        la[a] = r < 190 ? L_KV + (w3 * 19 + cc) * 96 + piece * 8 + (piece >= 5 ? 8 : 0) : L_KV + 40;
        pin(la[a]);
      }
#pragma unroll
      for (int j = 0; j < 8; ++j) {
        const u32 kap = 192 + (j >> 2) * 48 + wrow[j & 3] + seg * 288;
        vo2[j] = (kap < 512 ? 9 * (kap >> 3) + (kap & 7) : 9 * (kap - 512) + 8) * 1152 + ch9 * 16;
        pin(vo2[j]);
      }
    }
    FS(26);
    if (w3 < 3) {
      const char* pb = ws + W_KVPART + (cg * 96 + rb * 3 + w3) * 4096;
      f32x4 sum;
      for (u32 spins = 0; spins < SPIN_CAP; ++spins) {
        u32x4 v[4];
#pragma unroll
        for (int i = 0; i < 4; ++i) ld128_sc1(v[i], pb, lane16, i * 1024);
        waitv<0>();
        u32 bad = 0;
        sum = f32x4{0.f, 0.f, 0.f, 0.f};
#pragma unroll
        for (int i = 0; i < 4; ++i) {
          pin(v[i]);
          bad |= (v[i].y ^ tag) | (v[i].w ^ tag);
          sum += unpk(v[i].x, v[i].z);
        }
        pin(bad);
        if (__all(bad == 0)) break;
      }
#pragma unroll
      for (int j = 0; j < 4; ++j) sum[j] = sum_g(sum[j] + dpp<0x128>(sum[j]));
      const u32x2 pk = pack4(sum);
      st128_sc1_m(ws + W_KVFIN + (cg * 96 + rb * 3 + w3) * 128, lane16, u32x4{pk.x, tag, pk.y, tag}, 0xffull);
      cnt_add(8);
    } else {
      cnt_add(8); cnt_wait(8);
    }
    FS(27);
#pragma unroll
    for (int j = 0; j < 8; ++j) dma<54, false>(wob + (108 + (j >> 2) * 54) * 1152, voff[j & 3], wl, 4 * SLOT + (j >> 2) * 13824 + (j & 3) * 864);
    {
      FS(28);
      const char* fb = ws + W_KVFIN + (cg * 4 + w3) * 3072;
      u32x4 v[3];
      for (u32 spins = 0; spins < SPIN_CAP; ++spins) {
#pragma unroll
        for (int i = 0; i < 3; ++i) ld128_sc1(v[i], fb, lane16, i * 1024);
        waitv<0>();
        u32 bad = 0;
#pragma unroll
        for (int i = 0; i < 3; ++i) { pin(v[i]); bad |= (v[i].y ^ tag) | (v[i].w ^ tag); }
        pin(bad);
        if (__all(bad == 0)) break;
      }
#pragma unroll
      for (int i = 0; i < 3; ++i) lds_w64(la[i], u32x2{v[i].x, v[i].z});
    }
    FS(22);
    bar();
#pragma unroll
    for (int j = 0; j < 8; ++j) dma<54, false>(wob, vo2[j], wl, SLOT + (j >> 2) * 13824 + (j & 3) * 864);
    const LAddr wo0(trrow + p4 * 8), wo4(trrow + 128 + (p4 & 1) * 8);
    auto rdw = [&](u32 b, int n) __attribute__((always_inline)) -> u32x4 {
      return n < 4 ? lds_tr2(wo0(b + 32 * n), wo0(b + 32 * n + 2304)) : lds_tr2(wo4(b), wo4(b + 2304));
    };
    u32x4 wm[30];
#pragma unroll
    for (int u = 0; u < 30; ++u) wm[u] = rdw(((u / 5) % 3) * 9216 + (u / 15) * 144, u % 5);
    sync4<0>(5);
    f32x4 hc[5];
#pragma unroll
    for (int n = 0; n < 5; ++n) { pin(xres[n]); hc[n] = unpk(xres[n].x, xres[n].y); pin(hc[n]); }
    {
      u32x4 F[6];
      auto rd = [&](int u) __attribute__((always_inline)) {
        const int v = u / 5;
        F[u % 6] = rdw((v < 6 ? 4 * SLOT : SLOT) + (v % 3) * 9216 + ((v / 3) & 1) * 144, u % 5);
      };
#pragma unroll
      for (int u = 0; u < 5; ++u) rd(u);
      FS(29);
      poll_flags(ws + W_FLAGB + (rb * 2 + (w3 >> 1)) * 2048, l15 * 128, tag);
      STAMP(9); FS(30);
      constexpr int ord[18] = {3, 4, 5, 12, 13, 14, 6, 7, 8, 15, 16, 17, 0, 1, 2, 9, 10, 11};
      u32x4 of[18];
      const char* ob = ws + W_O + (rb * 4 + w3) * 18432;
#pragma unroll
      for (int j = 0; j < 18; ++j) ld128_sc1(of[j], ob + (ord[j] >> 2) * 4096, lane16, (ord[j] & 3) * 1024);
      cnt_add(6);
      FS(31);
#pragma unroll
      for (int j = 0; j < 18; ++j) {
        asm volatile("s_waitcnt vmcnt(%1)" : "+v"(of[j]) : "n"(17 - j) : "memory");
#pragma unroll
        for (int n = 0; n < 5; ++n) {
          const int u = j * 5 + n;
          if (u + 5 < 60) rd(u + 5);
          hc[n] = mfma32(u < 60 ? F[u % 6] : wm[u - 60], of[j], hc[n]);
          SB();
        }
      }
    }
    STAMP(10); FS(32);
    float s1 = 0.f, s2 = 0.f;
    u32x2 hr[5];
#pragma unroll
    for (int n = 0; n < 5; ++n) {
      hr[n] = pack4(hc[n]);
      if (n == 4 && g >= 2) hr[n] = u32x2{0u, 0u};
      s1 = dot2(hr[n].x, 0x3f803f80u, s1); s1 = dot2(hr[n].y, 0x3f803f80u, s1);
      s2 = dot2(hr[n].x, hr[n].x, s2); s2 = dot2(hr[n].y, hr[n].y, s2);
    }
    s1 = sum_g(s1); s2 = sum_g(s2);
    char* sb = ws + W_STATS + (rb * 4 + w3) * 2048;
    st128_sc1_m(sb + cg * 256, lane16, u32x4{asu(s1), tag, asu(s2), tag}, 0xffffull);
    FS(34);
    cnt_wait(9);
    u32 hw = L_HI + (w3 * 16 + l15) * 160 + g * 8;
#pragma unroll
    for (int n = 0; n < 5; ++n) {
      lds_w64(hw + n * 32, hr[n]);
      hc[n] = unpk(hr[n].x, hr[n].y);
      pin(hc[n]);
    }
    u32 hrd = L_HN + (w3 * 16 + 8 * (g & 1) + q4) * 160 + ((g >> 1) * 16 + p4 * 4) * 2;
    u32 hrx = L_HN + (w3 * 16 + 8 * (g & 1) + q4) * 160 + (64 + (p4 & 1) * 4) * 2;
    u32 ho[2];
#pragma unroll
    for (int j = 0; j < 2; ++j) {
      const u32 d = cg * 72 + (j ? 64 + (l15 & 7) : (g >> 1) * 16 + l15);
      ho[j] = (d >> 5) * 131072 + (g & 1) * 512 + (d & 31) * 16;
      pin(ho[j]);
    }
    const LAddr t1w((8 * (g >> 1) + q4) * 256 + (w3 ^ q4) * 64 + ((g & 1) ^ 1) * 32 + p4 * 8);
    pin(hw); pin(hrd); pin(hrx);
    char* hb = ws + W_HN + (rb * 4 + w3) * 1024;
    u32x4 v0, v1;
    FS(35);
    for (u32 spins = 0; spins < SPIN_CAP; ++spins) {
      ld128_sc1(v0, sb, lane16);
      ld128_sc1(v1, sb, lane16, 1024);
      waitv<0>();
      pin(v0); pin(v1);
      u32 bad = (v0.y ^ tag) | (v0.w ^ tag) | (v1.y ^ tag) | (v1.w ^ tag);
      pin(bad);
      if (__all(bad == 0)) break;
    }
    pin(pfd);
    cnt_add(7);
    FS(36);
    s1 = sum_g(asf(v0.x) + asf(v1.x));
    s2 = sum_g(asf(v0.z) + asf(v1.z));
    const float mean = s1 * (1.f / 576.f);
    const float var = fmaxf(s2 * (1.f / 576.f) - mean * mean, 0.f);
    const float r1 = __builtin_amdgcn_rsqf(var + 1e-5f);
    const float r2 = r1 * __builtin_amdgcn_rsqf(var * r1 * r1 + 1e-5f);
    const float nb = -mean * r2;
#pragma unroll
    for (int n = 0; n < 5; ++n) lds_w64(hw + 10240 + n * 32, pack4(hc[n] * r2 + nb));
    const u32x4 f0 = lds_tr2(hrd, hrd + 4 * 160), f1 = lds_tr2(hrd + 64, hrd + 64 + 4 * 160), f2 = lds_tr2(hrx, hrx + 4 * 160);
    STAMP(11); FS(37);
    st128_sc1(hb, ho[0], f0);
    st128_sc1(hb + 131072, ho[0], f1);
    st128_sc1_m(hb, ho[1], f2, 0x00ff00ffull);
    sync4<0>(10);
    if (w == 4) st_flag(ws + W_FLAGD + (cg * 32 + rb) * 128, tag);
    FS(38);
    asm volatile("s_barrier" ::: "memory");
#pragma unroll
    for (int ks = 0; ks < 16; ++ks) grab1(t1w, ks, ks < 2 ? ks + 7 : (ks < 9 ? ks - 2 : ks));
    FS(40);
    bar_v<0>();
    asm volatile(".Lfk_copy_end:");
  }
  STAMP(12); FS(39);
  LANEVARS;
  const u32 lane16 = lane * 16;
  const u32 wcol = (((q4 ^ g) << 2) | p4) * 16;

  const u32 Mq = idx & 3, Nq = kp * 8 + (idx >> 2), odd = Mq & 1;
  const char* w2b = P.w2 + (size_t)(kq * 1152 + w * 4) * 4096 + Nq * 256;
  const u32 w2off = g * 4096 + wcol;
  u32 w1k = w * 1024;
  asm volatile("" : "+s"(w1k));
  auto issue_w2 = [&](int ps, u32 unit) __attribute__((always_inline)) {
    const char* b = w2b + (ps < 9 ? ps + 9 * odd : ps - 9 * odd) * (32 * 4096);
    dma<64, false>(b, w2off, w1k, unit * 16384);
    dma<64, false>(b + 18 * (32 * 4096), w2off, w1k, unit * 16384 + 8192);
  };
  u32 tw2;
  { LANEVARS; tw2 = kh * 8192 + (4 * (g >> 1) + q4) * 256 + (w3 ^ q4) * 64 + (g & 1) * 32 + p4 * 8; }
#define GRAB2_DEF \
  const LAddr t2w(tw2); \
  auto grab = [&](int ps, u32 unit) __attribute__((always_inline)) { \
    const u32 sl = unit * 16384; \
    wf[2 * ps] = lds_tr2(t2w(sl), t2w(sl + 2048)); \
    wf[2 * ps + 1] = lds_tr2(t2w(sl + 4096), t2w(sl + 4096 + 2048)); \
  }
  const f32x16 zero16 = {0.f, 0.f, 0.f, 0.f, 0.f, 0.f, 0.f, 0.f, 0.f, 0.f, 0.f, 0.f, 0.f, 0.f, 0.f, 0.f};
  if (g1) {
    const u32 ord = mi == 0 ? 0x321504u : (mi == 1 ? 0x251403u : 0x504321u);
    auto tile_of = [&](int t) __attribute__((always_inline)) -> u32 {
      const u32 pos = t < 3 ? 2 * t + (kp ^ 1) : 2 * (t - 3) + kp;
      return mi * 6 + ((ord >> (4 * pos)) & 15u);
    };
    const char* hb = ws + W_HN + kp * 65536;
    u32x2 bias[2];
    const u32 f16 = ni * 8 + 2 * w3 + kh;
    ld64(bias[0], P.b1 + f16 * 32, (lane >> 5) * 8);
    ld64(bias[1], P.b1 + f16 * 32, (lane >> 5) * 8, 16);
    {
      const u32 cj = min((mi * 192) / 72 + ((lane >> 1) & 3), 7u);
      poll_flags(ws + W_FLAGD, (cj * 32 + kp * 16 + 2 * w + (lane & 1)) * 128, tag);
      FS(52);
      const char* tb = hb + tile_of(0) * 131072 + w * 8192;
      const u32 lb = ((w >> 1) & 1 ? 0u : 3 * 32768u) + (w >> 2) * 16384 + (w & 1) * 8192;
#pragma unroll
      for (int j = 0; j < 8; ++j) dma<64, true>(tb + j * 1024, lane16, lb, j * 1024);
    }
    STAMP(13); FS(54);
    bar_v<0>();
    if (w == 0) st_flag(ws + W_FLAGH + (rb * 8 + cg) * 128, tag);
    FS(55);
    auto issue_a = [&](int i, int j) __attribute__((always_inline)) {
      dma<64, true>(hb + tile_of(i >> 1) * 131072 + w1k + ((j >> 1) * 32 + (i & 1) * 16 + (j & 1) * 8) * 1024, lane16, w1k, ((i + 3) & 3) * 32768 + j * 8192);
    };
    const LAddr ar(kh * 16384 + lane16);
    u32 exw = 9 * 16384 + w * 1024 + lane16, exr = 9 * 16384 + (w ^ 4) * 1024 + lane16;
    pin(exw); pin(exr);
    f32x16 cc[2], ci = zero16;
    f32x4 kv0, kv1;
    u32x4 a[6], rx, pv[2], uo;
    pin(bias[0]); pin(bias[1]);
    auto set_init = [&](f32x4 lo, f32x4 hi) __attribute__((always_inline)) {
      ci[0] = lo.x; ci[1] = lo.y; ci[2] = lo.z; ci[3] = lo.w; ci[4] = hi.x; ci[5] = hi.y; ci[6] = hi.z; ci[7] = hi.w;
      pin(ci);
    };
    set_init(unpk(bias[0].x, bias[0].y), unpk(bias[1].x, bias[1].y));
    auto rd = [&](int n) __attribute__((always_inline)) { a[n % 6] = lds_r128(ar((((n >> 4) + 3) & 3) * 32768 + (n & 15) * 1024)); };
    auto load_pv = [&](int t) __attribute__((always_inline)) {
      const char* pb = ws + W_UP + (tile_of(t) * 288 + f16) * 2048;
      ld128_sc1(pv[0], pb, lane16);
      ld128_sc1(pv[1], pb, lane16, 1024);
    };
    auto flag_u = [&](int t) __attribute__((always_inline)) {
      if (w == 0) st_flag(ws + W_FLAGE + (tile_of(t) * 36 + ni) * 128, tag);
    };
    auto exch_p = [&](int t) __attribute__((always_inline)) {
      const u32x2 p2 = pack4(quad<2>(cc[t & 1])), p3 = pack4(quad<3>(cc[t & 1]));
      rx = u32x4{p2.x, p2.y, p3.x, p3.y};
      pin(rx);
    };
    auto combine = [&](int t) __attribute__((always_inline)) {
      kv0 = quad<0>(cc[t & 1]) + unpk(rx.x, rx.y);
      kv1 = quad<1>(cc[t & 1]) + unpk(rx.z, rx.w);
      pin(kv0); pin(kv1);
    };
    auto send = [&](int t, int h) __attribute__((always_inline)) {
      const u32x2 p0 = pack4(h ? kv1 : kv0);
      st128_sc1(ws + W_UP + (tile_of(t) * 288 + f16) * 2048 + h * 1024, lane16, u32x4{p0.x, tag, p0.y, tag});
    };
    auto gather = [&](int t) __attribute__((always_inline)) {
      for (u32 spins = 0; spins < SPIN_CAP; ++spins) {
        pin(pv[0]); pin(pv[1]);
        u32 bad = (pv[0].y ^ tag) | (pv[0].w ^ tag) | (pv[1].y ^ tag) | (pv[1].w ^ tag);
        pin(bad);
        if (__builtin_expect(__all(bad == 0), 1)) break;
        load_pv(t);
        waitv<0>();
      }
      pin(pv[0]); pin(pv[1]);
      set_init(unpk(pv[0].x, pv[0].z), unpk(pv[1].x, pv[1].z));
    };
    auto act = [&](int j) __attribute__((always_inline)) {
      const f32x4 v = j < 2 ? kv0 : kv1;
      u32 t = (j & 1) ? pack2(gelu_tanh(v.z), gelu_tanh(v.w)) : pack2(gelu_tanh(v.x), gelu_tanh(v.y));
      pin(t);
      uo[j] = t;
    };
    auto store_u = [&](int t) __attribute__((always_inline)) { st128_sc1(ws + W_U + (tile_of(t) * 288 + f16) * 1024, lane16, uo); };
#pragma unroll
    for (int n = 0; n < 5; ++n) rd(n);
    SB();
#pragma unroll
    for (int i = 0; i < 12; ++i)
#pragma unroll
      for (int k = 0; k < 16; ++k) {
        const int n = i * 16 + k, t = i >> 1;
        if (n + 5 < 192) rd(n + 5);
        cc[t & 1] = mfmaB(wf[n & 31], a[n % 6], (n & 31) ? cc[t & 1] : ci);
        SB();
        if (i == 0 && k < 4) issue_a(2, k);
        if (!(i & 1) && i > 0 && k == 2) exch_p(t - 1);
        if ((i & 1) && i > 1) {
          if (i < 9) { if (k == 1 || k == 4) send(t - 1, k >> 2); }
          else { if (k < 8 && !(k & 1)) act(k >> 1); if (k == 7) store_u(t - 1); }
          if (i >= 5 && i < 11 && k == 6) load_pv(t + 1);
        }
        if (k == PRIO_K1) PRIO(0);
        if (k == 8) {
          constexpr int vm[12] = {4, 4, 4, 6, 6, 8, 8, 8, 8, 9, 4, 63};
          if (!(i & 1) && i > 0) { lds_w128(exw, rx); asm volatile("s_waitcnt lgkmcnt(0)" ::: "memory"); }
          asm volatile("s_waitcnt vmcnt(%0)\n\ts_barrier" ::"n"(vm[i]) : "memory");
          PRIO(1);
          if (i == 10) flag_u(3);
          if (!(i & 1) && i > 0) rx = lds_r128(exr);
        }
        if (k >= 9 && k < 13) {
          if (i + 3 < 12) issue_a(i + 3, k - 9);
          else if (k & 1) issue_w2(2 * (i - 9) + 1 + ((k - 9) >> 1), ((i + 2) & 3) * 2 + ((k - 9) >> 1));
        }
        if (!(i & 1) && i > 0 && k == 14) combine(t - 1);
        if ((i & 1) && i >= 5 && i < 11 && k == 14) { if (i == 9) waitv<5>(); else waitv<4>(); gather(t + 1); }
        if (i == 8 && k == 13) issue_w2(0, 8);
        SB();
      }
    PRIO(0);
    STAMP(14); FS(57);
    GRAB2_DEF;
    exch_p(5);
    lds_w128(exw, rx);
    bar_v<4>();
    rx = lds_r128(exr);
    flag_u(4);
    issue_w2(7, 4); issue_w2(8, 5);
    combine(5);
#pragma unroll
    for (int j = 0; j < 4; ++j) act(j);
    store_u(5);
    grab(0, 8); grab(1, 6); grab(2, 7); grab(3, 0); grab(4, 1);
    bar_v<5>();
    {
      constexpr u32 un[6] = {8, 6, 7, 0, 1, 9};
#pragma unroll
      for (int ps = 9; ps < 15; ++ps) issue_w2(ps, un[ps - 9]);
    }
    grab(5, 2); grab(6, 3);
    bar_v<12>();
    flag_u(5);
  } else {
    if (w == 0) { poll_flags(ws + W_FLAGD, lane * 128, tag); __builtin_amdgcn_s_sleep(16); }
    bar();
    {
      const u32 part = (idx - 27) * 8 + w;
      const char* pb = P.w2 + (size_t)kq * 1152 * 4096 + kp * 2048;
      u32 d[8];
#pragma unroll
      for (int j = 0; j < 8; ++j)
        asm volatile("global_load_dword %0, %1, %2" POL_S : "=v"(d[j]) : "v"(min((j * 40 + part) * 4 + g, 1151u) * 4096 + l15 * 128), "s"(pb) : "memory");
      asm volatile("s_waitcnt vmcnt(0)" : "+v"(d[0]), "+v"(d[1]), "+v"(d[2]), "+v"(d[3]), "+v"(d[4]), "+v"(d[5]), "+v"(d[6]), "+v"(d[7])::"memory");
    }
    GRAB2_DEF;
    constexpr u32 un0[9] = {8, 6, 7, 0, 1, 2, 3, 4, 5};
    bar();
    if (w == 0) st_flag(ws + W_FLAGH + (rb * 8 + cg) * 128, tag);
#pragma unroll
    for (int ps = 0; ps < 9; ++ps) issue_w2(ps, un0[ps]);
    bar_v<0>();
    grab(0, 8); grab(1, 6); grab(2, 7); grab(3, 0); grab(4, 1);
    bar();
    {
      constexpr u32 un[6] = {8, 6, 7, 0, 1, 9};
#pragma unroll
      for (int ps = 9; ps < 15; ++ps) issue_w2(ps, un[ps - 9]);
    }
    grab(5, 2); grab(6, 3);
    bar();
  }
  STAMP(15); FS(58);

  {
    const u32 T0 = 9 * (Mq >> 1), TF = T0 + 5 * odd;
    auto owner_of = [&](int b) __attribute__((always_inline)) -> u32 { return b == 4 ? kq : (u32)(b - 1) + ((u32)(b - 1) >= kq ? 1u : 0u); };
    GRAB2_DEF;
    issue_w2(15, 2); issue_w2(16, 3);
    grab(7, 4); grab(8, 5);
    u32 fo;
    {
      const u32 b = min(lane / 9, 4u);
      fo = lane < 60 ? ((T0 + 4 * odd + b) * 36 + kq * 9 + lane % 9) * 128 : (W_FLAGH - W_FLAGE) + ((Nq * 2 + (lane & 1)) * 8 + Mq * 2 + ((lane >> 1) & 1)) * 128;
      if (lane == 58 || lane == 59) fo = (W_FLAGH - W_FLAGE) + ((Nq * 2 + (lane & 1)) * 8 + (Mq >> 1) * 4 + 2 - odd) * 128;
    }
    bar_v<4>();
    issue_w2(17, 4);
    {
      constexpr u32 un[6] = {8, 6, 7, 0, 1, 9};
#pragma unroll
      for (int ps = 9; ps < 15; ++ps) grab(ps, un[ps - 9]);
    }
    if (w == 0) poll_flags(ws + W_FLAGE, fo, tag);
    bar_v<0>();
    const char* ub = ws + W_U + kq * (72 * 1024);
    u32 lx = (16 + (w & 1) + 18 * ((w >> 1) & 1)) * 1024;
    asm volatile("" : "+s"(lx));
    auto issue_u = [&](int i, int j) __attribute__((always_inline)) {
      const u32 T = i == 0 ? T0 + 4 : TF + owner_of((i + 1) >> 1);
      const u32 part = (i == 0 ? 0u : (u32)((i + 1) & 1)) ^ odd;
      const char* b = ub + (T * 288 + part * 18) * 1024;
      if (j < 4) dma<64, true>(b + w1k + ((j >> 1) * 36 + (j & 1) * 8) * 1024, lane16, w1k, ((i + 3) & 3) * 36864 + ((j >> 1) * 18 + (j & 1) * 8) * 1024);
      else dma<64, true>(b + lx + (w & 2) * 9216, lane16, lx, ((i + 3) & 3) * 36864);
    };
#pragma unroll
    for (int j = 0; j < 5; ++j) issue_u(0, j);
    grab(15, 2); grab(16, 3); grab(17, 4);
    bar();
    STAMP(16); FS(59);
#pragma unroll
    for (int j = 0; j < 5; ++j) issue_u(1, j);
    const u32 grp = Mq * 16 + Nq, me = odd * 4 + kq;
    u32 exw = 147456 + w * 1024 + lane16, exr = 147456 + (w ^ 4) * 1024 + lane16;
    pin(exw); pin(exr);
    const LAddr ar(kh * 18432 + (lane16 ^ (kh * 256)));
    f32x16 cc[2];
    f32x4 kv0, kv1;
    u32x4 a[6], rx, pv[6];
    u32x2 hres[2]; u32 bb;
    char* ysb = ws + W_YS + (((Mq >> 1) * 16 + Nq) * 8 + w) * 16384;
    auto rd = [&](int n) __attribute__((always_inline)) { a[n % 6] = lds_r128(ar(((n / 18 + 3) & 3) * 36864 + (n % 18) * 1024)); };
    auto exch_p = [&](int b) __attribute__((always_inline)) {
      const u32x2 p2 = pack4(quad<2>(cc[b & 1])), p3 = pack4(quad<3>(cc[b & 1]));
      rx = u32x4{p2.x, p2.y, p3.x, p3.y};
      pin(rx);
    };
    auto send = [&](int b, int h) __attribute__((always_inline)) {
      const u32x2 pk = pack4(h ? quad<1>(cc[b & 1]) + unpk(rx.z, rx.w) : quad<0>(cc[b & 1]) + unpk(rx.x, rx.y));
      const u32 o = owner_of(b ? b : 1);
      char* pb = b == 0 ? ysb + me * 2048 : ws + W_YP + (((grp * 4 + o) * 3 + ((kq - o - 1) & 3u)) * 8 + w) * 2048;
      st128_sc1(pb + h * 1024, lane16, u32x4{pk.x, tag, pk.y, tag});
    };
    auto load_res = [&](u32 T, u32 ln) __attribute__((always_inline)) -> u32 {
      const u32 srow = Nq * 128 + w3 * 32 + (ln & 31), oo = srow * 1152 + T * 64 + kh * 32 + (ln >> 5) * 8;
      ld64_sc1(hres[0], ws + W_H, oo);
      ld64_sc1(hres[1], ws + W_H + 16, oo);
      ld16(bb, P.b2, srow * 2);
      return oo;
    };
    u32 so, oo;
    auto load_ps = [&]() __attribute__((always_inline)) { ld128_sc1(pv[0], ysb, so); ld128_sc1(pv[1], ysb, so, 1024); };
    auto load_pv1 = [&](int j) __attribute__((always_inline)) {
      ld128_sc1(pv[j], ws + W_YP + ((grp * 4 + kq) * 3 * 8 + w) * 2048 + (j >> 1) * 16384, lane16, (j & 1) * 1024);
    };
    auto load_pv = [&]() __attribute__((always_inline)) {
#pragma unroll
      for (int j = 0; j < 6; ++j) load_pv1(j);
    };
    bar_vo<5>();
#pragma unroll
    for (int n = 0; n < 5; ++n) rd(n);
    SB();
#pragma unroll
    for (int i = 0; i < 9; ++i)
#pragma unroll
      for (int k = 0; k < 18; ++k) {
        const int n = i * 18 + k, b = (i + 1) >> 1, x = (i == 0 || (i & 1)) ? k : 18 + k;
        if (n + 5 < 162) rd(n + 5);
        cc[b & 1] = mfmaB(a[n % 6], wf[x], x ? cc[b & 1] : zero16);
        SB();
        if (i == 0 && k < 5) issue_u(2, k);
        if ((i & 1) && k == 2) exch_p(b - 1);
        if (k == PRIO_K2) PRIO(0);
        if (k == 5) {
          constexpr int vm[9] = {5, 5, 7, 5, 7, 10, 7, 4, 63};
          if (i & 1) { lds_w128(exw, rx); asm volatile("s_waitcnt lgkmcnt(0)" ::: "memory"); }
          asm volatile("s_waitcnt vmcnt(%0)\n\ts_barrier" ::"n"(vm[i]) : "memory");
          PRIO(1);
          if (i & 1) rx = lds_r128(exr);
        }
        if ((i & 1) && (k == 8 || k == 10)) send(b - 1, (k - 8) >> 1);
        if (k >= 11 && k < 16 && i + 3 < 9) issue_u(i + 3, k - 11);
        if (i == 4 && k == 6) { so = (lane >> 3) * 2048 + (me * 8 + (lane & 7)) * 16; load_ps(); }
        if (i == 4 && k == 8) oo = load_res(T0 + 4, me * 8 + (lane & 7));
        if (i == 6) {
          if (k == 0) {
            waitv<12>();
            for (u32 spins = 0; spins < SPIN_CAP; ++spins) {
              pin(pv[0]); pin(pv[1]);
              u32 bad = (pv[0].y ^ tag) | (pv[0].w ^ tag) | (pv[1].y ^ tag) | (pv[1].w ^ tag);
              pin(bad);
              if (__builtin_expect(__all(bad == 0), 1)) break;
              load_ps();
              waitv<0>();
            }
            pin(pv[0]); pin(pv[1]);
            kv0 = unpk(pv[0].x, pv[0].z); kv1 = unpk(pv[1].x, pv[1].z);
            pin(kv0); pin(kv1);
          }
          if (k >= 1 && k < 5) { kv0[k - 1] = sum_g(kv0[k - 1] + dpp<0x128>(kv0[k - 1])); pin(kv0); }
          if (k >= 6 && k < 10) { kv1[k - 6] = sum_g(kv1[k - 6] + dpp<0x128>(kv1[k - 6])); pin(kv1); }
          if (k == 11) {
            pin(hres[0]); pin(hres[1]); pin(bb);
            const u32x2 o0 = pack4(kv0 + unpk(hres[0].x, hres[0].y) + bflo(bb)), o1 = pack4(kv1 + unpk(hres[1].x, hres[1].y) + bflo(bb));
            const bool hi = (lane & 8) != 0;
            asm volatile("s_mov_b64 exec, 0xffff\n\tglobal_store_dwordx2 %0, %1, %2\n\ts_mov_b64 exec, -1" ::"v"(oo + (lane & 8) * 2), "v"(hi ? o1 : o0), "s"(P.out) : "memory");
          }
          if (k == 14) oo = load_res(TF + kq, lane);
        }
        if (i == 8 && k == 2) {
          waitv<2>();
          pin(hres[0]); pin(hres[1]); pin(bb);
          kv0 = unpk(hres[0].x, hres[0].y) + bflo(bb); kv1 = unpk(hres[1].x, hres[1].y) + bflo(bb);
          pin(kv0); pin(kv1);
        }
        if (i == 8 && k >= 8 && k < 14) load_pv1(k - 8);
        SB();
      }
    PRIO(0);
    exch_p(4);
    lds_w128(exw, rx);
    bar();
    rx = lds_r128(exr);
    STAMP(17); FS(60);
    for (u32 spins = 0; spins < SPIN_CAP; ++spins) {
      waitv<0>();
      u32 bad = 0;
#pragma unroll
      for (int j = 0; j < 6; ++j) { pin(pv[j]); bad |= (pv[j].y ^ tag) | (pv[j].w ^ tag); }
      pin(bad);
      if (__all(bad == 0)) break;
      load_pv();
    }
    waitv<0>();
#pragma unroll
    for (int j = 0; j < 6; ++j) pin(pv[j]);
#pragma unroll
    for (int s = 0; s < 3; ++s) { kv0 += unpk(pv[2 * s].x, pv[2 * s].z); kv1 += unpk(pv[2 * s + 1].x, pv[2 * s + 1].z); }
    const u32x2 o0 = pack4(kv0 + quad<0>(cc[0]) + unpk(rx.x, rx.y)), o1 = pack4(kv1 + quad<1>(cc[0]) + unpk(rx.z, rx.w));
    auto s0 = __builtin_amdgcn_permlane32_swap(o0.x, o1.x, false, false);
    auto s1 = __builtin_amdgcn_permlane32_swap(o0.y, o1.y, false, false);
    asm volatile("global_store_dwordx4 %0, %1, %2\n\ts_nop 1" ::"v"(oo + (lane >> 5) * 8), "v"(u32x4{s0[0], s1[0], s0[1], s1[1]}), "s"(P.out) : "memory");
  }
  asm volatile(".Lfk_code_end:");
  if (bid == 0 && w == 0) st_flag(ws + W_EPOCH, tag);
}
}

extern "C" void kernel_launch(void* const* d_in, const int* in_sizes, int n_in, void* d_out, int out_size, void* d_ws,
                              size_t ws_size, hipStream_t stream) {
  Params p;
  p.x = (const char*)d_in[0]; p.wqkv = (const char*)d_in[1]; p.wphi = (const char*)d_in[2]; p.wo = (const char*)d_in[3];
  p.w1 = (const char*)d_in[5]; p.b1 = (const char*)d_in[6]; p.w2 = (const char*)d_in[7]; p.b2 = (const char*)d_in[8];
  p.out = (char*)d_out; p.ws = (char*)d_ws;
  (void)hipFuncSetAttribute((const void*)fused_kernel, hipFuncAttributeMaxDynamicSharedMemorySize, LDS_BYTES);
  fused_kernel<<<NWG, NT, LDS_BYTES, stream>>>(p);
}
```

```cpp
#include <hip/hip_runtime.h>
#include <hip/hip_bf16.h>

typedef unsigned u32;
typedef unsigned long long u64;
typedef u32 u32x2 __attribute__((ext_vector_type(2)));
typedef u32 u32x4 __attribute__((ext_vector_type(4)));
typedef float f32x2 __attribute__((ext_vector_type(2)));
typedef float f32x4 __attribute__((ext_vector_type(4)));
typedef float f32x16 __attribute__((ext_vector_type(16)));
typedef short s16x4 __attribute__((ext_vector_type(4)));
typedef __bf16 bf16x2 __attribute__((ext_vector_type(2)));
typedef __bf16 bf16x8 __attribute__((ext_vector_type(8)));
#define LAS __attribute__((address_space(3)))
#define DEVINL __device__ __forceinline__

namespace {
constexpr int S = 2048, D = 576, F = 4608;
constexpr int NWG = 256, NT = 512;
constexpr int LDS_BYTES = 163840;
constexpr u32 SPIN_CAP = 1u << 16;

constexpr u32 W_EPOCH = 0;
constexpr u32 W_FLAGB = 4096;
constexpr u32 W_FLAGD = W_FLAGB + 1024 * 128;
constexpr u32 W_FLAGE = W_FLAGD + 256 * 128;
constexpr u32 W_FLAGH = W_FLAGE + 18 * 36 * 128;
constexpr u32 W_KVFIN = W_FLAGH + 256 * 128;
constexpr u32 W_STATS = W_KVFIN + 8 * 96 * 128;
constexpr u32 W_KVPART = W_STATS + 32 * 4 * 8 * 256;
constexpr u32 W_O = W_KVPART + 8 * 96 * 32 * 128;
constexpr u32 W_H = W_O + 128 * 18 * 1024;
constexpr u32 W_HN = W_H + S * D * 2;
constexpr u32 W_U = W_HN + 18 * 128 * 1024;
constexpr u32 W_UP = W_U + 18 * 288 * 1024;
constexpr u32 W_YP = W_UP + 18 * 288 * 2048;
constexpr u32 W_YS = W_YP + 64 * 4 * 3 * 8 * 2048;
constexpr u32 W_END = W_YS + 32 * 8 * 8 * 2048;
constexpr u32 W_DBG = W_END;

constexpr u32 SLOT = 27648;
constexpr u32 L_XN = 3 * SLOT;
constexpr u32 L_PHI = 4 * SLOT;
constexpr u32 L_V = L_PHI + 64 * 328;
constexpr u32 L_KV = 147456;
constexpr u32 L_MISC = L_XN + 73728;
constexpr u32 L_WPHI = L_MISC;
constexpr u32 L_ZERO = L_MISC + 5760;
constexpr u32 L_EX = 131072;
constexpr u32 L_HI = L_EX + 20480;

struct Params {
  const char* x; const char* wqkv; const char* wphi; const char* wo;
  const char* w1; const char* b1; const char* w2; const char* b2;
  char* out; char* ws;
};

DEVINL f32x4 mfma32(u32x4 a, u32x4 b, f32x4 c) {
  return __builtin_amdgcn_mfma_f32_16x16x32_bf16(__builtin_bit_cast(bf16x8, a), __builtin_bit_cast(bf16x8, b), c, 0, 0, 0);
}
DEVINL f32x16 mfmaB(u32x4 a, u32x4 b, f32x16 c) {
  return __builtin_amdgcn_mfma_f32_32x32x16_bf16(__builtin_bit_cast(bf16x8, a), __builtin_bit_cast(bf16x8, b), c, 0, 0, 0);
}
template <int Q> DEVINL f32x4 quad(const f32x16& v) { return f32x4{v[4 * Q], v[4 * Q + 1], v[4 * Q + 2], v[4 * Q + 3]}; }
DEVINL f32x4 mfma16(u32x2 a, u32x2 b, f32x4 c) {
  return mfma32(u32x4{a.x, a.y, 0u, 0u}, u32x4{b.x, b.y, 0u, 0u}, c);
}
DEVINL u32 pack2(float lo, float hi) {
  f32x2 p = {lo, hi};
  return __builtin_bit_cast(u32, __builtin_convertvector(p, bf16x2));
}
DEVINL u32x2 pack4(f32x4 v) { return u32x2{pack2(v.x, v.y), pack2(v.z, v.w)}; }
DEVINL float bflo(u32 v) { return __builtin_bit_cast(float, v << 16); }
DEVINL float bfhi(u32 v) { return __builtin_bit_cast(float, v & 0xffff0000u); }
DEVINL float asf(u32 v) { return __builtin_bit_cast(float, v); }
DEVINL u32 asu(float v) { return __builtin_bit_cast(u32, v); }
DEVINL f32x4 unpk(u32 a, u32 b) { return f32x4{bflo(a), bfhi(a), bflo(b), bfhi(b)}; }
DEVINL float dot2(u32 a, u32 b, float c) {
  return __builtin_amdgcn_fdot2_f32_bf16(__builtin_bit_cast(bf16x2, a), __builtin_bit_cast(bf16x2, b), c, false);
}
DEVINL u32x2 lds_tr(u32 addr) {
  return __builtin_bit_cast(u32x2, __builtin_amdgcn_ds_read_tr16_b64_v4i16((LAS s16x4*)addr));
}
DEVINL u32x4 lds_tr2(u32 a0, u32 a1) {
  u32x2 lo = lds_tr(a0), hi = lds_tr(a1);
  return u32x4{lo.x, lo.y, hi.x, hi.y};
}
DEVINL u32x4 lds_r128(u32 addr) { return *(LAS u32x4*)addr; }
DEVINL u32x2 lds_r64(u32 addr) { return *(LAS u32x2*)addr; }
DEVINL void lds_w128(u32 addr, u32x4 v) { *(LAS u32x4*)addr = v; }
DEVINL void lds_w64(u32 addr, u32x2 v) { *(LAS u32x2*)addr = v; }
template <int CTRL> DEVINL float dpp(float v) { return asf(__builtin_amdgcn_update_dpp(0u, asu(v), CTRL, 0xf, 0xf, true)); }
DEVINL float sum8(float v) { v += dpp<0xB1>(v); v += dpp<0x4E>(v); return v + dpp<0x141>(v); }
DEVINL float sum_g(float v) {
  auto a = __builtin_amdgcn_permlane16_swap(asu(v), asu(v), false, false);
  v = asf(a[0]) + asf(a[1]);
  auto b = __builtin_amdgcn_permlane32_swap(asu(v), asu(v), false, false);
  return asf(b[0]) + asf(b[1]);
}
#define SB() __builtin_amdgcn_sched_barrier(0)
#define PRIO_K1 2
#define PRIO_K2 2
#define PRIO(v)
DEVINL u32 fresh_lane() { u32 l; asm volatile("v_mbcnt_lo_u32_b32 %0, -1, 0\n\tv_mbcnt_hi_u32_b32 %0, -1, %0" : "=v"(l)); __builtin_assume(l < 64); return l; }
#define LANEVARS const u32 lane = fresh_lane(), l15 = lane & 15, g = lane >> 4, q4 = l15 >> 2, p4 = lane & 3; \
  const u32 trrow = (16 * (g >> 1) + 4 * (g & 1) + q4) * 288; (void)l15; (void)g; (void)q4; (void)p4; (void)trrow
struct LAddr {
  u32 b[3];
  DEVINL explicit LAddr(u32 a) { b[0] = a; b[1] = a + 65536u; b[2] = a + 131072u; asm volatile("" : "+v"(b[0]), "+v"(b[1]), "+v"(b[2])); }
  DEVINL u32 operator()(u32 c) const { return b[c >> 16] + (c & 0xffffu); }
};

#define POL_W ""
#define POL_T ""
#define POL_S ""
#define POL_X ""
template <int NL, bool SC1> DEVINL void dma(const char* base, u32 voff, u32 lbase, u32 loff) {
  if constexpr (NL == 64) {
    if constexpr (SC1) asm volatile("s_add_u32 m0, %2, %3\n\ts_nop 0\n\tglobal_load_lds_dwordx4 %0, %1 sc1" ::"v"(voff), "s"(base), "s"(lbase), "n"(loff) : "memory", "scc");
    else asm volatile("s_add_u32 m0, %2, %3\n\ts_nop 0\n\tglobal_load_lds_dwordx4 %0, %1" POL_W ::"v"(voff), "s"(base), "s"(lbase), "n"(loff) : "memory", "scc");
  } else {
    if constexpr (SC1) asm volatile("s_add_u32 m0, %2, %3\n\ts_mov_b32 exec_hi, %4\n\tglobal_load_lds_dwordx4 %0, %1 sc1\n\ts_mov_b32 exec_hi, -1" ::"v"(voff), "s"(base), "s"(lbase), "n"(loff), "n"((1u << (NL - 32)) - 1u) : "memory", "scc");
    else asm volatile("s_add_u32 m0, %2, %3\n\ts_mov_b32 exec_hi, %4\n\tglobal_load_lds_dwordx4 %0, %1" POL_X "\n\ts_mov_b32 exec_hi, -1" ::"v"(voff), "s"(base), "s"(lbase), "n"(loff), "n"((1u << (NL - 32)) - 1u) : "memory", "scc");
  }
}
DEVINL void st128_sc1(char* base, u32 voff, u32x4 v) {
  asm volatile("global_store_dwordx4 %0, %1, %2 sc1\n\ts_nop 1" ::"v"(voff), "v"(v), "s"(base) : "memory");
}
DEVINL void st128_sc1_m(char* base, u32 voff, u32x4 v, u64 mask) {
  asm volatile("s_mov_b64 exec, %3\n\tglobal_store_dwordx4 %0, %1, %2 sc1\n\ts_mov_b64 exec, -1\n\ts_nop 0" ::"v"(voff), "v"(v), "s"(base), "s"(mask) : "memory");
}
DEVINL void st64_sc1(char* base, u32 voff, u32x2 v) {
  asm volatile("global_store_dwordx2 %0, %1, %2 sc1" ::"v"(voff), "v"(v), "s"(base) : "memory");
}
DEVINL void st64(char* base, u32 voff, u32x2 v) {
  asm volatile("global_store_dwordx2 %0, %1, %2" ::"v"(voff), "v"(v), "s"(base) : "memory");
}
DEVINL void st32_sc1(char* base, u32 voff, u32 v) {
  asm volatile("global_store_dword %0, %1, %2 sc1" ::"v"(voff), "v"(v), "s"(base) : "memory");
}
DEVINL void st_flag(char* addr, u32 tag) {
  u32 z, t;
  asm volatile("v_mov_b32 %0, 0\n\tv_mov_b32 %1, %3\n\ts_mov_b64 exec, 1\n\tglobal_store_dword %0, %1, %2 sc1\n\ts_mov_b64 exec, -1" : "=&v"(z), "=&v"(t) : "s"(addr), "s"(tag) : "memory");
}
DEVINL void ld128(u32x4& d, const char* base, u32 voff, int imm) {
  asm volatile("global_load_dwordx4 %0, %1, %2 offset:%3" POL_X : "=v"(d) : "v"(voff), "s"(base), "n"(imm) : "memory");
}
DEVINL void ld128_sc1(u32x4& d, const char* base, u32 voff, int imm = 0) {
  asm volatile("global_load_dwordx4 %0, %1, %2 offset:%3 sc1" : "=v"(d) : "v"(voff), "s"(base), "n"(imm) : "memory");
}
DEVINL void ld64(u32x2& d, const char* base, u32 voff, int imm = 0) {
  asm volatile("global_load_dwordx2 %0, %1, %2 offset:%3" : "=v"(d) : "v"(voff), "s"(base), "n"(imm) : "memory");
}
DEVINL void ld32(u32& d, const char* base, u32 voff) {
  asm volatile("global_load_dword %0, %1, %2" POL_T : "=v"(d) : "v"(voff), "s"(base) : "memory");
}
DEVINL void touch(u32& d, const char* base, u32 voff) {
  asm volatile("global_load_dword %0, %1, %2" POL_T : "+v"(d) : "v"(voff), "s"(base) : "memory");
}
DEVINL void ld64_sc1(u32x2& d, const char* base, u32 voff) {
  asm volatile("global_load_dwordx2 %0, %1, %2 sc1" : "=v"(d) : "v"(voff), "s"(base) : "memory");
}
DEVINL void ld16(u32& d, const char* base, u32 voff) {
  asm volatile("global_load_ushort %0, %1, %2" : "=v"(d) : "v"(voff), "s"(base) : "memory");
}
template <int N> DEVINL void waitv() { asm volatile("s_waitcnt vmcnt(%0)" ::"n"(N) : "memory"); }
template <typename T> DEVINL void pin(T& v) { asm volatile("" : "+v"(v)); }
template <int N> DEVINL void bar_v() { asm volatile("s_waitcnt vmcnt(%0) lgkmcnt(0)\n\ts_barrier" ::"n"(N) : "memory"); }
template <int N> DEVINL void bar_vo() { asm volatile("s_waitcnt vmcnt(%0)\n\ts_barrier" ::"n"(N) : "memory"); }
DEVINL void bar() { asm volatile("s_waitcnt lgkmcnt(0)\n\ts_barrier" ::: "memory"); }

DEVINL void poll_flags(const char* base, u32 voff, u32 tag) {
  for (u32 spins = 0; spins < SPIN_CAP; ++spins) {
    u32 v;
    asm volatile("global_load_dword %0, %1, %2 sc1\n\ts_waitcnt vmcnt(0)" : "=v"(v) : "v"(voff), "s"(base) : "memory");
    if (__all(v == tag)) break;
  }
}

#define STAMP(i)

#define FS(i)
DEVINL void feature_map2(const u32x4 (&wa)[5], const u32x2 (&wb)[5], const f32x4 (&t0)[2], const f32x4 (&t1)[2], const f32x4 (&t2)[2], u32 (&ph)[2][9]) {
  float p[2][20];
#pragma unroll
  for (int m = 0; m < 2; ++m) {
    const u32x2 p0 = pack4(t0[m]), p1 = pack4(t1[m]), p2 = pack4(t2[m]);
    const u32x4 b01 = {p0.x, p0.y, p1.x, p1.y};
    f32x4 a[5];
#pragma unroll
    for (int t = 0; t < 5; ++t) a[t] = mfma32(wa[t], b01, f32x4{0.f, 0.f, 0.f, 0.f});
#pragma unroll
    for (int t = 0; t < 5; ++t) {
      a[t] = mfma16(wb[t], p2, a[t]);
      p[m][4 * t] = a[t].x; p[m][4 * t + 1] = a[t].y; p[m][4 * t + 2] = a[t].z; p[m][4 * t + 3] = a[t].w;
    }
  }
  const float L2E = 1.44269504088896341f;
#pragma unroll
  for (int m = 0; m < 2; ++m) {
    float mx = p[m][0];
#pragma unroll
    for (int i = 1; i < 18; ++i) mx = fmaxf(mx, p[m][i]);
    const float nm = -mx * L2E;
    f32x2 sum = {0.f, 0.f};
#pragma unroll
    for (int i = 0; i < 18; ++i) p[m][i] = __builtin_amdgcn_exp2f(__builtin_fmaf(p[m][i], L2E, nm));
#pragma unroll
    for (int i = 0; i < 9; ++i) sum += f32x2{p[m][2 * i], p[m][2 * i + 1]};
    const float inv = __builtin_amdgcn_rcpf(sum.x + sum.y);
#pragma unroll
    for (int i = 0; i < 9; ++i) ph[m][i] = pack2(p[m][2 * i] * inv, p[m][2 * i + 1] * inv);
  }
}

DEVINL float gelu_tanh(float x) {
  const float C = -2.f * 0.7978845608028654f * 1.44269504088896341f;
  float y = x * __builtin_fmaf(x * x, 0.044715f * C, C);
  float e = __builtin_amdgcn_exp2f(y);
  return x * __builtin_amdgcn_rcpf(1.f + e);
}

__global__ __launch_bounds__(NT, 2) void fused_kernel(Params P) {
  extern __shared__ __attribute__((aligned(16))) char smem[];
  STAMP(0); FS(0);
  const u32 w = __builtin_amdgcn_readfirstlane(threadIdx.x >> 6);
  const u32 bid = blockIdx.x, xcd = bid & 7, idx = bid >> 3;
  char* const ws = P.ws;
  u64 pc; u32 len;
  asm volatile("s_getpc_b64 %0\n.Lfk_code_begin:\n\ts_mov_b32 %1, .Lfk_code_end-.Lfk_code_begin\n\ts_max_u32 %1, %1, .Lfk_copy_end-.Lfk_code_begin" : "=s"(pc), "=s"(len)::"scc");

  const u32 cg = (xcd & 1) * 4 + (idx & 3), rb = (xcd >> 1) * 8 + (idx >> 2);
  const u32 rot = ((idx >> 2) % 3);
  const u32 w3 = w & 3, kh = w >> 2;
  u32 tag, tag0;
  asm volatile("s_load_dword %0, %1, 0x0" : "=s"(tag0) : "s"(ws) : "memory");
  u32x4 wm[9][5];
  f32x4 hc[5];
  auto grab_wo = [&](const LAddr& a0, const LAddr& a4, int part) __attribute__((always_inline)) {
    constexpr u32 pslot[3] = {0, 4, 1};
#pragma unroll
    for (int kk = 0; kk < 3; ++kk) {
      const u32 b = pslot[part] * SLOT + kk * 9216;
#pragma unroll
      for (int n = 0; n < 4; ++n) wm[part * 3 + kk][n] = lds_tr2(a0(b + 32 * n), a0(b + 32 * n + 2304));
      wm[part * 3 + kk][4] = lds_tr2(a4(b), a4(b + 2304));
    }
  };

  if (__builtin_expect(w < 4, 1)) {
    const u32 mp = w >> 1, hp = w & 1;
    u32x4 xf[2][18];
    {
    LANEVARS;
    u32x4 xr[2][9];
    {
      const u32 xo = (rb * 64 + w * 16 + (lane >> 3)) * 1152 + (lane & 7) * 16;
#pragma unroll
      for (int i = 0; i < 9; ++i) ld128(xr[0][i], P.x, xo, i * 128);
#pragma unroll
      for (int i = 0; i < 9; ++i) ld128(xr[1][i], P.x + 8 * 1152, xo, i * 128);
    }
    asm volatile("s_barrier" ::: "memory");
    FS(1);
    SB();
#pragma unroll
    for (int h = 0; h < 2; ++h) {
      if (h == 0) asm volatile("s_waitcnt vmcnt(9)" : "+v"(xr[0][0]), "+v"(xr[0][1]), "+v"(xr[0][2]), "+v"(xr[0][3]), "+v"(xr[0][4]), "+v"(xr[0][5]), "+v"(xr[0][6]), "+v"(xr[0][7]), "+v"(xr[0][8])::"memory");
      else asm volatile("s_waitcnt vmcnt(0)" : "+v"(xr[1][0]), "+v"(xr[1][1]), "+v"(xr[1][2]), "+v"(xr[1][3]), "+v"(xr[1][4]), "+v"(xr[1][5]), "+v"(xr[1][6]), "+v"(xr[1][7]), "+v"(xr[1][8])::"memory");
      if (h == 0) { STAMP(1); FS(2); } else FS(3);
      f32x2 c[36];
      f32x2 s1 = {0.f, 0.f}, s2 = {0.f, 0.f};
#pragma unroll
      for (int i = 0; i < 9; ++i)
#pragma unroll
        for (int j = 0; j < 4; ++j) {
          const f32x2 v = {bflo(xr[h][i][j]), bfhi(xr[h][i][j])};
          c[i * 4 + j] = v;
          s1 += v; s2 += v * v;
        }
      const float mean = sum8(s1.x + s1.y) * (1.f / 576.f);
      const float var = fmaxf(sum8(s2.x + s2.y) * (1.f / 576.f) - mean * mean, 0.f);
      const float rs = __builtin_amdgcn_rsqf(var + 1e-5f), nb = -mean * rs;
      const u32 a0 = L_XN + (w * 18 + ((lane & 7) >> 2)) * 1024 + (lane & 3) * 256 + (((8 * h + (lane >> 3)) ^ (lane & 7)) * 16);
#pragma unroll
      for (int i = 0; i < 9; ++i) {
        u32x4 o;
#pragma unroll
        for (int j = 0; j < 4; ++j) { const f32x2 v = c[i * 4 + j] * rs + nb; o[j] = pack2(v.x, v.y); }
        lds_w128(a0 + i * 2048, o);
      }
      SB();
    }
    FS(4);
    asm volatile("s_waitcnt lgkmcnt(0)\n\ts_barrier\n\ts_add_u32 %0, %1, 1" : "=s"(tag) : "s"(tag0) : "memory", "scc");
    FS(5);
    {
      u32 xa[2];
#pragma unroll
      for (int i = 0; i < 2; ++i) xa[i] = L_XN + mp * 36864 + g * 256 + ((l15 ^ (i * 4 + g)) * 16);
#pragma unroll
      for (int u = 0; u < 3; ++u) {
        u32 st = rot + u; if (st >= 3) st -= 3;
#pragma unroll
        for (int i = 0; i < 6; ++i) {
          xf[0][u * 6 + i] = lds_r128(xa[i & 1] + st * 6144 + i * 1024);
          xf[1][u * 6 + i] = lds_r128(xa[i & 1] + st * 6144 + i * 1024 + 18432);
        }
      }
    }
    }
    u32 ta0, ta1;
    { LANEVARS; ta0 = trrow + hp * 72 + p4 * 8; ta1 = trrow + hp * 72 + 64 + (p4 & 1) * 144; }
    const LAddr trA(ta0);
    FS(6);
    bar();
    STAMP(2); FS(7);
    f32x4 acc[5][2];
#pragma unroll
    for (int i = 0; i < 5; ++i) { acc[i][0] = f32x4{0.f, 0.f, 0.f, 0.f}; acc[i][1] = acc[i][0]; }
    u32x4 wa[5]; u32x2 wb[5];
    {
      const LAddr trM(ta1);
      u32x4 F[6];
      u32 ad[3];
      auto rd = [&](int u) __attribute__((always_inline)) {
        constexpr u32 toff[4] = {0, 32, 144, 176};
        const int n = u / 5, i = u % 5;
        const u32 b = ((n / 3) % 5) * SLOT + (n % 3) * 9216;
        F[u % 6] = i < 4 ? lds_tr2(trA(b + toff[i & 3]), trA(b + toff[i & 3] + 2304)) : lds_tr2(trM(b), trM(b + 2304));
      };
#pragma unroll
      for (int u = 0; u < 5; ++u) rd(u);
      SB();
#pragma unroll
      for (int n = 0; n < 18; ++n) {
        if (n == 14) {
          LANEVARS;
#pragma unroll
          for (int a = 0; a < 3; ++a) {
            const u32 ic = a < 2 ? 16 * a + 4 * g + q4 : 32 + q4;
            const bool ok = (ic >= 18 ? 1u : 0u) == (p4 >> 1) && (a < 2 || g == 0);
            ad[a] = ok ? L_WPHI + (p4 & 1) * 2880 + (hp * 36 + ic) * 40 : L_ZERO;
            pin(ad[a]);
          }
        }
        if (n == 4 || n == 8 || n == 11 || n == 14) { FS(8 + 2 * (n / 3 - 1)); bar(); FS(9 + 2 * (n / 3 - 1)); }
#pragma unroll
        for (int i = 0; i < 5; ++i) {
          const int u = n * 5 + i;
          if (u + 5 < 90) rd(u + 5);
          else { wa[i] = lds_tr2(ad[0] + 8 * i, ad[1] + 8 * i); wb[i] = lds_tr(ad[2] + 8 * i); }
          acc[i][0] = mfma32(F[u % 6], xf[0][n], acc[i][0]);
          acc[i][1] = mfma32(F[u % 6], xf[1][n], acc[i][1]);
          SB();
        }
      }
    }
    STAMP(3); FS(16);
    {
      LANEVARS;
      const u32 va = L_V + (mp * 32 + l15) * 152 + hp * 72 + g * 8;
#pragma unroll
      for (int m = 0; m < 2; ++m) {
        lds_w64(va + m * (16 * 152), pack4(acc[2][m]));
        lds_w64(va + m * (16 * 152) + 32, pack4(acc[3][m]));
        if (g == 1) lds_w64(va + m * (16 * 152) + 56, pack4(acc[4][m]));
      }
      u32 ph[2][9];
      feature_map2(wa, wb, acc[0], acc[1], acc[4], ph);
      const u32 pa = L_PHI + (mp * 32 + l15) * 328 + (hp * 4 + g) * 40;
#pragma unroll
      for (int m = 0; m < 2; ++m)
#pragma unroll
        for (int i = 0; i < 5; ++i) lds_w64(pa + m * (16 * 328) + 8 * i, u32x2{ph[m][2 * i], i < 4 ? ph[m][2 * i + 1] : 0u});
    }
    FS(17);
    bar();
    FS(18);
    bar();
    STAMP(4); FS(20);
#pragma unroll
    for (int i = 0; i < 3; ++i) { acc[i][0] = f32x4{0.f, 0.f, 0.f, 0.f}; acc[i][1] = acc[i][0]; }
    {
      u32 tq;
      { LANEVARS; tq = trrow + hp * 72 + 64; }
      const LAddr trQ(tq);
      u32x4 F[6];
      auto rd = [&](int u) __attribute__((always_inline)) {
        const int n = u / 3, i = u % 3;
        const u32 b = (1 + n / 6) * SLOT + (n % 3) * 9216 + ((n % 6) / 3) * 144;
        F[u % 6] = i < 2 ? lds_tr2(trA(b + 32 * i), trA(b + 32 * i + 2304)) : lds_tr2(trQ(b), trQ(b + 2304));
      };
#pragma unroll
      for (int u = 0; u < 5; ++u) rd(u);
      SB();
#pragma unroll
      for (int u = 0; u < 54; ++u) {
        if (u + 5 < 54) rd(u + 5);
        acc[u % 3][0] = mfma32(F[u % 6], xf[0][u / 3], acc[u % 3][0]);
        acc[u % 3][1] = mfma32(F[u % 6], xf[1][u / 3], acc[u % 3][1]);
        SB();
      }
    }
    STAMP(5); FS(21);
    u32 pq[2][9];
    feature_map2(wa, wb, acc[0], acc[1], acc[2], pq);
    LANEVARS;
    u32 oa[3];
    {
      const bool same = (q4 >> 1) == (g >> 1);
#pragma unroll
      for (int t = 0; t < 3; ++t) {
        const u32 slot = 4 * t + p4;
        const u32 cc = slot < 9 ? 9 * (q4 & 1) + slot : 18;
        oa[t] = same ? L_KV + ((hp * 2 + (g >> 1)) * 19 + cc) * 96 + (g & 1) * 48 : L_ZERO;
        pin(oa[t]);
      }
    }
    const LAddr wo0(trrow + p4 * 8), wo4(trrow + 128 + (p4 & 1) * 8);
#pragma unroll
    for (int m = 0; m < 2; ++m)
#pragma unroll
      for (int i = 0; i < 9; ++i) pin(pq[m][i]);
    STAMP(6); FS(22);
    bar();
    STAMP(7); FS(23);
    {
      u32x4 k0[3], k1[3]; u32x2 k2[3];
#pragma unroll
      for (int t = 2; t >= 0; --t) { k0[t] = lds_r128(oa[t]); k1[t] = lds_r128(oa[t] + 16); k2[t] = lds_r64(oa[t] + 32); }
      SB();
      f32x4 nu[2][3];
#pragma unroll
      for (int t = 2; t >= 0; --t)
#pragma unroll
        for (int m = 0; m < 2; ++m) {
          f32x4 c = mfma32(k0[t], u32x4{pq[m][0], pq[m][1], pq[m][2], pq[m][3]}, f32x4{0.f, 0.f, 0.f, 0.f});
          c = mfma32(k1[t], u32x4{pq[m][4], pq[m][5], pq[m][6], pq[m][7]}, c);
          nu[m][t] = mfma16(k2[t], u32x2{pq[m][8], 0u}, c);
        }
      char* ob = ws + W_O + (rb * 4 + mp * 2) * 18432;
#pragma unroll
      for (int m = 0; m < 2; ++m) {
        const float inv = __builtin_amdgcn_rcpf(nu[m][2].y + 1e-6f);
        const u32x2 a = pack4(nu[m][0] * inv), b = pack4(nu[m][1] * inv);
        st128_sc1(ob + m * 18432 + (cg * 2 + hp) * 1024, lane * 16, u32x4{a.x, a.y, b.x, b.y});
        const u32 e0 = asu(nu[m][2].x * inv);
        auto t16 = __builtin_amdgcn_permlane16_swap(e0, e0, false, false);
        const u32 e01 = pack2(asf(t16[0]), asf(t16[1]));
        auto t32 = __builtin_amdgcn_permlane32_swap(e01, e01, false, false);
        if (g == 0) st64_sc1(ob + m * 18432 + 16384 + cg * 256 + hp * 8, lane * 16, u32x2{t32[0], t32[1]});
      }
    }
    FS(24);
    SB();
    grab_wo(wo0, wo4, 0);
    waitv<0>();
    st_flag(ws + W_FLAGB + ((rb * 2 + mp) * 16 + cg * 2 + hp) * 128, tag);
    STAMP(8); FS(25);
    grab_wo(wo0, wo4, 1);
#pragma unroll
    for (int n = 0; n < 5; ++n) hc[n] = f32x4{0.f, 0.f, 0.f, 0.f};
    bar();
    grab_wo(wo0, wo4, 2);
  } else {
    LANEVARS;
    u32 pfd = 0;
    asm volatile("global_load_dword %0, %1, %2" : "+v"(pfd) : "v"(min(((idx * 4 + w3) * 4 + p4) * 128u, len)), "s"(pc) : "memory");
    const u32 r3 = lane / 18, c18 = lane % 18, seg = c18 / 9, ch9 = c18 % 9;
    u32 wrow[4], voff[4];
#pragma unroll
    for (int i = 0; i < 4; ++i) {
      const u32 r = 12 * w3 + 3 * i + r3;
      wrow[i] = (r & ~12u) | ((r & 4u) << 1) | ((r & 8u) >> 1);
      voff[i] = wrow[i] * 3456 + seg * 1152 + ch9 * 16;
    }
    const u32 wl = w3 * 3456;
    const char* wq = P.wqkv + cg * 144;
    auto issue1 = [&](int n, int j0, int j1) __attribute__((always_inline)) {
      constexpr u32 slot[9] = {0, 1, 2, 3, 4, 0, 1, 2, 3};
      u32 st = n < 6 ? 2 * rot + n : 2 * (rot + n - 6);
      if (st >= 6) st -= 6;
      const char* b = wq + (n < 6 ? 1152 : 0) + st * (96 * 3456);
#pragma unroll
      for (int j = j0; j < j1; ++j) dma<54, false>(b + (j >> 2) * (48 * 3456), voff[j & 3], wl, slot[n] * SLOT + (j >> 2) * 13824 + (j & 3) * 864);
    };
    asm volatile("s_barrier" ::: "memory");
    issue1(0, 0, 8);
    u32 wp[3];
    {
      const u32 so = (min(lane, 59u) / 10) * 36 + min(lane % 10, 8u) * 4;
#pragma unroll
      for (int j = 0; j < 3; ++j) ld32(wp[j], P.wphi + cg * 2592 + w3 * 648 + j * 216, so);
    }
    issue1(1, 0, 8);
    issue1(2, 0, 4);
    asm volatile("s_waitcnt vmcnt(12)" : "+v"(wp[0]), "+v"(wp[1]), "+v"(wp[2])::"memory");
    if (lane < 60) {
#pragma unroll
      for (int j = 0; j < 3; ++j) {
        *(LAS u32*)(L_WPHI + w3 * 720 + j * 240 + lane * 4) = wp[j];
        *(LAS u32*)(L_WPHI + 2880 + w3 * 720 + j * 240 + lane * 4) = wp[j] ^ 0x80008000u;
      }
    }
    if (lane < 16) *(LAS u32*)(L_ZERO + lane * 4) = 0u;
    const char* pw1 = P.w1 + (size_t)((xcd & 1) * 1024 + idx * 32 + w3 * 8) * 9216 + (xcd >> 1) * 2304;
    const u32 po = min(lane, 17u) * 128;
    FS(4);
    asm volatile("s_waitcnt lgkmcnt(0)\n\ts_barrier\n\ts_add_u32 %0, %1, 1" : "=s"(tag) : "s"(tag0) : "memory", "scc");
    issue1(2, 4, 8);
    FS(6);
    bar_vo<8>();
    issue1(3, 0, 8);
    issue1(4, 0, 4);
    FS(8);
    bar_vo<12>();
    issue1(4, 4, 8);
    issue1(5, 0, 8);
#pragma unroll
    for (int i = 0; i < 4; ++i) voff[i] += seg * (96 * 3456 - 1152);
    FS(10);
    bar_vo<16>();
    {
      const u32 ln = min((idx * 4 + w3) * 23 + min(lane, 22u), 2879u);
      touch(pfd, P.wo + (xcd & 1) * 512, (ln / 5) * 1152 + (ln % 5) * 128);
    }
    touch(pfd, pw1, po);
    FS(12);
    bar_vo<10>();
    issue1(6, 0, 4);
    FS(14);
    bar_vo<6>();
    issue1(6, 4, 8);
    touch(pfd, pw1 + 9216, po);
    issue1(7, 0, 8);
    touch(pfd, pw1 + 2 * 9216, po);
    issue1(8, 0, 8);
    touch(pfd, pw1 + 3 * 9216, po);
    const u32 srow = 8 * g + q4;
    u32 e_vb0 = L_V + srow * 152 + (w3 >> 1) * 72 + ((w3 & 1) ? 40 : 0) + p4 * 8, e_vb1 = L_V + srow * 152 + (w3 >> 1) * 72 + 32;
    u32 e_pb = L_PHI + srow * 328 + w3 * 80 + p4 * 8, e_pb2 = L_PHI + srow * 328 + w3 * 80 + 64 + (p4 & 1) * 8;
    u32 e_s0 = (lane >> 3) * 4096 + (lane & 7) * 16, e_s1[3];
    const u32 colp = l15 == 4 ? 2u : l15 - 2 * (w3 & 1);
#pragma unroll
    for (int i = 0; i < 3; ++i) {
      const u32 sidx = (160 + min(colp, 2u) * 10 + 4 * i + g);
      e_s1[i] = (sidx >> 3) * 4096 + (sidx & 7) * 16;
      pin(e_s1[i]);
    }
    const u64 e_m01 = __ballot(l15 == 4 || colp < 2), e_m2 = __ballot(l15 == 4 || (colp < 2 && g < 2));
    pin(e_vb0); pin(e_vb1); pin(e_pb); pin(e_pb2); pin(e_s0);
#pragma unroll
    for (int i = 0; i < 4; ++i) { voff[i] = (9 * (wrow[i] >> 3) + (wrow[i] & 7) + seg * 324) * 1152 + ch9 * 16; pin(voff[i]); }
    const char* wob = P.wo + cg * 144;
    FS(17);
    bar();
    FS(18);
    {
      u32x4 vf[2][2], pf[2][3];
#pragma unroll
      for (int ks = 0; ks < 2; ++ks) {
        vf[ks][0] = lds_tr2(e_vb0 + ks * (32 * 152), e_vb0 + ks * (32 * 152) + 4 * 152);
        pf[ks][0] = lds_tr2(e_pb + ks * (32 * 328), e_pb + ks * (32 * 328) + 4 * 328);
        pf[ks][1] = lds_tr2(e_pb + ks * (32 * 328) + 32, e_pb + ks * (32 * 328) + 32 + 4 * 328);
        pf[ks][2] = lds_tr2(e_pb2 + ks * (32 * 328), e_pb2 + ks * (32 * 328) + 4 * 328);
      }
#pragma unroll
      for (int ks = 0; ks < 2; ++ks) vf[ks][1] = lds_tr2(e_vb1 + ks * (32 * 152), e_vb1 + ks * (32 * 152) + 4 * 152);
      SB();
      f32x4 kv[2][3];
#pragma unroll
      for (int vt = 0; vt < 2; ++vt)
#pragma unroll
        for (int ks = 0; ks < 2; ++ks) {
          if (vt && l15 >= 4) vf[ks][1] = u32x4{0x3f803f80u, 0x3f803f80u, 0x3f803f80u, 0x3f803f80u};
#pragma unroll
          for (int i = 0; i < 3; ++i) kv[vt][i] = mfma32(pf[ks][i], vf[ks][vt], ks ? kv[vt][i] : f32x4{0.f, 0.f, 0.f, 0.f});
        }
      char* kb = ws + W_KVPART + ((cg * 96 + w3 * 24) * 32 + rb) * 128;
#pragma unroll
      for (int i = 0; i < 3; ++i) {
        const u32x2 pk = pack4(kv[0][i]);
        st128_sc1_m(kb + i * 32768, e_s0, u32x4{pk.x, tag, pk.y, tag}, i < 2 ? ~0ull : 0xffffffffull);
      }
#pragma unroll
      for (int i = 0; i < 3; ++i) {
        const u32x2 pk = pack4(kv[1][i]);
        st128_sc1_m(kb, e_s1[i], u32x4{pk.x, tag, pk.y, tag}, i < 2 ? e_m01 : e_m2);
      }
    }
    FS(19);
    bar_v<7>();
    touch(pfd, pw1 + 4 * 9216, po);
    touch(pfd, pw1 + 5 * 9216, po);
#pragma unroll
    for (int j = 0; j < 8; ++j) dma<54, false>(wob + (j >> 2) * (54 * 1152), voff[j & 3], wl, (j >> 2) * 13824 + (j & 3) * 864);
    FS(26);
    if (w3 < 3) {
      const char* pb = ws + W_KVPART + (cg * 96 + rb * 3 + w3) * 4096;
      f32x4 sum;
      for (u32 spins = 0; spins < SPIN_CAP; ++spins) {
        u32x4 v[4];
#pragma unroll
        for (int i = 0; i < 4; ++i) ld128_sc1(v[i], pb, lane * 16, i * 1024);
        waitv<0>();
        u32 bad = 0;
        sum = f32x4{0.f, 0.f, 0.f, 0.f};
#pragma unroll
        for (int i = 0; i < 4; ++i) {
          pin(v[i]);
          bad |= (v[i].y ^ tag) | (v[i].w ^ tag);
          sum += f32x4{bflo(v[i].x), bfhi(v[i].x), bflo(v[i].z), bfhi(v[i].z)};
        }
        pin(bad);
        if (__all(bad == 0)) break;
      }
#pragma unroll
      for (int j = 0; j < 4; ++j) sum[j] = sum_g(sum[j] + dpp<0x128>(sum[j]));
      const u32x2 pk = pack4(sum);
      if (lane < 8) st128_sc1(ws + W_KVFIN + (cg * 96 + rb * 3 + w3) * 128, lane * 16, u32x4{pk.x, tag, pk.y, tag});
    }
    FS(27);
    touch(pfd, pw1 + 6 * 9216, po);
    touch(pfd, pw1 + 7 * 9216, po);
#pragma unroll
    for (int j = 0; j < 8; ++j) dma<54, false>(wob + (108 + (j >> 2) * 54) * 1152, voff[j & 3], wl, 4 * SLOT + (j >> 2) * 13824 + (j & 3) * 864);
    u32x2 xres[5];
    {
      const u32 xo = (rb * 64 + w3 * 16 + l15) * 1152 + cg * 144;
#pragma unroll
      for (int n = 0; n < 4; ++n) ld64(xres[n], P.x, xo + g * 8, n * 32);
      ld64(xres[4], P.x, xo + (g & 1) * 8, 128);
    }
    u32 vo2[8];
    {
      u32 la[3];
#pragma unroll
      for (int a = 0; a < 3; ++a) {
        const u32 r = a * 64 + lane;
        const u32 rr = r - 160, cp = (rr * 205) >> 11;
        const u32 cc = r < 160 ? (r & 15) + 2 * (w3 & 1) : (cp >= 2 ? 18 : ((w3 & 1) ? cp : 16 + cp));
        const u32 piece = r < 160 ? (r >> 4) : rr - cp * 10;
        la[a] = r < 190 ? L_KV + (w3 * 19 + cc) * 96 + piece * 8 + (piece >= 5 ? 8 : 0) : L_KV + 40;
        pin(la[a]);
      }
#pragma unroll
      for (int j = 0; j < 8; ++j) {
        const u32 kap = 192 + (j >> 2) * 48 + wrow[j & 3] + seg * 288;
        vo2[j] = (kap < 512 ? 9 * (kap >> 3) + (kap & 7) : 9 * (kap - 512) + 8) * 1152 + ch9 * 16;
        pin(vo2[j]);
      }
      FS(28);
      const char* fb = ws + W_KVFIN + (cg * 4 + w3) * 3072;
      u32x4 v[3];
      for (u32 spins = 0; spins < SPIN_CAP; ++spins) {
#pragma unroll
        for (int i = 0; i < 3; ++i) ld128_sc1(v[i], fb, lane * 16, i * 1024);
        waitv<0>();
        u32 bad = 0;
#pragma unroll
        for (int i = 0; i < 3; ++i) { pin(v[i]); bad |= (v[i].y ^ tag) | (v[i].w ^ tag); }
        pin(bad);
        if (__all(bad == 0)) break;
      }
#pragma unroll
      for (int i = 0; i < 3; ++i) lds_w64(la[i], u32x2{v[i].x, v[i].z});
    }
    pin(pfd);
    FS(22);
    bar();
#pragma unroll
    for (int j = 0; j < 8; ++j) dma<54, false>(wob, vo2[j], wl, SLOT + (j >> 2) * 13824 + (j & 3) * 864);
    const LAddr wo0(trrow + 144 + p4 * 8), wo4(trrow + 144 + 128 + (p4 & 1) * 8);
    grab_wo(wo0, wo4, 0);
    grab_wo(wo0, wo4, 1);
#pragma unroll
    for (int n = 0; n < 5; ++n) {
      pin(xres[n]);
      hc[n] = f32x4{bflo(xres[n].x), bfhi(xres[n].x), bflo(xres[n].y), bfhi(xres[n].y)};
    }
    FS(25);
    bar_v<0>();
    asm volatile(".Lfk_copy_end:");
    grab_wo(wo0, wo4, 2);
  }
  LANEVARS;
  const u32 lane16 = lane * 16;
  if (w >= 4) __builtin_amdgcn_s_setprio(1);
  FS(29);
  poll_flags(ws + W_FLAGB + (rb * 2 + (w3 >> 1)) * 2048, l15 * 128, tag);
  STAMP(9); FS(30);
  {
    u32x4 of[9];
    const char* ob = ws + W_O + ((rb * 4 + w3) * 18 + kh * 9 + 4) * 1024;
#pragma unroll
    for (int j = 0; j < 8; ++j) ld128_sc1(of[j], ob, lane16, (j - 4) * 1024);
    ld128_sc1(of[8], ob + 4096, lane16);
    FS(31);
#pragma unroll
    for (int j = 0; j < 9; ++j) {
      asm volatile("s_waitcnt vmcnt(%1)" : "+v"(of[j]) : "n"(8 - j) : "memory");
#pragma unroll
      for (int n = 0; n < 5; ++n) hc[n] = mfma32(wm[j][n], of[j], hc[n]);
      SB();
    }
  }
  STAMP(10); FS(32);
  if (w >= 4) {
#pragma unroll
    for (int n = 0; n < 5; ++n) lds_w128(L_EX + (w3 * 5 + n) * 1024 + lane16, __builtin_bit_cast(u32x4, hc[n]));
    __builtin_amdgcn_s_setprio(0);
  }
  bar();
  FS(33);
  const u32 kq = xcd >> 1, kp = xcd & 1;
  const bool g1 = idx < 27;
  const u32 ni = kq * 9 + (g1 ? idx / 3 : 0), mi = idx % 3;
  const char* w1b = P.w1 + (size_t)(kp * 1024 + w3 * 4) * 9216 + ni * 256;
  const u32 wcol = (((q4 ^ g) << 2) | p4) * 16;
  const u32 w1off = g * 9216 + wcol;
  auto issue_w1 = [&](int ps, u32 unit) __attribute__((always_inline)) {
#pragma unroll
    for (int j = 0; j < 4; ++j)
      dma<64, false>(w1b + (size_t)(((j >> 1) * 16 + ps) * 32 + (j & 1) * 16) * 9216, w1off, w3 * 1024, unit * 16384 + j * 4096);
  };
  u32x4 wf[36];
  if (w < 4) {
    float s1 = 0.f, s2 = 0.f;
    u32x4 rx[5];
#pragma unroll
    for (int n = 0; n < 5; ++n) rx[n] = lds_r128(L_EX + (w3 * 5 + n) * 1024 + lane16);
    SB();
    u32x2 hr[5];
#pragma unroll
    for (int n = 0; n < 5; ++n) {
      hc[n] += __builtin_bit_cast(f32x4, rx[n]);
      hr[n] = pack4(hc[n]);
      if (n == 4 && g >= 2) hr[n] = u32x2{0u, 0u};
      s1 = dot2(hr[n].x, 0x3f803f80u, s1); s1 = dot2(hr[n].y, 0x3f803f80u, s1);
      s2 = dot2(hr[n].x, hr[n].x, s2); s2 = dot2(hr[n].y, hr[n].y, s2);
    }
    s1 = sum_g(s1); s2 = sum_g(s2);
    char* sb = ws + W_STATS + (rb * 4 + w) * 2048;
    st128_sc1_m(sb + cg * 256, lane16, u32x4{asu(s1), tag, asu(s2), tag}, 0xffffull);
    FS(34);
    u32 hw = l15 * 160 + g * 8;
#pragma unroll
    for (int n = 0; n < 5; ++n) {
      lds_w64(L_HI + w * 2560 + hw + n * 32, hr[n]);
      hc[n] = f32x4{bflo(hr[n].x), bfhi(hr[n].x), bflo(hr[n].y), bfhi(hr[n].y)};
      pin(hc[n]);
    }
    hw += L_EX + w * 5120;
    u32 hrd = L_EX + w * 5120 + (8 * (g & 1) + q4) * 160 + ((g >> 1) * 16 + p4 * 4) * 2;
    u32 hrx = L_EX + w * 5120 + (8 * (g & 1) + q4) * 160 + (64 + (p4 & 1) * 4) * 2;
    u32 ho[2];
#pragma unroll
    for (int j = 0; j < 2; ++j) {
      const u32 d = cg * 72 + (j ? 64 + (l15 & 7) : (g >> 1) * 16 + l15);
      ho[j] = (d >> 5) * 131072 + (g & 1) * 512 + (d & 31) * 16;
      pin(ho[j]);
    }
    pin(hw); pin(hrd); pin(hrx);
    char* hb = ws + W_HN + (rb * 4 + w) * 1024;
    u32x4 v0, v1;
    FS(35);
    for (u32 spins = 0; spins < SPIN_CAP; ++spins) {
      ld128_sc1(v0, sb, lane16);
      ld128_sc1(v1, sb, lane16, 1024);
      waitv<0>();
      pin(v0); pin(v1);
      u32 bad = (v0.y ^ tag) | (v0.w ^ tag) | (v1.y ^ tag) | (v1.w ^ tag);
      pin(bad);
      if (__all(bad == 0)) break;
    }
    FS(36);
    s1 = sum_g(asf(v0.x) + asf(v1.x));
    s2 = sum_g(asf(v0.z) + asf(v1.z));
    const float mean = s1 * (1.f / 576.f);
    const float var = fmaxf(s2 * (1.f / 576.f) - mean * mean, 0.f);
    const float r1 = __builtin_amdgcn_rsqf(var + 1e-5f);
    const float r2 = r1 * __builtin_amdgcn_rsqf(var * r1 * r1 + 1e-5f);
    const float nb = -mean * r2;
#pragma unroll
    for (int n = 0; n < 5; ++n) lds_w64(hw + n * 32, pack4(hc[n] * r2 + nb));
    const u32x4 f0 = lds_tr2(hrd, hrd + 4 * 160), f1 = lds_tr2(hrd + 64, hrd + 64 + 4 * 160), f2 = lds_tr2(hrx, hrx + 4 * 160);
    STAMP(11); FS(37);
    st128_sc1(hb, ho[0], f0);
    st128_sc1(hb + 131072, ho[0], f1);
    st128_sc1_m(hb, ho[1], f2, 0x00ff00ffull);
    FS(38);
    asm volatile("s_barrier" ::: "memory");
  } else {
#pragma unroll
    for (int ps = 0; ps < 8; ++ps) issue_w1(ps, ps);
    FS(38);
    bar_vo<0>();
  }
  const LAddr t1w(kh * 8192 + (8 * (g >> 1) + q4) * 256 + (w3 ^ q4) * 64 + ((g & 1) ^ kh) * 32 + p4 * 8);
  auto grab1 = [&](int ps, u32 unit) __attribute__((always_inline)) {
    const u32 sl = unit * 16384;
    wf[2 * ps] = lds_tr2(t1w(sl), t1w(sl + 1024));
    wf[2 * ps + 1] = lds_tr2(t1w(sl + 4096), t1w(sl + 4096 + 1024));
  };
  FS(41);
#pragma unroll
  for (int ps = 0; ps < 8; ++ps) grab1(ps, ps);
  u32x4 hrow[3];
  if (w >= 4) {
#pragma unroll
    for (int j = 0; j < 3; ++j) { const u32 it = min(j * 64 + lane, 143u); hrow[j] = lds_r128(L_HI + (w3 * 16 + it / 9) * 160 + (it % 9) * 16); }
  }
  FS(40);
  bar_v<0>();
  if (w == 0) st_flag(ws + W_FLAGD + (cg * 32 + rb) * 128, tag);
  STAMP(12); FS(39);

  const u32 Mq = idx & 3, Nq = kp * 8 + (idx >> 2), odd = Mq & 1;
  const char* w2b = P.w2 + (size_t)(kq * 1152 + w * 4) * 4096 + Nq * 256;
  const u32 w2off = g * 4096 + wcol;
  u32 w1k = w * 1024;
  asm volatile("" : "+s"(w1k));
  auto issue_w2 = [&](int ps, u32 unit) __attribute__((always_inline)) {
    const char* b = w2b + (ps < 9 ? ps + 9 * odd : ps - 9 * odd) * (32 * 4096);
    dma<64, false>(b, w2off, w1k, unit * 16384);
    dma<64, false>(b + 18 * (32 * 4096), w2off, w1k, unit * 16384 + 8192);
  };
  auto store_h = [&]() __attribute__((always_inline)) {
#pragma unroll
    for (int j = 0; j < 3; ++j) {
      const u32 it = min(j * 64 + lane, 143u);
      st128_sc1(ws + W_H + (rb * 64 + w3 * 16) * 1152 + cg * 144, (it / 9) * 1152 + (it % 9) * 16, hrow[j]);
    }
  };
  u32 tw2;
  { LANEVARS; tw2 = kh * 8192 + (4 * (g >> 1) + q4) * 256 + (w3 ^ q4) * 64 + (g & 1) * 32 + p4 * 8; }
#define GRAB2_DEF \
  const LAddr t2w(tw2); \
  auto grab = [&](int ps, u32 unit) __attribute__((always_inline)) { \
    const u32 sl = unit * 16384; \
    wf[2 * ps] = lds_tr2(t2w(sl), t2w(sl + 2048)); \
    wf[2 * ps + 1] = lds_tr2(t2w(sl + 4096), t2w(sl + 4096 + 2048)); \
  }
  const f32x16 zero16 = {0.f, 0.f, 0.f, 0.f, 0.f, 0.f, 0.f, 0.f, 0.f, 0.f, 0.f, 0.f, 0.f, 0.f, 0.f, 0.f};
  if (g1) {
    const u32 ord = mi == 0 ? 0x321504u : (mi == 1 ? 0x251403u : 0x504321u);
    auto tile_of = [&](int t) __attribute__((always_inline)) -> u32 {
      const u32 pos = t < 3 ? 2 * t + (kp ^ 1) : 2 * (t - 3) + kp;
      return mi * 6 + ((ord >> (4 * pos)) & 15u);
    };
    const char* hb = ws + W_HN + kp * 65536;
    constexpr u32 un2[8] = {0, 1, 2, 3, 4, 5, 8, 9};
    u32x2 bias[2];
    if (w >= 4) {
#pragma unroll
      for (int ps = 8; ps < 16; ++ps) issue_w1(ps, un2[ps - 8]);
      FS(51);
      store_h();
      waitv<3>();
    } else {
      const u32 cj = min((mi * 192) / 72 + q4, 7u);
      poll_flags(ws + W_FLAGD, (cj * 32 + kp * 16 + 8 * (w >> 1) + 2 * (w & 1) + (p4 & 1) + 4 * (p4 >> 1)) * 128, tag);
      FS(52);
      const char* tb = hb + tile_of(0) * 131072 + ((w >> 1) * 32 + (w & 1) * 8) * 1024;
#pragma unroll
      for (int j = 0; j < 8; ++j) dma<64, true>(tb + j * 1024, lane16, w * 8192, 3 * 32768 + j * 1024);
    }
    const u32 f16 = ni * 8 + 2 * w3 + kh;
    ld64(bias[0], P.b1 + f16 * 32, (lane >> 5) * 8);
    ld64(bias[1], P.b1 + f16 * 32, (lane >> 5) * 8, 16);
    FS(53);
    asm volatile("s_barrier" ::: "memory");
    STAMP(13); FS(54);
#pragma unroll
    for (int ps = 8; ps < 16; ++ps) grab1(ps, un2[ps - 8]);
    bar_v<0>();
    if (w == 4) st_flag(ws + W_FLAGH + (rb * 8 + cg) * 128, tag);
    FS(55);
    auto issue_a = [&](int i, int j) __attribute__((always_inline)) {
      dma<64, true>(hb + tile_of(i >> 1) * 131072 + w1k + ((j >> 1) * 32 + (i & 1) * 16 + (j & 1) * 8) * 1024, lane16, w1k, ((i + 3) & 3) * 32768 + j * 8192);
    };
    const LAddr ar(kh * 16384 + lane16);
    u32 exw = 9 * 16384 + w * 1024 + lane16, exr = 9 * 16384 + (w ^ 4) * 1024 + lane16;
    pin(exw); pin(exr);
    f32x16 cc[2], ci = zero16;
    f32x4 kv0, kv1;
    u32x4 a[6], rx, pv[2], uo;
    pin(bias[0]); pin(bias[1]);
    auto set_init = [&](f32x4 lo, f32x4 hi) __attribute__((always_inline)) {
      ci[0] = lo.x; ci[1] = lo.y; ci[2] = lo.z; ci[3] = lo.w; ci[4] = hi.x; ci[5] = hi.y; ci[6] = hi.z; ci[7] = hi.w;
      pin(ci);
    };
    set_init(unpk(bias[0].x, bias[0].y), unpk(bias[1].x, bias[1].y));
    auto rd = [&](int n) __attribute__((always_inline)) { a[n % 6] = lds_r128(ar((((n >> 4) + 3) & 3) * 32768 + (n & 15) * 1024)); };
    auto load_pv = [&](int t) __attribute__((always_inline)) {
      const char* pb = ws + W_UP + (tile_of(t) * 288 + f16) * 2048;
      ld128_sc1(pv[0], pb, lane16);
      ld128_sc1(pv[1], pb, lane16, 1024);
    };
    auto flag_u = [&](int t) __attribute__((always_inline)) {
      if (w == 0) st_flag(ws + W_FLAGE + (tile_of(t) * 36 + ni) * 128, tag);
    };
    auto exch_p = [&](int t) __attribute__((always_inline)) {
      const u32x2 p2 = pack4(quad<2>(cc[t & 1])), p3 = pack4(quad<3>(cc[t & 1]));
      rx = u32x4{p2.x, p2.y, p3.x, p3.y};
      pin(rx);
    };
    auto combine = [&](int t) __attribute__((always_inline)) {
      kv0 = quad<0>(cc[t & 1]) + unpk(rx.x, rx.y);
      kv1 = quad<1>(cc[t & 1]) + unpk(rx.z, rx.w);
      pin(kv0); pin(kv1);
    };
    auto send = [&](int t, int h) __attribute__((always_inline)) {
      const u32x2 p0 = pack4(h ? kv1 : kv0);
      st128_sc1(ws + W_UP + (tile_of(t) * 288 + f16) * 2048 + h * 1024, lane16, u32x4{p0.x, tag, p0.y, tag});
    };
    auto gather = [&](int t) __attribute__((always_inline)) {
      for (u32 spins = 0; spins < SPIN_CAP; ++spins) {
        pin(pv[0]); pin(pv[1]);
        u32 bad = (pv[0].y ^ tag) | (pv[0].w ^ tag) | (pv[1].y ^ tag) | (pv[1].w ^ tag);
        pin(bad);
        if (__builtin_expect(__all(bad == 0), 1)) break;
        load_pv(t);
        waitv<0>();
      }
      pin(pv[0]); pin(pv[1]);
      set_init(unpk(pv[0].x, pv[0].z), unpk(pv[1].x, pv[1].z));
    };
    auto act = [&](int j) __attribute__((always_inline)) {
      const f32x4 v = j < 2 ? kv0 : kv1;
      u32 t = (j & 1) ? pack2(gelu_tanh(v.z), gelu_tanh(v.w)) : pack2(gelu_tanh(v.x), gelu_tanh(v.y));
      pin(t);
      uo[j] = t;
    };
    auto store_u = [&](int t) __attribute__((always_inline)) { st128_sc1(ws + W_U + (tile_of(t) * 288 + f16) * 1024, lane16, uo); };
#pragma unroll
    for (int n = 0; n < 5; ++n) rd(n);
#pragma unroll
    for (int j = 0; j < 4; ++j) issue_a(1, j);
    SB();
#pragma unroll
    for (int i = 0; i < 12; ++i)
#pragma unroll
      for (int k = 0; k < 16; ++k) {
        const int n = i * 16 + k, t = i >> 1;
        if (n + 5 < 192) rd(n + 5);
        cc[t & 1] = mfmaB(wf[n & 31], a[n % 6], (n & 31) ? cc[t & 1] : ci);
        SB();
        if (i == 0 && k < 4) issue_a(2, k);
        if (!(i & 1) && i > 0 && k == 2) exch_p(t - 1);
        if ((i & 1) && i > 1) {
          if (i < 9) { if (k == 1 || k == 4) send(t - 1, k >> 2); }
          else { if (k < 8 && !(k & 1)) act(k >> 1); if (k == 7) store_u(t - 1); }
          if (i >= 5 && i < 11 && k == 6) load_pv(t + 1);
        }
        if (k == PRIO_K1) PRIO(0);
        if (k == 8) {
          constexpr int vm[12] = {4, 4, 4, 6, 6, 8, 8, 8, 8, 9, 4, 63};
          if (!(i & 1) && i > 0) { lds_w128(exw, rx); asm volatile("s_waitcnt lgkmcnt(0)" ::: "memory"); }
          asm volatile("s_waitcnt vmcnt(%0)\n\ts_barrier" ::"n"(vm[i]) : "memory");
          PRIO(1);
          if (i == 10) flag_u(3);
          if (!(i & 1) && i > 0) rx = lds_r128(exr);
        }
        if (k >= 9 && k < 13) {
          if (i + 3 < 12) issue_a(i + 3, k - 9);
          else if (k & 1) issue_w2(2 * (i - 9) + 1 + ((k - 9) >> 1), ((i + 2) & 3) * 2 + ((k - 9) >> 1));
        }
        if (!(i & 1) && i > 0 && k == 14) combine(t - 1);
        if ((i & 1) && i >= 5 && i < 11 && k == 14) { if (i == 9) waitv<5>(); else waitv<4>(); gather(t + 1); }
        if (i == 8 && k == 13) issue_w2(0, 8);
        SB();
      }
    PRIO(0);
    STAMP(14); FS(57);
    GRAB2_DEF;
    exch_p(5);
    lds_w128(exw, rx);
    bar_v<4>();
    rx = lds_r128(exr);
    flag_u(4);
    issue_w2(7, 4); issue_w2(8, 5);
    combine(5);
#pragma unroll
    for (int j = 0; j < 4; ++j) act(j);
    store_u(5);
    grab(0, 8); grab(1, 6); grab(2, 7); grab(3, 0); grab(4, 1);
    bar_v<5>();
    {
      constexpr u32 un[6] = {8, 6, 7, 0, 1, 9};
#pragma unroll
      for (int ps = 9; ps < 15; ++ps) issue_w2(ps, un[ps - 9]);
    }
    grab(5, 2); grab(6, 3);
    bar_v<12>();
    flag_u(5);
  } else {
    if (w >= 4) store_h();
    if (w == 0) { poll_flags(ws + W_FLAGD, lane * 128, tag); __builtin_amdgcn_s_sleep(16); }
    bar();
    {
      const u32 part = (idx - 27) * 8 + w;
      const char* pb = P.w2 + (size_t)kq * 1152 * 4096 + kp * 2048;
      u32 d[8];
#pragma unroll
      for (int j = 0; j < 8; ++j)
        asm volatile("global_load_dword %0, %1, %2" POL_S : "=v"(d[j]) : "v"(min((j * 40 + part) * 4 + g, 1151u) * 4096 + l15 * 128), "s"(pb) : "memory");
      asm volatile("s_waitcnt vmcnt(0)" : "+v"(d[0]), "+v"(d[1]), "+v"(d[2]), "+v"(d[3]), "+v"(d[4]), "+v"(d[5]), "+v"(d[6]), "+v"(d[7])::"memory");
    }
    GRAB2_DEF;
    constexpr u32 un0[9] = {8, 6, 7, 0, 1, 2, 3, 4, 5};
    bar();
    if (w == 4) st_flag(ws + W_FLAGH + (rb * 8 + cg) * 128, tag);
#pragma unroll
    for (int ps = 0; ps < 9; ++ps) issue_w2(ps, un0[ps]);
    bar_v<0>();
    grab(0, 8); grab(1, 6); grab(2, 7); grab(3, 0); grab(4, 1);
    bar();
    {
      constexpr u32 un[6] = {8, 6, 7, 0, 1, 9};
#pragma unroll
      for (int ps = 9; ps < 15; ++ps) issue_w2(ps, un[ps - 9]);
    }
    grab(5, 2); grab(6, 3);
    bar();
  }
  STAMP(15); FS(58);

  {
    const u32 T0 = 9 * (Mq >> 1), TF = T0 + 5 * odd;
    auto owner_of = [&](int b) __attribute__((always_inline)) -> u32 { return b == 4 ? kq : (u32)(b - 1) + ((u32)(b - 1) >= kq ? 1u : 0u); };
    GRAB2_DEF;
    issue_w2(15, 2); issue_w2(16, 3);
    grab(7, 4); grab(8, 5);
    u32 fo;
    {
      const u32 b = min(lane / 9, 4u);
      fo = lane < 60 ? ((T0 + 4 * odd + b) * 36 + kq * 9 + lane % 9) * 128 : (W_FLAGH - W_FLAGE) + ((Nq * 2 + (lane & 1)) * 8 + Mq * 2 + ((lane >> 1) & 1)) * 128;
      if (lane == 58 || lane == 59) fo = (W_FLAGH - W_FLAGE) + ((Nq * 2 + (lane & 1)) * 8 + (Mq >> 1) * 4 + 2 - odd) * 128;
    }
    bar_v<4>();
    issue_w2(17, 4);
    {
      constexpr u32 un[6] = {8, 6, 7, 0, 1, 9};
#pragma unroll
      for (int ps = 9; ps < 15; ++ps) grab(ps, un[ps - 9]);
    }
    if (w == 0) poll_flags(ws + W_FLAGE, fo, tag);
    bar_v<0>();
    const char* ub = ws + W_U + kq * (72 * 1024);
    u32 lx = (16 + (w & 1) + 18 * ((w >> 1) & 1)) * 1024;
    asm volatile("" : "+s"(lx));
    auto issue_u = [&](int i, int j) __attribute__((always_inline)) {
      const u32 T = i == 0 ? T0 + 4 : TF + owner_of((i + 1) >> 1);
      const u32 part = (i == 0 ? 0u : (u32)((i + 1) & 1)) ^ odd;
      const char* b = ub + (T * 288 + part * 18) * 1024;
      if (j < 4) dma<64, true>(b + w1k + ((j >> 1) * 36 + (j & 1) * 8) * 1024, lane16, w1k, ((i + 3) & 3) * 36864 + ((j >> 1) * 18 + (j & 1) * 8) * 1024);
      else dma<64, true>(b + lx + (w & 2) * 9216, lane16, lx, ((i + 3) & 3) * 36864);
    };
#pragma unroll
    for (int j = 0; j < 5; ++j) issue_u(0, j);
    grab(15, 2); grab(16, 3); grab(17, 4);
    bar();
    STAMP(16); FS(59);
#pragma unroll
    for (int j = 0; j < 5; ++j) issue_u(1, j);
    const u32 grp = Mq * 16 + Nq, me = odd * 4 + kq;
    u32 exw = 147456 + w * 1024 + lane16, exr = 147456 + (w ^ 4) * 1024 + lane16;
    pin(exw); pin(exr);
    const LAddr ar(kh * 18432 + (lane16 ^ (kh * 256)));
    f32x16 cc[2];
    f32x4 kv0, kv1;
    u32x4 a[6], rx, pv[6];
    u32x2 hres[2]; u32 bb;
    char* ysb = ws + W_YS + (((Mq >> 1) * 16 + Nq) * 8 + w) * 16384;
    auto rd = [&](int n) __attribute__((always_inline)) { a[n % 6] = lds_r128(ar(((n / 18 + 3) & 3) * 36864 + (n % 18) * 1024)); };
    auto exch_p = [&](int b) __attribute__((always_inline)) {
      const u32x2 p2 = pack4(quad<2>(cc[b & 1])), p3 = pack4(quad<3>(cc[b & 1]));
      rx = u32x4{p2.x, p2.y, p3.x, p3.y};
      pin(rx);
    };
    auto send = [&](int b, int h) __attribute__((always_inline)) {
      const u32x2 pk = pack4(h ? quad<1>(cc[b & 1]) + unpk(rx.z, rx.w) : quad<0>(cc[b & 1]) + unpk(rx.x, rx.y));
      const u32 o = owner_of(b ? b : 1);
      char* pb = b == 0 ? ysb + me * 2048 : ws + W_YP + (((grp * 4 + o) * 3 + ((kq - o - 1) & 3u)) * 8 + w) * 2048;
      st128_sc1(pb + h * 1024, lane16, u32x4{pk.x, tag, pk.y, tag});
    };
    auto load_res = [&](u32 T, u32 ln) __attribute__((always_inline)) -> u32 {
      const u32 srow = Nq * 128 + w3 * 32 + (ln & 31), oo = srow * 1152 + T * 64 + kh * 32 + (ln >> 5) * 8;
      ld64_sc1(hres[0], ws + W_H, oo);
      ld64_sc1(hres[1], ws + W_H + 16, oo);
      ld16(bb, P.b2, srow * 2);
      return oo;
    };
    u32 so, oo;
    auto load_ps = [&]() __attribute__((always_inline)) { ld128_sc1(pv[0], ysb, so); ld128_sc1(pv[1], ysb, so, 1024); };
    auto load_pv1 = [&](int j) __attribute__((always_inline)) {
      ld128_sc1(pv[j], ws + W_YP + ((grp * 4 + kq) * 3 * 8 + w) * 2048 + (j >> 1) * 16384, lane16, (j & 1) * 1024);
    };
    auto load_pv = [&]() __attribute__((always_inline)) {
#pragma unroll
      for (int j = 0; j < 6; ++j) load_pv1(j);
    };
    bar_vo<5>();
#pragma unroll
    for (int n = 0; n < 5; ++n) rd(n);
    SB();
#pragma unroll
    for (int i = 0; i < 9; ++i)
#pragma unroll
      for (int k = 0; k < 18; ++k) {
        const int n = i * 18 + k, b = (i + 1) >> 1, x = (i == 0 || (i & 1)) ? k : 18 + k;
        if (n + 5 < 162) rd(n + 5);
        cc[b & 1] = mfmaB(a[n % 6], wf[x], x ? cc[b & 1] : zero16);
        SB();
        if (i == 0 && k < 5) issue_u(2, k);
        if ((i & 1) && k == 2) exch_p(b - 1);
        if (k == PRIO_K2) PRIO(0);
        if (k == 5) {
          constexpr int vm[9] = {5, 5, 7, 5, 7, 10, 7, 4, 63};
          if (i & 1) { lds_w128(exw, rx); asm volatile("s_waitcnt lgkmcnt(0)" ::: "memory"); }
          asm volatile("s_waitcnt vmcnt(%0)\n\ts_barrier" ::"n"(vm[i]) : "memory");
          PRIO(1);
          if (i & 1) rx = lds_r128(exr);
        }
        if ((i & 1) && (k == 8 || k == 10)) send(b - 1, (k - 8) >> 1);
        if (k >= 11 && k < 16 && i + 3 < 9) issue_u(i + 3, k - 11);
        if (i == 4 && k == 6) { so = (lane >> 3) * 2048 + (me * 8 + (lane & 7)) * 16; load_ps(); }
        if (i == 4 && k == 8) oo = load_res(T0 + 4, me * 8 + (lane & 7));
        if (i == 6) {
          if (k == 0) {
            waitv<12>();
            for (u32 spins = 0; spins < SPIN_CAP; ++spins) {
              pin(pv[0]); pin(pv[1]);
              u32 bad = (pv[0].y ^ tag) | (pv[0].w ^ tag) | (pv[1].y ^ tag) | (pv[1].w ^ tag);
              pin(bad);
              if (__builtin_expect(__all(bad == 0), 1)) break;
              load_ps();
              waitv<0>();
            }
            pin(pv[0]); pin(pv[1]);
            kv0 = unpk(pv[0].x, pv[0].z); kv1 = unpk(pv[1].x, pv[1].z);
            pin(kv0); pin(kv1);
          }
          if (k >= 1 && k < 5) { kv0[k - 1] = sum_g(kv0[k - 1] + dpp<0x128>(kv0[k - 1])); pin(kv0); }
          if (k >= 6 && k < 10) { kv1[k - 6] = sum_g(kv1[k - 6] + dpp<0x128>(kv1[k - 6])); pin(kv1); }
          if (k == 11) {
            pin(hres[0]); pin(hres[1]); pin(bb);
            const u32x2 o0 = pack4(kv0 + unpk(hres[0].x, hres[0].y) + bflo(bb)), o1 = pack4(kv1 + unpk(hres[1].x, hres[1].y) + bflo(bb));
            const bool hi = (lane & 8) != 0;
            asm volatile("s_mov_b64 exec, 0xffff\n\tglobal_store_dwordx2 %0, %1, %2\n\ts_mov_b64 exec, -1" ::"v"(oo + (lane & 8) * 2), "v"(hi ? o1 : o0), "s"(P.out) : "memory");
          }
          if (k == 14) oo = load_res(TF + kq, lane);
        }
        if (i == 8 && k == 2) {
          waitv<2>();
          pin(hres[0]); pin(hres[1]); pin(bb);
          kv0 = unpk(hres[0].x, hres[0].y) + bflo(bb); kv1 = unpk(hres[1].x, hres[1].y) + bflo(bb);
          pin(kv0); pin(kv1);
        }
        if (i == 8 && k >= 8 && k < 14) load_pv1(k - 8);
        SB();
      }
    PRIO(0);
    exch_p(4);
    lds_w128(exw, rx);
    bar();
    rx = lds_r128(exr);
    STAMP(17); FS(60);
    for (u32 spins = 0; spins < SPIN_CAP; ++spins) {
      waitv<0>();
      u32 bad = 0;
#pragma unroll
      for (int j = 0; j < 6; ++j) { pin(pv[j]); bad |= (pv[j].y ^ tag) | (pv[j].w ^ tag); }
      pin(bad);
      if (__all(bad == 0)) break;
      load_pv();
    }
    waitv<0>();
#pragma unroll
    for (int j = 0; j < 6; ++j) pin(pv[j]);
#pragma unroll
    for (int s = 0; s < 3; ++s) { kv0 += unpk(pv[2 * s].x, pv[2 * s].z); kv1 += unpk(pv[2 * s + 1].x, pv[2 * s + 1].z); }
    const u32x2 o0 = pack4(kv0 + quad<0>(cc[0]) + unpk(rx.x, rx.y)), o1 = pack4(kv1 + quad<1>(cc[0]) + unpk(rx.z, rx.w));
    auto s0 = __builtin_amdgcn_permlane32_swap(o0.x, o1.x, false, false);
    auto s1 = __builtin_amdgcn_permlane32_swap(o0.y, o1.y, false, false);
    asm volatile("global_store_dwordx4 %0, %1, %2\n\ts_nop 1" ::"v"(oo + (lane >> 5) * 8), "v"(u32x4{s0[0], s1[0], s0[1], s1[1]}), "s"(P.out) : "memory");
  }
  asm volatile(".Lfk_code_end:");
  if (bid == 0 && w == 0) st_flag(ws + W_EPOCH, tag);
}
}

extern "C" void kernel_launch(void* const* d_in, const int* in_sizes, int n_in, void* d_out, int out_size, void* d_ws,
                              size_t ws_size, hipStream_t stream) {
  Params p;
  p.x = (const char*)d_in[0]; p.wqkv = (const char*)d_in[1]; p.wphi = (const char*)d_in[2]; p.wo = (const char*)d_in[3];
  p.w1 = (const char*)d_in[5]; p.b1 = (const char*)d_in[6]; p.w2 = (const char*)d_in[7]; p.b2 = (const char*)d_in[8];
  p.out = (char*)d_out; p.ws = (char*)d_ws;
  (void)hipFuncSetAttribute((const void*)fused_kernel, hipFuncAttributeMaxDynamicSharedMemorySize, LDS_BYTES);
  fused_kernel<<<NWG, NT, LDS_BYTES, stream>>>(p);
}
```

```cpp
#include <hip/hip_runtime.h>
#include <hip/hip_bf16.h>

typedef unsigned u32;
typedef unsigned long long u64;
typedef u32 u32x2 __attribute__((ext_vector_type(2)));
typedef u32 u32x4 __attribute__((ext_vector_type(4)));
typedef float f32x2 __attribute__((ext_vector_type(2)));
typedef float f32x4 __attribute__((ext_vector_type(4)));
typedef float f32x16 __attribute__((ext_vector_type(16)));
typedef short s16x4 __attribute__((ext_vector_type(4)));
typedef __bf16 bf16x2 __attribute__((ext_vector_type(2)));
typedef __bf16 bf16x8 __attribute__((ext_vector_type(8)));
#define LAS __attribute__((address_space(3)))
#define DEVINL __device__ __forceinline__

namespace {
constexpr int S = 2048, D = 576, F = 4608;
constexpr int NWG = 256, NT = 512;
constexpr int LDS_BYTES = 163840;
constexpr u32 SPIN_CAP = 1u << 16;

constexpr u32 W_EPOCH = 0;
constexpr u32 W_FLAGB = 4096;
constexpr u32 W_FLAGD = W_FLAGB + 1024 * 128;
constexpr u32 W_FLAGE = W_FLAGD + 256 * 128;
constexpr u32 W_FLAGH = W_FLAGE + 18 * 36 * 128;
constexpr u32 W_KVFIN = W_FLAGH + 256 * 128;
constexpr u32 W_STATS = W_KVFIN + 8 * 96 * 128;
constexpr u32 W_KVPART = W_STATS + 32 * 4 * 8 * 256;
constexpr u32 W_O = W_KVPART + 8 * 96 * 32 * 128;
constexpr u32 W_H = W_O + 128 * 18 * 1024;
constexpr u32 W_HN = W_H + S * D * 2;
constexpr u32 W_U = W_HN + 18 * 128 * 1024;
constexpr u32 W_UP = W_U + 18 * 288 * 1024;
constexpr u32 W_YP = W_UP + 18 * 288 * 2048;
constexpr u32 W_YS = W_YP + 64 * 4 * 3 * 8 * 2048;
constexpr u32 W_END = W_YS + 32 * 8 * 8 * 2048;
constexpr u32 W_DBG = W_END;

constexpr u32 SLOT = 27648;
constexpr u32 L_XN = 3 * SLOT;
constexpr u32 L_PHI = 4 * SLOT;
constexpr u32 L_V = L_PHI + 64 * 328;
constexpr u32 L_KV = 147456;
constexpr u32 L_MISC = L_XN + 73728;
constexpr u32 L_WPHI = L_MISC;
constexpr u32 L_ZERO = L_MISC + 5760;
constexpr u32 L_CNT = L_MISC + 5824;
constexpr u32 L_HI = 5 * SLOT;
constexpr u32 L_HN = L_HI + 10240;

struct Params {
  const char* x; const char* wqkv; const char* wphi; const char* wo;
  const char* w1; const char* b1; const char* w2; const char* b2;
  char* out; char* ws;
};

DEVINL f32x4 mfma32(u32x4 a, u32x4 b, f32x4 c) {
  return __builtin_amdgcn_mfma_f32_16x16x32_bf16(__builtin_bit_cast(bf16x8, a), __builtin_bit_cast(bf16x8, b), c, 0, 0, 0);
}
DEVINL f32x16 mfmaB(u32x4 a, u32x4 b, f32x16 c) {
  return __builtin_amdgcn_mfma_f32_32x32x16_bf16(__builtin_bit_cast(bf16x8, a), __builtin_bit_cast(bf16x8, b), c, 0, 0, 0);
}
template <int Q> DEVINL f32x4 quad(const f32x16& v) { return f32x4{v[4 * Q], v[4 * Q + 1], v[4 * Q + 2], v[4 * Q + 3]}; }
DEVINL f32x4 mfma16(u32x2 a, u32x2 b, f32x4 c) {
  return mfma32(u32x4{a.x, a.y, 0u, 0u}, u32x4{b.x, b.y, 0u, 0u}, c);
}
DEVINL u32 pack2(float lo, float hi) {
  f32x2 p = {lo, hi};
  return __builtin_bit_cast(u32, __builtin_convertvector(p, bf16x2));
}
DEVINL u32x2 pack4(f32x4 v) { return u32x2{pack2(v.x, v.y), pack2(v.z, v.w)}; }
DEVINL float bflo(u32 v) { return __builtin_bit_cast(float, v << 16); }
DEVINL float bfhi(u32 v) { return __builtin_bit_cast(float, v & 0xffff0000u); }
DEVINL float asf(u32 v) { return __builtin_bit_cast(float, v); }
DEVINL u32 asu(float v) { return __builtin_bit_cast(u32, v); }
DEVINL f32x4 unpk(u32 a, u32 b) { return f32x4{bflo(a), bfhi(a), bflo(b), bfhi(b)}; }
DEVINL float dot2(u32 a, u32 b, float c) {
  return __builtin_amdgcn_fdot2_f32_bf16(__builtin_bit_cast(bf16x2, a), __builtin_bit_cast(bf16x2, b), c, false);
}
DEVINL u32x2 lds_tr(u32 addr) {
  return __builtin_bit_cast(u32x2, __builtin_amdgcn_ds_read_tr16_b64_v4i16((LAS s16x4*)addr));
}
DEVINL u32x4 lds_tr2(u32 a0, u32 a1) {
  u32x2 lo = lds_tr(a0), hi = lds_tr(a1);
  return u32x4{lo.x, lo.y, hi.x, hi.y};
}
DEVINL u32x4 lds_r128(u32 addr) { return *(LAS u32x4*)addr; }
DEVINL u32x2 lds_r64(u32 addr) { return *(LAS u32x2*)addr; }
DEVINL void lds_w128(u32 addr, u32x4 v) { *(LAS u32x4*)addr = v; }
DEVINL void lds_w64(u32 addr, u32x2 v) { *(LAS u32x2*)addr = v; }
template <int CTRL> DEVINL float dpp(float v) { return asf(__builtin_amdgcn_update_dpp(0u, asu(v), CTRL, 0xf, 0xf, true)); }
DEVINL float sum8(float v) { v += dpp<0xB1>(v); v += dpp<0x4E>(v); return v + dpp<0x141>(v); }
DEVINL float sum_g(float v) {
  auto a = __builtin_amdgcn_permlane16_swap(asu(v), asu(v), false, false);
  v = asf(a[0]) + asf(a[1]);
  auto b = __builtin_amdgcn_permlane32_swap(asu(v), asu(v), false, false);
  return asf(b[0]) + asf(b[1]);
}
#define SB() __builtin_amdgcn_sched_barrier(0)
#define PRIO_K1 2
#define PRIO_K2 2
#define PRIO(v)
DEVINL u32 fresh_lane() { u32 l; asm volatile("v_mbcnt_lo_u32_b32 %0, -1, 0\n\tv_mbcnt_hi_u32_b32 %0, -1, %0" : "=v"(l)); __builtin_assume(l < 64); return l; }
#define LANEVARS const u32 lane = fresh_lane(), l15 = lane & 15, g = lane >> 4, q4 = l15 >> 2, p4 = lane & 3; \
  const u32 trrow = (16 * (g >> 1) + 4 * (g & 1) + q4) * 288; (void)l15; (void)g; (void)q4; (void)p4; (void)trrow
struct LAddr {
  u32 b[3];
  DEVINL explicit LAddr(u32 a) { b[0] = a; b[1] = a + 65536u; b[2] = a + 131072u; asm volatile("" : "+v"(b[0]), "+v"(b[1]), "+v"(b[2])); }
  DEVINL u32 operator()(u32 c) const { return b[c >> 16] + (c & 0xffffu); }
};

#define POL_W ""
#define POL_T ""
#define POL_S ""
#define PFMASK 0xff
#define B1N 4
#define B3WAIT 0
#define POL_1 ""
#define ROT1 1
#define POL_X ""
template <int NL, bool SC1> DEVINL void dma(const char* base, u32 voff, u32 lbase, u32 loff) {
  if constexpr (NL == 64) {
    if constexpr (SC1) asm volatile("s_add_u32 m0, %2, %3\n\ts_nop 0\n\tglobal_load_lds_dwordx4 %0, %1 sc1" ::"v"(voff), "s"(base), "s"(lbase), "n"(loff) : "memory", "scc");
    else asm volatile("s_add_u32 m0, %2, %3\n\ts_nop 0\n\tglobal_load_lds_dwordx4 %0, %1" POL_W ::"v"(voff), "s"(base), "s"(lbase), "n"(loff) : "memory", "scc");
  } else {
    if constexpr (SC1) asm volatile("s_add_u32 m0, %2, %3\n\ts_mov_b32 exec_hi, %4\n\tglobal_load_lds_dwordx4 %0, %1 sc1\n\ts_mov_b32 exec_hi, -1" ::"v"(voff), "s"(base), "s"(lbase), "n"(loff), "n"((1u << (NL - 32)) - 1u) : "memory", "scc");
    else asm volatile("s_add_u32 m0, %2, %3\n\ts_mov_b32 exec_hi, %4\n\tglobal_load_lds_dwordx4 %0, %1" POL_X "\n\ts_mov_b32 exec_hi, -1" ::"v"(voff), "s"(base), "s"(lbase), "n"(loff), "n"((1u << (NL - 32)) - 1u) : "memory", "scc");
  }
}
DEVINL void dma_w1(const char* base, u32 voff, u32 lds) {
  asm volatile("s_mov_b32 m0, %2\n\ts_nop 0\n\tglobal_load_lds_dwordx4 %0, %1" POL_1 ::"v"(voff), "s"(base), "s"(lds) : "memory");
}
DEVINL void st128_sc1(char* base, u32 voff, u32x4 v) {
  asm volatile("global_store_dwordx4 %0, %1, %2 sc1\n\ts_nop 1" ::"v"(voff), "v"(v), "s"(base) : "memory");
}
DEVINL void st128_sc1_m(char* base, u32 voff, u32x4 v, u64 mask) {
  asm volatile("s_mov_b64 exec, %3\n\tglobal_store_dwordx4 %0, %1, %2 sc1\n\ts_mov_b64 exec, -1\n\ts_nop 0" ::"v"(voff), "v"(v), "s"(base), "s"(mask) : "memory");
}
DEVINL void st64_sc1(char* base, u32 voff, u32x2 v) {
  asm volatile("global_store_dwordx2 %0, %1, %2 sc1" ::"v"(voff), "v"(v), "s"(base) : "memory");
}
DEVINL void st64(char* base, u32 voff, u32x2 v) {
  asm volatile("global_store_dwordx2 %0, %1, %2" ::"v"(voff), "v"(v), "s"(base) : "memory");
}
DEVINL void st32_sc1(char* base, u32 voff, u32 v) {
  asm volatile("global_store_dword %0, %1, %2 sc1" ::"v"(voff), "v"(v), "s"(base) : "memory");
}
DEVINL void st_flag(char* addr, u32 tag) {
  u32 z, t;
  asm volatile("v_mov_b32 %0, 0\n\tv_mov_b32 %1, %3\n\ts_mov_b64 exec, 1\n\tglobal_store_dword %0, %1, %2 sc1\n\ts_mov_b64 exec, -1" : "=&v"(z), "=&v"(t) : "s"(addr), "s"(tag) : "memory");
}
DEVINL void ld128(u32x4& d, const char* base, u32 voff, int imm) {
  asm volatile("global_load_dwordx4 %0, %1, %2 offset:%3" POL_X : "=v"(d) : "v"(voff), "s"(base), "n"(imm) : "memory");
}
DEVINL void ld128_sc1(u32x4& d, const char* base, u32 voff, int imm = 0) {
  asm volatile("global_load_dwordx4 %0, %1, %2 offset:%3 sc1" : "=v"(d) : "v"(voff), "s"(base), "n"(imm) : "memory");
}
DEVINL void ld64(u32x2& d, const char* base, u32 voff, int imm = 0) {
  asm volatile("global_load_dwordx2 %0, %1, %2 offset:%3" : "=v"(d) : "v"(voff), "s"(base), "n"(imm) : "memory");
}
DEVINL void ld32(u32& d, const char* base, u32 voff) {
  asm volatile("global_load_dword %0, %1, %2" POL_T : "=v"(d) : "v"(voff), "s"(base) : "memory");
}
DEVINL void touch(u32& d, const char* base, u32 voff) {
  asm volatile("global_load_dword %0, %1, %2" POL_T : "+v"(d) : "v"(voff), "s"(base) : "memory");
}
DEVINL void ld64_sc1(u32x2& d, const char* base, u32 voff) {
  asm volatile("global_load_dwordx2 %0, %1, %2 sc1" : "=v"(d) : "v"(voff), "s"(base) : "memory");
}
DEVINL void ld16(u32& d, const char* base, u32 voff) {
  asm volatile("global_load_ushort %0, %1, %2" : "=v"(d) : "v"(voff), "s"(base) : "memory");
}
template <int N> DEVINL void waitv() { asm volatile("s_waitcnt vmcnt(%0)" ::"n"(N) : "memory"); }
template <typename T> DEVINL void pin(T& v) { asm volatile("" : "+v"(v)); }
template <int N> DEVINL void bar_v() { asm volatile("s_waitcnt vmcnt(%0) lgkmcnt(0)\n\ts_barrier" ::"n"(N) : "memory"); }
template <int N> DEVINL void bar_vo() { asm volatile("s_waitcnt vmcnt(%0)\n\ts_barrier" ::"n"(N) : "memory"); }
DEVINL void bar() { asm volatile("s_waitcnt lgkmcnt(0)\n\ts_barrier" ::: "memory"); }

DEVINL void cnt_add(u32 c) {
  u32 a, o;
  asm volatile("v_mov_b32 %0, %2\n\tv_mov_b32 %1, 1\n\ts_mov_b64 exec, 1\n\tds_add_u32 %0, %1\n\ts_mov_b64 exec, -1" : "=&v"(a), "=&v"(o) : "n"(L_CNT + 4 * c) : "memory");
}
DEVINL void cnt_wait(u32 c) {
  for (u32 spins = 0; spins < (SPIN_CAP << 4); ++spins) {
    u32 v;
    asm volatile("v_mov_b32 %0, %1\n\tds_read_b32 %0, %0\n\ts_waitcnt lgkmcnt(0)" : "=v"(v) : "n"(L_CNT + 4 * c) : "memory");
    if (__builtin_amdgcn_readfirstlane(v) >= 4u) break;
    __builtin_amdgcn_s_sleep(1);
  }
}
template <int N> DEVINL void sync4(u32 c) {
  asm volatile("s_waitcnt vmcnt(%0) lgkmcnt(0)" ::"n"(N) : "memory");
  cnt_add(c);
  cnt_wait(c);
}
DEVINL void poll_flags(const char* base, u32 voff, u32 tag) {
  for (u32 spins = 0; spins < SPIN_CAP; ++spins) {
    u32 v;
    asm volatile("global_load_dword %0, %1, %2 sc1\n\ts_waitcnt vmcnt(0)" : "=v"(v) : "v"(voff), "s"(base) : "memory");
    if (__all(v == tag)) break;
  }
}

#define STAMP(i)

#define FS(i)
DEVINL void feature_map2(const u32x4 (&wa)[5], const u32x2 (&wb)[5], const f32x4 (&t0)[2], const f32x4 (&t1)[2], const f32x4 (&t2)[2], u32 (&ph)[2][9]) {
  float p[2][20];
#pragma unroll
  for (int m = 0; m < 2; ++m) {
    const u32x2 p0 = pack4(t0[m]), p1 = pack4(t1[m]), p2 = pack4(t2[m]);
    const u32x4 b01 = {p0.x, p0.y, p1.x, p1.y};
    f32x4 a[5];
#pragma unroll
    for (int t = 0; t < 5; ++t) a[t] = mfma32(wa[t], b01, f32x4{0.f, 0.f, 0.f, 0.f});
#pragma unroll
    for (int t = 0; t < 5; ++t) {
      a[t] = mfma16(wb[t], p2, a[t]);
      p[m][4 * t] = a[t].x; p[m][4 * t + 1] = a[t].y; p[m][4 * t + 2] = a[t].z; p[m][4 * t + 3] = a[t].w;
    }
  }
  const float L2E = 1.44269504088896341f;
#pragma unroll
  for (int m = 0; m < 2; ++m) {
    float mx = p[m][0];
#pragma unroll
    for (int i = 1; i < 18; ++i) mx = fmaxf(mx, p[m][i]);
    const float nm = -mx * L2E;
    f32x2 sum = {0.f, 0.f};
#pragma unroll
    for (int i = 0; i < 18; ++i) p[m][i] = __builtin_amdgcn_exp2f(__builtin_fmaf(p[m][i], L2E, nm));
#pragma unroll
    for (int i = 0; i < 9; ++i) sum += f32x2{p[m][2 * i], p[m][2 * i + 1]};
    const float inv = __builtin_amdgcn_rcpf(sum.x + sum.y);
#pragma unroll
    for (int i = 0; i < 9; ++i) ph[m][i] = pack2(p[m][2 * i] * inv, p[m][2 * i + 1] * inv);
  }
}

DEVINL float gelu_tanh(float x) {
  const float C = -2.f * 0.7978845608028654f * 1.44269504088896341f;
  float y = x * __builtin_fmaf(x * x, 0.044715f * C, C);
  float e = __builtin_amdgcn_exp2f(y);
  return x * __builtin_amdgcn_rcpf(1.f + e);
}

__global__ __launch_bounds__(NT, 2) void fused_kernel(Params P) {
  extern __shared__ __attribute__((aligned(16))) char smem[];
  STAMP(0); FS(0);
  const u32 w = __builtin_amdgcn_readfirstlane(threadIdx.x >> 6);
  const u32 bid = blockIdx.x, xcd = bid & 7, idx = bid >> 3;
  char* const ws = P.ws;
  u64 pc; u32 len;
  asm volatile("s_getpc_b64 %0\n.Lfk_code_begin:\n\ts_mov_b32 %1, .Lfk_code_end-.Lfk_code_begin\n\ts_max_u32 %1, %1, .Lfk_copy_end-.Lfk_code_begin" : "=s"(pc), "=s"(len)::"scc");

  const u32 cg = (xcd & 1) * 4 + (idx & 3), rb = (xcd >> 1) * 8 + (idx >> 2);
  const u32 rot = (idx >> 2) % 6;
  const u32 w3 = w & 3, kh = w >> 2;
  const u32 kq = xcd >> 1, kp = xcd & 1;
  const bool g1 = idx < 27;
  const u32 ni = kq * 9 + (g1 ? idx / 3 : 0), mi = idx % 3;
  u32 tag, tag0;
  u32x4 wf[36];
  u32x4 xr[9];
  const u32 rgp = w ^ ((cg & 1) * 4);
  auto load_x = [&]() __attribute__((always_inline)) {
    LANEVARS;
    const u32 xo = (rb * 64 + rgp * 8 + (lane >> 3)) * 1152 + (lane & 7) * 16;
#pragma unroll
    for (int i = 0; i < 9; ++i) ld128(xr[i], P.x, xo, i * 128);
    asm volatile("s_load_dword %0, %1, 0x0" : "=s"(tag0) : "s"(ws) : "memory");
    FS(1);
  };
  auto layer_norm = [&]() __attribute__((always_inline)) {
    LANEVARS;
    f32x2 c[36];
    f32x2 s1 = {0.f, 0.f}, s2 = {0.f, 0.f};
#pragma unroll
    for (int i = 0; i < 9; ++i) {
#pragma unroll
      for (int j = 0; j < 4; ++j) {
        const f32x2 v = {bflo(xr[i][j]), bfhi(xr[i][j])};
        c[i * 4 + j] = v;
        s1 += v; s2 += v * v;
      }
    }
    const float mean = sum8(s1.x + s1.y) * (1.f / 576.f);
    const float var = fmaxf(sum8(s2.x + s2.y) * (1.f / 576.f) - mean * mean, 0.f);
    const float rs = __builtin_amdgcn_rsqf(var + 1e-5f), nb = -mean * rs;
    const u32 a0 = L_XN + ((rgp >> 1) * 18 + ((lane & 7) >> 2)) * 1024 + (lane & 3) * 256 + (((8 * (rgp & 1) + (lane >> 3)) ^ (lane & 7)) * 16);
#pragma unroll
    for (int i = 0; i < 9; ++i) {
      u32x4 o;
#pragma unroll
      for (int j = 0; j < 4; ++j) { const f32x2 v = c[i * 4 + j] * rs + nb; o[j] = pack2(v.x, v.y); }
      lds_w128(a0 + i * 2048, o);
    }
  };
#define WAIT_X(N) asm volatile("s_waitcnt vmcnt(" #N ")" : "+v"(xr[0]), "+v"(xr[1]), "+v"(xr[2]), "+v"(xr[3]), "+v"(xr[4]), "+v"(xr[5]), "+v"(xr[6]), "+v"(xr[7]), "+v"(xr[8])::"memory")
  auto unit_at = [](int u) -> u32 { return u < 6 ? 2 * SLOT + u * 8192 : (u < 9 ? (u - 6) * 8192 : (u < 12 ? SLOT + (u - 9) * 8192 : 2 * SLOT + (u - 6) * 8192)); };
  auto grab1 = [&](const LAddr& t0, const LAddr& t1, int ks, int u) __attribute__((always_inline)) {
    const u32 sl = unit_at(u);
    wf[2 * ks] = lds_tr2(t0(sl), t0(sl + 1024));
    wf[2 * ks + 1] = lds_tr2(t1(sl), t1(sl + 1024));
  };
#define T1W_DEF const u32 t1a = (8 * g + q4) * 256 + (((2 * w3) ^ (4 * (g & 1) + q4)) * 32) + p4 * 8; const LAddr t1w(t1a), t1x(t1a ^ 32)
  u32x4 hrow[3];
  const u32 wl = w3 * 3456;
  const char* wq = P.wqkv + cg * 144;
  auto stage = [&](u32 n) __attribute__((always_inline)) -> u32 { const u32 st = rot + n; return (st >= 6 ? st - 6 : st) * (96 * 3456); };
#define RING_DEF \
  const u32 r3 = lane / 18, c18 = lane % 18, seg = c18 / 9, ch9 = c18 % 9; \
  u32 wrow[4], voff[4]; \
  _Pragma("unroll") for (int i = 0; i < 4; ++i) { \
    const u32 r = 12 * w3 + 3 * i + r3; \
    wrow[i] = (r & ~12u) | ((r & 4u) << 1) | ((r & 8u) >> 1); \
    voff[i] = wrow[i] * 3456 + seg * 1152 + ch9 * 16; \
    pin(voff[i]); \
  } \
  auto issue1 = [&](int n, int j0, int j1) __attribute__((always_inline)) {   \
    constexpr u32 slot[9] = {0, 1, 2, 3, 4, 0, 1, 2, 3}; \
    const char* b = n < 6 ? wq + 1152 + stage(n) : wq; \
    const u32 qo = n < 6 ? 0u : (seg ? stage(2 * (n - 6) + 1) - 1152 : stage(2 * (n - 6))); \
    _Pragma("unroll") for (int j = j0; j < j1; ++j) \
      dma<54, false>(b + (j >> 2) * (48 * 3456), voff[j & 3] + qo, wl, slot[n] * SLOT + (j >> 2) * 13824 + (j & 3) * 864); \
  }

  if (__builtin_expect(w < 4, 1)) {
    const u32 mp = w >> 1, hp = w & 1;
    load_x();
    {
      LANEVARS;
      RING_DEF;
      WAIT_X(0);
      STAMP(1); FS(2);
      layer_norm();
      FS(3);
      issue1(1, 0, 8);
    }
    u32 ta0, ta1, xa[2];
    {
      LANEVARS;
      if (lane < 32) *(LAS u32*)(L_ZERO + lane * 4) = 0u;
      ta0 = trrow + hp * 72 + p4 * 8; ta1 = trrow + hp * 72 + 64 + (p4 & 1) * 144;
#pragma unroll
      for (int i = 0; i < 2; ++i) { xa[i] = L_XN + mp * 36864 + g * 256 + ((l15 ^ (((i + rot) & 1) * 4 + g)) * 16); pin(xa[i]); }
    }
    const LAddr trA(ta0);
    FS(4);
    asm volatile("s_waitcnt lgkmcnt(0)\n\ts_barrier\n\ts_add_u32 %0, %1, 1" : "=s"(tag) : "s"(tag0) : "memory", "scc");
    STAMP(2); FS(5);
    u32x4 xf[2][18];
    auto rdx = [&](int n) __attribute__((always_inline)) {
      u32 st = rot + n / 3; if (st >= 6) st -= 6;
      const u32 ad = xa[(n / 3 + n % 3) & 1] + st * 3072;
      xf[0][n] = lds_r128(ad + (n % 3) * 1024);
      xf[1][n] = lds_r128(ad + (n % 3) * 1024 + 18432);
    };
    f32x4 acc[5][2];
#pragma unroll
    for (int i = 0; i < 5; ++i) { acc[i][0] = f32x4{0.f, 0.f, 0.f, 0.f}; acc[i][1] = acc[i][0]; }
    u32x4 wa[5]; u32x2 wb[5];
    {
      const LAddr trM(ta1);
      u32x4 F[6];
      u32 ad[3];
      auto rd = [&](int u) __attribute__((always_inline)) {
        constexpr u32 toff[4] = {0, 32, 144, 176};
        const int n = u / 5, i = u % 5;
        const u32 b = ((n / 3) % 5) * SLOT + (n % 3) * 9216;
        F[u % 6] = i < 4 ? lds_tr2(trA(b + toff[i & 3]), trA(b + toff[i & 3] + 2304)) : lds_tr2(trM(b), trM(b + 2304));
      };
      rdx(0);
#pragma unroll
      for (int u = 0; u < 5; ++u) rd(u);
      rdx(1);
      SB();
#pragma unroll
      for (int n = 0; n < 18; ++n) {
        if (n == 14) {
          LANEVARS;
#pragma unroll
          for (int a = 0; a < 3; ++a) {
            const u32 ic = a < 2 ? 16 * a + 4 * g + q4 : 32 + q4;
            const bool ok = (ic >= 18 ? 1u : 0u) == (p4 >> 1) && (a < 2 || g == 0);
            ad[a] = ok ? L_WPHI + (p4 & 1) * 2880 + (hp * 36 + ic) * 40 : L_ZERO;
            pin(ad[a]);
          }
        }
        if (n % 3 == 2 && n < 17) { FS(6 + 2 * (n / 3)); bar_v<0>(); FS(7 + 2 * (n / 3)); }
#pragma unroll
        for (int i = 0; i < 5; ++i) {
          const int u = n * 5 + i;
          if (u + 5 < 90) rd(u + 5);
          else { wa[i] = lds_tr2(ad[0] + 8 * i, ad[1] + 8 * i); wb[i] = lds_tr(ad[2] + 8 * i); }
          if (u < 8) { rdx(2 + 2 * u); rdx(3 + 2 * u); }
          acc[i][0] = mfma32(F[u % 6], xf[0][n], acc[i][0]);
          acc[i][1] = mfma32(F[u % 6], xf[1][n], acc[i][1]);
          SB();
        }
      }
    }
    STAMP(3); FS(16);
    {
      LANEVARS;
      const u32 va = L_V + (mp * 32 + l15) * 152 + hp * 72 + g * 8;
#pragma unroll
      for (int m = 0; m < 2; ++m) {
        lds_w64(va + m * (16 * 152), pack4(acc[2][m]));
        lds_w64(va + m * (16 * 152) + 32, pack4(acc[3][m]));
        if (g == 1) lds_w64(va + m * (16 * 152) + 56, pack4(acc[4][m]));
      }
      u32 ph[2][9];
      feature_map2(wa, wb, acc[0], acc[1], acc[4], ph);
      const u32 pa = L_PHI + (mp * 32 + l15) * 328 + (hp * 4 + g) * 40;
#pragma unroll
      for (int m = 0; m < 2; ++m)
#pragma unroll
        for (int i = 0; i < 5; ++i) lds_w64(pa + m * (16 * 328) + 8 * i, u32x2{ph[m][2 * i], i < 4 ? ph[m][2 * i + 1] : 0u});
    }
    FS(17);
    bar();
    FS(18);
    bar();
    STAMP(4); FS(20);
#pragma unroll
    for (int i = 0; i < 3; ++i) { acc[i][0] = f32x4{0.f, 0.f, 0.f, 0.f}; acc[i][1] = acc[i][0]; }
    {
      u32 tq;
      { LANEVARS; tq = trrow + hp * 72 + 64; }
      const LAddr trQ(tq);
      u32x4 F[6];
      auto rd = [&](int u) __attribute__((always_inline)) {
        const int n = u / 3, i = u % 3;
        const u32 b = (1 + n / 6) * SLOT + (n % 3) * 9216 + ((n % 6) / 3) * 144;
        F[u % 6] = i < 2 ? lds_tr2(trA(b + 32 * i), trA(b + 32 * i + 2304)) : lds_tr2(trQ(b), trQ(b + 2304));
      };
#pragma unroll
      for (int u = 0; u < 5; ++u) rd(u);
      SB();
#pragma unroll
      for (int u = 0; u < 54; ++u) {
        if (u + 5 < 54) rd(u + 5);
        acc[u % 3][0] = mfma32(F[u % 6], xf[0][u / 3], acc[u % 3][0]);
        acc[u % 3][1] = mfma32(F[u % 6], xf[1][u / 3], acc[u % 3][1]);
        SB();
      }
    }
    STAMP(5); FS(21);
    u32 pq[2][9];
    feature_map2(wa, wb, acc[0], acc[1], acc[2], pq);
    LANEVARS;
    u32 oa[3];
    {
      const bool same = (q4 >> 1) == (g >> 1);
#pragma unroll
      for (int t = 0; t < 3; ++t) {
        const u32 slot = 4 * t + p4;
        const u32 cc = slot < 9 ? 9 * (q4 & 1) + slot : 18;
        oa[t] = same ? L_KV + ((hp * 2 + (g >> 1)) * 19 + cc) * 96 + (g & 1) * 48 : L_ZERO;
        pin(oa[t]);
      }
    }
    const char* w1b = P.w1 + (size_t)(kp * 1024 + w * 4) * 9216 + ni * 256;
    u32 w1off = g * 9216 + ((((l15 >> 1) ^ (g | ((w & 2) << 1))) << 1) | (l15 & 1)) * 16, lane16 = lane * 16;
    T1W_DEF;
    auto issue_w1 = [&](int k0, int u0, int n) __attribute__((always_inline)) {
#pragma unroll
      for (int j = 0; j < n; ++j) {
        u32 r = j + ROT1 * ((mi * n) / 3); if (r >= (u32)n) r -= n;
        const u32 u = u0 + r;
        const u32 lds = (u < 6 ? 2 * SLOT + u * 8192 : (u < 9 ? (u - 6) * 8192 : (u < 12 ? SLOT + (u - 9) * 8192 : 2 * SLOT + (u - 6) * 8192))) + w * 1024;
        const char* b = w1b + (size_t)(k0 + r) * (32 * 9216);
        dma_w1(b, w1off, lds);
        dma_w1(b + 16 * 9216, w1off, lds + 4096);
      }
    };
    pin(w1off); pin(lane16);
#pragma unroll
    for (int m = 0; m < 2; ++m)
#pragma unroll
      for (int i = 0; i < 9; ++i) pin(pq[m][i]);
    STAMP(6); FS(22);
    bar();
    STAMP(7); FS(23);
    {
      u32x4 k0[3], k1[3]; u32x2 k2[3];
#pragma unroll
      for (int t = 2; t >= 0; --t) { k0[t] = lds_r128(oa[t]); k1[t] = lds_r128(oa[t] + 16); k2[t] = lds_r64(oa[t] + 32); }
      SB();
      f32x4 nu[2][3];
#pragma unroll
      for (int t = 2; t >= 0; --t)
#pragma unroll
        for (int m = 0; m < 2; ++m) {
          f32x4 c = mfma32(k0[t], u32x4{pq[m][0], pq[m][1], pq[m][2], pq[m][3]}, f32x4{0.f, 0.f, 0.f, 0.f});
          c = mfma32(k1[t], u32x4{pq[m][4], pq[m][5], pq[m][6], pq[m][7]}, c);
          nu[m][t] = mfma16(k2[t], u32x2{pq[m][8], 0u}, c);
        }
      char* ob = ws + W_O + (rb * 4 + mp * 2) * 18432;
#pragma unroll
      for (int m = 0; m < 2; ++m) {
        const float inv = __builtin_amdgcn_rcpf(nu[m][2].y + 1e-6f);
        const u32x2 a = pack4(nu[m][0] * inv), b = pack4(nu[m][1] * inv);
        st128_sc1(ob + m * 18432 + (cg * 2 + hp) * 1024, lane16, u32x4{a.x, a.y, b.x, b.y});
        const u32 e0 = asu(nu[m][2].x * inv);
        auto t16 = __builtin_amdgcn_permlane16_swap(e0, e0, false, false);
        const u32 e01 = pack2(asf(t16[0]), asf(t16[1]));
        auto t32 = __builtin_amdgcn_permlane32_swap(e01, e01, false, false);
        asm volatile("s_mov_b64 exec, 0xffff\n\tglobal_store_dwordx2 %0, %1, %2 sc1\n\ts_mov_b64 exec, -1" ::"v"(lane16), "v"(u32x2{t32[0], t32[1]}), "s"(ob + m * 18432 + 16384 + cg * 256 + hp * 8) : "memory");
      }
    }
    FS(24);
    cnt_add(9);
    waitv<0>();
    st_flag(ws + W_FLAGB + ((rb * 2 + mp) * 16 + cg * 2 + hp) * 128, tag);
    STAMP(8); FS(25);
    cnt_wait(6);
    FS(42);
    issue_w1(0, 0, 9);
    sync4<0>(0);
#pragma unroll
    for (int ks = 0; ks < 9; ++ks) grab1(t1w, t1x, ks, ks);
    sync4<63>(1);
    FS(44);
    issue_w1(9, 0, 9);
    sync4<0>(2);
#pragma unroll
    for (int ks = 9; ks < 16; ++ks) grab1(t1w, t1x, ks, ks - 9);
    sync4<63>(3);
    FS(43);
    cnt_wait(7);
    issue_w1(18, 0, 7);
    issue_w1(25, 9, 7);
    FS(38);
    bar_vo<0>();
#pragma unroll
    for (int j = 0; j < 3; ++j) { const u32 it = min(j * 64 + lane, 143u); hrow[j] = lds_r128(L_HI + (w * 16 + it / 9) * 160 + (it % 9) * 16); }
#pragma unroll
    for (int j = 0; j < 3; ++j) {
      const u32 it = min(j * 64 + lane, 143u);
      st128_sc1(ws + W_H + (rb * 64 + w * 16) * 1152 + cg * 144, (it / 9) * 1152 + (it % 9) * 16, hrow[j]);
    }
    FS(40);
    bar();
  } else {
    LANEVARS;
    const u32 lane16 = lane * 16;
    u32 pfd = 0;
    asm volatile("global_load_dword %0, %1, %2" : "+v"(pfd) : "v"(min(((idx * 4 + w3) * 4 + p4) * 128u, len)), "s"(pc) : "memory");
    load_x();
    RING_DEF;
    u32 wp[3];
    {
      const u32 so = (min(lane, 59u) / 10) * 36 + min(lane % 10, 8u) * 4;
#pragma unroll
      for (int j = 0; j < 3; ++j) ld32(wp[j], P.wphi + cg * 2592 + w3 * 648 + j * 216, so);
    }
    issue1(0, 0, 8);
    WAIT_X(8);
    FS(2);
    pin(wp[0]); pin(wp[1]); pin(wp[2]);
    layer_norm();
    if (lane < 60) {
#pragma unroll
      for (int j = 0; j < 3; ++j) {
        *(LAS u32*)(L_WPHI + w3 * 720 + j * 240 + lane * 4) = wp[j];
        *(LAS u32*)(L_WPHI + 2880 + w3 * 720 + j * 240 + lane * 4) = wp[j] ^ 0x80008000u;
      }
    }
    FS(4);
    asm volatile("s_waitcnt vmcnt(0) lgkmcnt(0)\n\ts_barrier\n\ts_add_u32 %0, %1, 1" : "=s"(tag) : "s"(tag0) : "memory", "scc");
    issue1(2, 0, 8);
    FS(6);
    bar_vo<8>();
    issue1(3, 0, 8);
    FS(8);
    bar_vo<8>();
    issue1(4, 0, 8);
    FS(10);
    bar_vo<8>();
    issue1(5, 0, 8);
    FS(12);
    bar_vo<8>();
    {
      const u32 ln = min((idx * 4 + w3) * 23 + min(lane, 22u), 2879u);
      touch(pfd, P.wo + (xcd & 1) * 512, (ln / 5) * 1152 + (ln % 5) * 128);
    }
    issue1(6, 0, 4);
    FS(14);
    bar_vo<5>();
    issue1(6, 4, 8);
    issue1(7, 0, 8);
    const u32 srow = 8 * g + q4;
    u32 e_vb0 = L_V + srow * 152 + (w3 >> 1) * 72 + ((w3 & 1) ? 40 : 0) + p4 * 8, e_vb1 = L_V + srow * 152 + (w3 >> 1) * 72 + 32;
    u32 e_pb = L_PHI + srow * 328 + w3 * 80 + p4 * 8, e_pb2 = L_PHI + srow * 328 + w3 * 80 + 64 + (p4 & 1) * 8;
    u32 e_s0 = (lane >> 3) * 4096 + (lane & 7) * 16, e_s1[3];
    const u32 colp = l15 == 4 ? 2u : l15 - 2 * (w3 & 1);
#pragma unroll
    for (int i = 0; i < 3; ++i) {
      const u32 sidx = (160 + min(colp, 2u) * 10 + 4 * i + g);
      e_s1[i] = (sidx >> 3) * 4096 + (sidx & 7) * 16;
      pin(e_s1[i]);
    }
    const u64 e_m01 = __ballot(l15 == 4 || colp < 2), e_m2 = __ballot(l15 == 4 || (colp < 2 && g < 2));
    pin(e_vb0); pin(e_vb1); pin(e_pb); pin(e_pb2); pin(e_s0);
    issue1(8, 0, 8);
#pragma unroll
    for (int i = 0; i < 4; ++i) { voff[i] = (9 * (wrow[i] >> 3) + (wrow[i] & 7) + seg * 324) * 1152 + ch9 * 16; pin(voff[i]); }
    const char* wob = P.wo + cg * 144;
    FS(17);
    bar();
    FS(18);
    {
      u32x4 vf[2][2], pf_[2][3];
#pragma unroll
      for (int ks = 0; ks < 2; ++ks) {
        vf[ks][0] = lds_tr2(e_vb0 + ks * (32 * 152), e_vb0 + ks * (32 * 152) + 4 * 152);
        pf_[ks][0] = lds_tr2(e_pb + ks * (32 * 328), e_pb + ks * (32 * 328) + 4 * 328);
        pf_[ks][1] = lds_tr2(e_pb + ks * (32 * 328) + 32, e_pb + ks * (32 * 328) + 32 + 4 * 328);
        pf_[ks][2] = lds_tr2(e_pb2 + ks * (32 * 328), e_pb2 + ks * (32 * 328) + 4 * 328);
      }
#pragma unroll
      for (int ks = 0; ks < 2; ++ks) vf[ks][1] = lds_tr2(e_vb1 + ks * (32 * 152), e_vb1 + ks * (32 * 152) + 4 * 152);
      SB();
      f32x4 kv[2][3];
#pragma unroll
      for (int vt = 0; vt < 2; ++vt)
#pragma unroll
        for (int ks = 0; ks < 2; ++ks) {
          if (vt && l15 >= 4) vf[ks][1] = u32x4{0x3f803f80u, 0x3f803f80u, 0x3f803f80u, 0x3f803f80u};
#pragma unroll
          for (int i = 0; i < 3; ++i) kv[vt][i] = mfma32(pf_[ks][i], vf[ks][vt], ks ? kv[vt][i] : f32x4{0.f, 0.f, 0.f, 0.f});
        }
      char* kb = ws + W_KVPART + ((cg * 96 + w3 * 24) * 32 + rb) * 128;
#pragma unroll
      for (int i = 0; i < 3; ++i) {
        const u32x2 pk = pack4(kv[0][i]);
        st128_sc1_m(kb + i * 32768, e_s0, u32x4{pk.x, tag, pk.y, tag}, i < 2 ? ~0ull : 0xffffffffull);
      }
#pragma unroll
      for (int i = 0; i < 3; ++i) {
        const u32x2 pk = pack4(kv[1][i]);
        st128_sc1_m(kb, e_s1[i], u32x4{pk.x, tag, pk.y, tag}, i < 2 ? e_m01 : e_m2);
      }
    }
    FS(19);
    bar_v<6>();
#pragma unroll
    for (int j = 0; j < 8; ++j) dma<54, false>(wob + (j >> 2) * (54 * 1152), voff[j & 3], wl, (j >> 2) * 13824 + (j & 3) * 864);
    u32x2 xres[5];
    {
      const u32 xo = (rb * 64 + w3 * 16 + l15) * 1152 + cg * 144;
#pragma unroll
      for (int n = 0; n < 4; ++n) ld64(xres[n], P.x, xo + g * 8, n * 32);
      ld64(xres[4], P.x, xo + (g & 1) * 8, 128);
    }
    u32 la[3], vo2[8];
    {
#pragma unroll
      for (int a = 0; a < 3; ++a) {
        const u32 r = a * 64 + lane;
        const u32 rr = r - 160, cp = (rr * 205) >> 11;
        const u32 cc = r < 160 ? (r & 15) + 2 * (w3 & 1) : (cp >= 2 ? 18 : ((w3 & 1) ? cp : 16 + cp));
        const u32 piece = r < 160 ? (r >> 4) : rr - cp * 10;
        la[a] = r < 190 ? L_KV + (w3 * 19 + cc) * 96 + piece * 8 + (piece >= 5 ? 8 : 0) : L_KV + 40;
        pin(la[a]);
      }
#pragma unroll
      for (int j = 0; j < 8; ++j) {
        const u32 kap = 192 + (j >> 2) * 48 + wrow[j & 3] + seg * 288;
        vo2[j] = (kap < 512 ? 9 * (kap >> 3) + (kap & 7) : 9 * (kap - 512) + 8) * 1152 + ch9 * 16;
        pin(vo2[j]);
      }
    }
    FS(26);
    if (w3 < 3) {
      const char* pb = ws + W_KVPART + (cg * 96 + rb * 3 + w3) * 4096;
      f32x4 sum;
      for (u32 spins = 0; spins < SPIN_CAP; ++spins) {
        u32x4 v[4];
#pragma unroll
        for (int i = 0; i < 4; ++i) ld128_sc1(v[i], pb, lane16, i * 1024);
        waitv<0>();
        u32 bad = 0;
        sum = f32x4{0.f, 0.f, 0.f, 0.f};
#pragma unroll
        for (int i = 0; i < 4; ++i) {
          pin(v[i]);
          bad |= (v[i].y ^ tag) | (v[i].w ^ tag);
          sum += unpk(v[i].x, v[i].z);
        }
        pin(bad);
        if (__all(bad == 0)) break;
      }
#pragma unroll
      for (int j = 0; j < 4; ++j) sum[j] = sum_g(sum[j] + dpp<0x128>(sum[j]));
      const u32x2 pk = pack4(sum);
      st128_sc1_m(ws + W_KVFIN + (cg * 96 + rb * 3 + w3) * 128, lane16, u32x4{pk.x, tag, pk.y, tag}, 0xffull);
      cnt_add(8);
    } else {
      cnt_add(8); cnt_wait(8);
    }
    FS(27);
#pragma unroll
    for (int j = 0; j < 8; ++j) dma<54, false>(wob + (108 + (j >> 2) * 54) * 1152, voff[j & 3], wl, 4 * SLOT + (j >> 2) * 13824 + (j & 3) * 864);
    {
      FS(28);
      const char* fb = ws + W_KVFIN + (cg * 4 + w3) * 3072;
      u32x4 v[3];
      for (u32 spins = 0; spins < SPIN_CAP; ++spins) {
#pragma unroll
        for (int i = 0; i < 3; ++i) ld128_sc1(v[i], fb, lane16, i * 1024);
        waitv<0>();
        u32 bad = 0;
#pragma unroll
        for (int i = 0; i < 3; ++i) { pin(v[i]); bad |= (v[i].y ^ tag) | (v[i].w ^ tag); }
        pin(bad);
        if (__all(bad == 0)) break;
      }
#pragma unroll
      for (int i = 0; i < 3; ++i) lds_w64(la[i], u32x2{v[i].x, v[i].z});
    }
    FS(22);
    bar();
#pragma unroll
    for (int j = 0; j < 8; ++j) dma<54, false>(wob, vo2[j], wl, SLOT + (j >> 2) * 13824 + (j & 3) * 864);
    const LAddr wo0(trrow + p4 * 8), wo4(trrow + 128 + (p4 & 1) * 8);
    auto rdw = [&](u32 b, int n) __attribute__((always_inline)) -> u32x4 {
      return n < 4 ? lds_tr2(wo0(b + 32 * n), wo0(b + 32 * n + 2304)) : lds_tr2(wo4(b), wo4(b + 2304));
    };
    u32x4 wm[30];
#pragma unroll
    for (int u = 0; u < 30; ++u) wm[u] = rdw(((u / 5) % 3) * 9216 + (u / 15) * 144, u % 5);
    sync4<0>(5);
    f32x4 hc[5];
#pragma unroll
    for (int n = 0; n < 5; ++n) { pin(xres[n]); hc[n] = unpk(xres[n].x, xres[n].y); pin(hc[n]); }
    {
      u32x4 F[6];
      auto rd = [&](int u) __attribute__((always_inline)) {
        const int v = u / 5;
        F[u % 6] = rdw((v < 6 ? 4 * SLOT : SLOT) + (v % 3) * 9216 + ((v / 3) & 1) * 144, u % 5);
      };
#pragma unroll
      for (int u = 0; u < 5; ++u) rd(u);
      FS(29);
      poll_flags(ws + W_FLAGB + (rb * 2 + (w3 >> 1)) * 2048, l15 * 128, tag);
      STAMP(9); FS(30);
      constexpr int ord[18] = {3, 4, 5, 12, 13, 14, 6, 7, 8, 15, 16, 17, 0, 1, 2, 9, 10, 11};
      u32x4 of[18];
      const char* ob = ws + W_O + (rb * 4 + w3) * 18432;
#pragma unroll
      for (int j = 0; j < 18; ++j) ld128_sc1(of[j], ob + (ord[j] >> 2) * 4096, lane16, (ord[j] & 3) * 1024);
      cnt_add(6);
      FS(31);
#pragma unroll
      for (int j = 0; j < 18; ++j) {
        asm volatile("s_waitcnt vmcnt(%1)" : "+v"(of[j]) : "n"(17 - j) : "memory");
#pragma unroll
        for (int n = 0; n < 5; ++n) {
          const int u = j * 5 + n;
          if (u + 5 < 60) rd(u + 5);
          hc[n] = mfma32(u < 60 ? F[u % 6] : wm[u - 60], of[j], hc[n]);
          SB();
        }
      }
    }
    STAMP(10); FS(32);
    float s1 = 0.f, s2 = 0.f;
    u32x2 hr[5];
#pragma unroll
    for (int n = 0; n < 5; ++n) {
      hr[n] = pack4(hc[n]);
      if (n == 4 && g >= 2) hr[n] = u32x2{0u, 0u};
      s1 = dot2(hr[n].x, 0x3f803f80u, s1); s1 = dot2(hr[n].y, 0x3f803f80u, s1);
      s2 = dot2(hr[n].x, hr[n].x, s2); s2 = dot2(hr[n].y, hr[n].y, s2);
    }
    s1 = sum_g(s1); s2 = sum_g(s2);
    char* sb = ws + W_STATS + (rb * 4 + w3) * 2048;
    st128_sc1_m(sb + cg * 256, lane16, u32x4{asu(s1), tag, asu(s2), tag}, 0xffffull);
    FS(34);
    cnt_wait(9);
    u32 hw = L_HI + (w3 * 16 + l15) * 160 + g * 8;
#pragma unroll
    for (int n = 0; n < 5; ++n) {
      lds_w64(hw + n * 32, hr[n]);
      hc[n] = unpk(hr[n].x, hr[n].y);
      pin(hc[n]);
    }
    u32 hrd = L_HN + (w3 * 16 + 8 * (g & 1) + q4) * 160 + ((g >> 1) * 16 + p4 * 4) * 2;
    u32 hrx = L_HN + (w3 * 16 + 8 * (g & 1) + q4) * 160 + (64 + (p4 & 1) * 4) * 2;
    u32 ho[2];
#pragma unroll
    for (int j = 0; j < 2; ++j) {
      const u32 d = cg * 72 + (j ? 64 + (l15 & 7) : (g >> 1) * 16 + l15);
      ho[j] = (d >> 5) * 131072 + (g & 1) * 512 + (d & 31) * 16;
      pin(ho[j]);
    }
    T1W_DEF;
    pin(hw); pin(hrd); pin(hrx);
    char* hb = ws + W_HN + (rb * 4 + w3) * 1024;
    u32x4 v0, v1;
    FS(35);
    for (u32 spins = 0; spins < SPIN_CAP; ++spins) {
      ld128_sc1(v0, sb, lane16);
      ld128_sc1(v1, sb, lane16, 1024);
      waitv<0>();
      pin(v0); pin(v1);
      u32 bad = (v0.y ^ tag) | (v0.w ^ tag) | (v1.y ^ tag) | (v1.w ^ tag);
      pin(bad);
      if (__all(bad == 0)) break;
    }
    pin(pfd);
    cnt_add(7);
    FS(36);
    s1 = sum_g(asf(v0.x) + asf(v1.x));
    s2 = sum_g(asf(v0.z) + asf(v1.z));
    const float mean = s1 * (1.f / 576.f);
    const float var = fmaxf(s2 * (1.f / 576.f) - mean * mean, 0.f);
    const float r1 = __builtin_amdgcn_rsqf(var + 1e-5f);
    const float r2 = r1 * __builtin_amdgcn_rsqf(var * r1 * r1 + 1e-5f);
    const float nb = -mean * r2;
#pragma unroll
    for (int n = 0; n < 5; ++n) lds_w64(hw + 10240 + n * 32, pack4(hc[n] * r2 + nb));
    const u32x4 f0 = lds_tr2(hrd, hrd + 4 * 160), f1 = lds_tr2(hrd + 64, hrd + 64 + 4 * 160), f2 = lds_tr2(hrx, hrx + 4 * 160);
    STAMP(11); FS(37);
    st128_sc1(hb, ho[0], f0);
    st128_sc1(hb + 131072, ho[0], f1);
    st128_sc1_m(hb, ho[1], f2, 0x00ff00ffull);
    sync4<0>(10);
    if (w == 4) st_flag(ws + W_FLAGD + (cg * 32 + rb) * 128, tag);
    FS(38);
    asm volatile("s_barrier" ::: "memory");
#pragma unroll
    for (int ks = 0; ks < 16; ++ks) grab1(t1w, t1x, ks, ks < 2 ? ks + 7 : (ks < 9 ? ks - 2 : ks));
    FS(40);
    bar_v<0>();
    asm volatile(".Lfk_copy_end:");
  }
  STAMP(12); FS(39);
  LANEVARS;
  const u32 lane16 = lane * 16;
  const u32 wcol = ((((l15 >> 1) ^ ((w * 4 + g) & 7)) << 1) | (l15 & 1)) * 16;

  const u32 Mq = idx & 3, Nq = kp * 8 + (idx >> 2), odd = Mq & 1;
  const char* w2b = P.w2 + (size_t)(kq * 1152 + w * 4) * 4096 + Nq * 256;
  const u32 w2off = g * 4096 + wcol;
  u32 w1k = w * 1024;
  asm volatile("" : "+s"(w1k));
  auto issue_w2 = [&](int ps, u32 unit) __attribute__((always_inline)) {
    const char* b = w2b + (ps < 9 ? ps + 9 * odd : ps - 9 * odd) * (32 * 4096);
    dma<64, false>(b, w2off, w1k, unit * 16384);
    dma<64, false>(b + 18 * (32 * 4096), w2off, w1k, unit * 16384 + 8192);
  };
  u32 tw2;
  { LANEVARS; tw2 = kh * 8192 + (4 * g + q4) * 256 + (((2 * w3 + kh) ^ (4 * (g & 1) + q4)) * 32) + p4 * 8; }
#define GRAB2_DEF \
  const LAddr t2w(tw2), t2x(tw2 ^ 32); \
  auto grab = [&](int ps, u32 unit) __attribute__((always_inline)) { \
    const u32 sl = unit * 16384; \
    wf[2 * ps] = lds_tr2(t2w(sl), t2w(sl + 4096)); \
    wf[2 * ps + 1] = lds_tr2(t2x(sl), t2x(sl + 4096)); \
  }
  const f32x4 zero4 = {0.f, 0.f, 0.f, 0.f};
  if (g1) {
    const u32 ord = mi == 0 ? 0x321504u : (mi == 1 ? 0x251403u : 0x504321u);
    auto tile_of = [&](int t) __attribute__((always_inline)) -> u32 {
      const u32 pos = t < 3 ? 2 * t + (kp ^ 1) : 2 * (t - 3) + kp;
      return mi * 6 + ((ord >> (4 * pos)) & 15u);
    };
    const char* hb = ws + W_HN + kp * 65536;
    u32x2 bias[2];
    const u32 f16 = ni * 4 + w3 + kh * 144;
    ld64(bias[0], P.b1 + (ni * 4 + w3) * 64, g * 8);
    ld64(bias[1], P.b1 + (ni * 4 + w3) * 64, g * 8, 32);
    {
      const u32 cj = min((mi * 192) / 72 + ((lane >> 1) & 3), 7u);
      poll_flags(ws + W_FLAGD, (cj * 32 + kp * 16 + 2 * w + (lane & 1)) * 128, tag);
      FS(52);
      const char* tb = hb + tile_of(0) * 131072 + w * 8192;
      const u32 lb = ((w >> 1) & 1 ? 0u : 3 * 32768u) + (w >> 2) * 16384 + (w & 1) * 8192;
#pragma unroll
      for (int j = 0; j < 8; ++j) dma<64, true>(tb + j * 1024, lane16, lb, j * 1024);
    }
    STAMP(13); FS(54);
    bar_v<0>();
    if (w == 0) st_flag(ws + W_FLAGH + (rb * 8 + cg) * 128, tag);
    FS(55);
    auto issue_a = [&](int i, int j) __attribute__((always_inline)) {
      dma<64, true>(hb + tile_of(i >> 1) * 131072 + w1k + ((j >> 1) * 32 + (i & 1) * 16 + (j & 1) * 8) * 1024, lane16, w1k, ((i + 3) & 3) * 32768 + j * 8192);
    };
    const u32 aa = kh * (16384 + 256) + (g >> 1) * 1024 + (g & 1) * 512 + l15 * 16;
    const LAddr ar0(aa), ar1(aa ^ 256);
    u32 exw = 9 * 16384 + w * 1024 + lane16, exr = 9 * 16384 + (w ^ 4) * 1024 + lane16;
    pin(exw); pin(exr);
    f32x4 cc[2][2][2], ci[2];
    f32x4 kv0, kv1;
    u32x4 a[6], rx, pv[2], uo;
    pin(bias[0]); pin(bias[1]);
    auto set_init = [&](f32x4 lo, f32x4 hi) __attribute__((always_inline)) {
      ci[0] = lo; ci[1] = hi;
      pin(ci[0]); pin(ci[1]);
    };
    set_init(unpk(bias[0].x, bias[0].y), unpk(bias[1].x, bias[1].y));
    auto rd = [&](int n) __attribute__((always_inline)) {
      const u32 o = (((n >> 4) + 3) & 3) * 32768 + (n & 14) * 1024;
      a[n % 6] = lds_r128((n & 1) ? ar1(o) : ar0(o));
    };
    auto load_pv = [&](int t) __attribute__((always_inline)) {
      const char* pb = ws + W_UP + (tile_of(t) * 288 + f16) * 2048;
      ld128_sc1(pv[0], pb, lane16);
      ld128_sc1(pv[1], pb, lane16, 1024);
    };
    auto flag_u = [&](int t) __attribute__((always_inline)) {
      if (w == 0) st_flag(ws + W_FLAGE + (tile_of(t) * 36 + ni) * 128, tag);
    };
    auto exch_p = [&](int t) __attribute__((always_inline)) {
      const u32x2 p2 = pack4(cc[t & 1][1][0]), p3 = pack4(cc[t & 1][1][1]);
      rx = u32x4{p2.x, p2.y, p3.x, p3.y};
      pin(rx);
    };
    auto combine = [&](int t) __attribute__((always_inline)) {
      kv0 = cc[t & 1][0][0] + unpk(rx.x, rx.y);
      kv1 = cc[t & 1][0][1] + unpk(rx.z, rx.w);
      pin(kv0); pin(kv1);
    };
    auto send = [&](int t, int h) __attribute__((always_inline)) {
      const u32x2 p0 = pack4(h ? kv1 : kv0);
      st128_sc1(ws + W_UP + (tile_of(t) * 288 + f16) * 2048 + h * 1024, lane16, u32x4{p0.x, tag, p0.y, tag});
    };
    auto gather = [&](int t) __attribute__((always_inline)) {
      for (u32 spins = 0; spins < SPIN_CAP; ++spins) {
        pin(pv[0]); pin(pv[1]);
        u32 bad = (pv[0].y ^ tag) | (pv[0].w ^ tag) | (pv[1].y ^ tag) | (pv[1].w ^ tag);
        pin(bad);
        if (__builtin_expect(__all(bad == 0), 1)) break;
        load_pv(t);
        waitv<0>();
      }
      pin(pv[0]); pin(pv[1]);
      set_init(unpk(pv[0].x, pv[0].z), unpk(pv[1].x, pv[1].z));
    };
    auto act = [&](int j) __attribute__((always_inline)) {
      const f32x4 v = j < 2 ? kv0 : kv1;
      u32 t = (j & 1) ? pack2(gelu_tanh(v.z), gelu_tanh(v.w)) : pack2(gelu_tanh(v.x), gelu_tanh(v.y));
      pin(t);
      uo[j] = t;
    };
    auto store_u = [&](int t) __attribute__((always_inline)) { st128_sc1(ws + W_U + (tile_of(t) * 288 + f16) * 1024, lane16, uo); };
#pragma unroll
    for (int n = 0; n < 5; ++n) rd(n);
    SB();
#pragma unroll
    for (int i = 0; i < 12; ++i)
#pragma unroll
      for (int k = 0; k < 16; ++k) {
        const int n = i * 16 + k, t = i >> 1;
        if (n + 5 < 192) rd(n + 5);
#pragma unroll
        for (int e = 0; e < 2; ++e)
          cc[t & 1][n & 1][e] = mfma32(wf[(n & 30) + e], a[n % 6], (n & 30) ? cc[t & 1][n & 1][e] : ((n & 1) ? zero4 : ci[e]));
        SB();
        if (i == 0 && k < 4) issue_a(2, k);
        if (!(i & 1) && i > 0 && k == 2) exch_p(t - 1);
        if ((i & 1) && i > 1) {
          if (i < 9) { if (k == 1 || k == 4) send(t - 1, k >> 2); }
          else { if (k < 8 && !(k & 1)) act(k >> 1); if (k == 7) store_u(t - 1); }
          if (i >= 5 && i < 11 && k == 6) load_pv(t + 1);
        }
        if (k == PRIO_K1) PRIO(0);
        if (k == 8) {
          constexpr int vm[12] = {4, 4, 4, 6, 6, 8, 8, 8, 8, 9, 4, 63};
          if (!(i & 1) && i > 0) { lds_w128(exw, rx); asm volatile("s_waitcnt lgkmcnt(0)" ::: "memory"); }
          asm volatile("s_waitcnt vmcnt(%0)\n\ts_barrier" ::"n"(vm[i]) : "memory");
          PRIO(1);
          if (i == 10) flag_u(3);
          if (!(i & 1) && i > 0) rx = lds_r128(exr);
        }
        if (k >= 9 && k < 13) {
          if (i + 3 < 12) issue_a(i + 3, k - 9);
          else if (k & 1) issue_w2(2 * (i - 9) + 1 + ((k - 9) >> 1), ((i + 2) & 3) * 2 + ((k - 9) >> 1));
        }
        if (!(i & 1) && i > 0 && k == 14) combine(t - 1);
        if ((i & 1) && i >= 5 && i < 11 && k == 14) { if (i == 9) waitv<5>(); else waitv<4>(); gather(t + 1); }
        if (i == 8 && k == 13) issue_w2(0, 8);
        SB();
      }
    PRIO(0);
    STAMP(14); FS(57);
    GRAB2_DEF;
    exch_p(5);
    lds_w128(exw, rx);
    bar_v<4>();
    rx = lds_r128(exr);
    flag_u(4);
    issue_w2(7, 4); issue_w2(8, 5);
    combine(5);
#pragma unroll
    for (int j = 0; j < 4; ++j) act(j);
    store_u(5);
    grab(0, 8); grab(1, 6); grab(2, 7); grab(3, 0); grab(4, 1);
    bar_v<5>();
    {
      constexpr u32 un[6] = {8, 6, 7, 0, 1, 9};
#pragma unroll
      for (int ps = 9; ps < 15; ++ps) issue_w2(ps, un[ps - 9]);
    }
    grab(5, 2); grab(6, 3);
    bar_v<12>();
    flag_u(5);
  } else {
    if (w == 0) { poll_flags(ws + W_FLAGD, lane * 128, tag); __builtin_amdgcn_s_sleep(16); }
    bar();
    {
      const u32 part = (idx - 27) * 8 + w;
      const char* pb = P.w2 + (size_t)kq * 1152 * 4096 + kp * 2048;
      u32 d[8];
#pragma unroll
      for (int j = 0; j < 8; ++j)
        asm volatile("global_load_dword %0, %1, %2" POL_S : "=v"(d[j]) : "v"(min((j * 40 + part) * 4 + g, 1151u) * 4096 + l15 * 128), "s"(pb) : "memory");
      asm volatile("s_waitcnt vmcnt(0)" : "+v"(d[0]), "+v"(d[1]), "+v"(d[2]), "+v"(d[3]), "+v"(d[4]), "+v"(d[5]), "+v"(d[6]), "+v"(d[7])::"memory");
    }
    GRAB2_DEF;
    constexpr u32 un0[9] = {8, 6, 7, 0, 1, 2, 3, 4, 5};
    bar();
    if (w == 0) st_flag(ws + W_FLAGH + (rb * 8 + cg) * 128, tag);
#pragma unroll
    for (int ps = 0; ps < 9; ++ps) issue_w2(ps, un0[ps]);
    bar_v<0>();
    grab(0, 8); grab(1, 6); grab(2, 7); grab(3, 0); grab(4, 1);
    bar();
    {
      constexpr u32 un[6] = {8, 6, 7, 0, 1, 9};
#pragma unroll
      for (int ps = 9; ps < 15; ++ps) issue_w2(ps, un[ps - 9]);
    }
    grab(5, 2); grab(6, 3);
    bar();
  }
  STAMP(15); FS(58);

  {
    const u32 T0 = 9 * (Mq >> 1), TF = T0 + 5 * odd;
    auto owner_of = [&](int b) __attribute__((always_inline)) -> u32 { return b == 4 ? kq : (u32)(b - 1) + ((u32)(b - 1) >= kq ? 1u : 0u); };
    GRAB2_DEF;
    issue_w2(15, 2); issue_w2(16, 3);
    grab(7, 4); grab(8, 5);
    u32 fo;
    {
      const u32 b = min(lane / 9, 4u);
      fo = lane < 60 ? ((T0 + 4 * odd + b) * 36 + kq * 9 + lane % 9) * 128 : (W_FLAGH - W_FLAGE) + ((Nq * 2 + (lane & 1)) * 8 + Mq * 2 + ((lane >> 1) & 1)) * 128;
      if (lane == 58 || lane == 59) fo = (W_FLAGH - W_FLAGE) + ((Nq * 2 + (lane & 1)) * 8 + (Mq >> 1) * 4 + 2 - odd) * 128;
    }
    bar_v<4>();
    issue_w2(17, 4);
    {
      constexpr u32 un[6] = {8, 6, 7, 0, 1, 9};
#pragma unroll
      for (int ps = 9; ps < 15; ++ps) grab(ps, un[ps - 9]);
    }
    if (w == 0) poll_flags(ws + W_FLAGE, fo, tag);
    bar_v<0>();
    const char* ub = ws + W_U + kq * (36 * 1024);
    u32 uo_[5], lx = (32 + w3) * 1024;
#pragma unroll
    for (int j = 0; j < 5; ++j) {
      const u32 b = j < 4 ? 8 * j + w : 32 + w3;
      uo_[j] = (((b / 9) & 1) * 144 + (b / 18) * 18 + b % 9) * 1024;
      asm volatile("" : "+s"(uo_[j]));
    }
    asm volatile("" : "+s"(lx));
    auto issue_u = [&](int i, int j) __attribute__((always_inline)) {
      const u32 T = i == 0 ? T0 + 4 : TF + owner_of((i + 1) >> 1);
      const u32 part = (i == 0 ? 0u : (u32)((i + 1) & 1)) ^ odd;
      const char* b = ub + (T * 288 + part * 9) * 1024 + uo_[j];
      if (j < 4) dma<64, true>(b, lane16, w1k, ((i + 3) & 3) * 36864 + j * 8192);
      else dma<64, true>(b, lane16, lx, ((i + 3) & 3) * 36864);
    };
#pragma unroll
    for (int j = 0; j < 5; ++j) issue_u(0, j);
    grab(15, 2); grab(16, 3); grab(17, 4);
    bar();
    STAMP(16); FS(59);
#pragma unroll
    for (int j = 0; j < 5; ++j) issue_u(1, j);
    const u32 grp = Mq * 16 + Nq, me = odd * 4 + kq;
    u32 exw = 147456 + w * 1024 + lane16, exr = 147456 + (w ^ 4) * 1024 + lane16;
    pin(exw); pin(exr);
    const LAddr ar(kh * 18432 + lane16);
    f32x4 cc[2][2][2];
    f32x4 kv0, kv1;
    u32x4 a[6], rx, pv[6];
    u32x2 hres[2]; u32 bb;
    char* ysb = ws + W_YS + (((Mq >> 1) * 16 + Nq) * 8 + w) * 16384;
    auto rd = [&](int n) __attribute__((always_inline)) { a[n % 6] = lds_r128(ar(((n / 18 + 3) & 3) * 36864 + (n & 1) * 9216 + ((n % 18) >> 1) * 1024)); };
    auto exch_p = [&](int b) __attribute__((always_inline)) {
      const u32x2 p2 = pack4(cc[b & 1][0][1]), p3 = pack4(cc[b & 1][1][1]);
      rx = u32x4{p2.x, p2.y, p3.x, p3.y};
      pin(rx);
    };
    auto send = [&](int b, int h) __attribute__((always_inline)) {
      const u32x2 pk = pack4(h ? cc[b & 1][1][0] + unpk(rx.z, rx.w) : cc[b & 1][0][0] + unpk(rx.x, rx.y));
      const u32 o = owner_of(b ? b : 1);
      char* pb = b == 0 ? ysb + me * 2048 : ws + W_YP + (((grp * 4 + o) * 3 + ((kq - o - 1) & 3u)) * 8 + w) * 2048;
      st128_sc1(pb + h * 1024, lane16, u32x4{pk.x, tag, pk.y, tag});
    };
    auto load_res = [&](u32 T, u32 ln) __attribute__((always_inline)) -> u32 {
      const u32 srow = Nq * 128 + w3 * 32 + kh * 16 + (ln & 15), oo = srow * 1152 + T * 64 + (ln >> 4) * 8;
      ld64_sc1(hres[0], ws + W_H, oo);
      ld64_sc1(hres[1], ws + W_H + 32, oo);
      ld16(bb, P.b2, srow * 2);
      return oo;
    };
    u32 so, oo;
    auto load_ps = [&]() __attribute__((always_inline)) { ld128_sc1(pv[0], ysb, so); ld128_sc1(pv[1], ysb, so, 1024); };
    auto load_pv1 = [&](int j) __attribute__((always_inline)) {
      ld128_sc1(pv[j], ws + W_YP + ((grp * 4 + kq) * 3 * 8 + w) * 2048 + (j >> 1) * 16384, lane16, (j & 1) * 1024);
    };
    auto load_pv = [&]() __attribute__((always_inline)) {
#pragma unroll
      for (int j = 0; j < 6; ++j) load_pv1(j);
    };
    bar_vo<5>();
#pragma unroll
    for (int n = 0; n < 5; ++n) rd(n);
    SB();
#pragma unroll
    for (int i = 0; i < 9; ++i)
#pragma unroll
      for (int k = 0; k < 18; ++k) {
        const int n = i * 18 + k, b = (i + 1) >> 1, x = (i == 0 || (i & 1)) ? k : 18 + k;
        if (n + 5 < 162) rd(n + 5);
#pragma unroll
        for (int e = 0; e < 2; ++e)
          cc[b & 1][x & 1][e] = mfma32(a[n % 6], wf[(x & ~1) + e], x > 1 ? cc[b & 1][x & 1][e] : zero4);
        SB();
        if (i == 0 && k < 5) issue_u(2, k);
        if ((i & 1) && k == 2) exch_p(b - 1);
        if (k == PRIO_K2) PRIO(0);
        if (k == 5) {
          constexpr int vm[9] = {5, 5, 7, 5, 7, 10, 7, 4, 63};
          if (i & 1) { lds_w128(exw, rx); asm volatile("s_waitcnt lgkmcnt(0)" ::: "memory"); }
          asm volatile("s_waitcnt vmcnt(%0)\n\ts_barrier" ::"n"(vm[i]) : "memory");
          PRIO(1);
          if (i & 1) rx = lds_r128(exr);
        }
        if ((i & 1) && (k == 8 || k == 10)) send(b - 1, (k - 8) >> 1);
        if (k >= 11 && k < 16 && i + 3 < 9) issue_u(i + 3, k - 11);
        if (i == 4 && k == 6) { so = (lane >> 3) * 2048 + (me * 8 + (lane & 7)) * 16; load_ps(); }
        if (i == 4 && k == 8) oo = load_res(T0 + 4, me * 8 + (lane & 7));
        if (i == 6) {
          if (k == 0) {
            waitv<12>();
            for (u32 spins = 0; spins < SPIN_CAP; ++spins) {
              pin(pv[0]); pin(pv[1]);
              u32 bad = (pv[0].y ^ tag) | (pv[0].w ^ tag) | (pv[1].y ^ tag) | (pv[1].w ^ tag);
              pin(bad);
              if (__builtin_expect(__all(bad == 0), 1)) break;
              load_ps();
              waitv<0>();
            }
            pin(pv[0]); pin(pv[1]);
            kv0 = unpk(pv[0].x, pv[0].z); kv1 = unpk(pv[1].x, pv[1].z);
            pin(kv0); pin(kv1);
          }
          if (k >= 1 && k < 5) { kv0[k - 1] = sum_g(kv0[k - 1] + dpp<0x128>(kv0[k - 1])); pin(kv0); }
          if (k >= 6 && k < 10) { kv1[k - 6] = sum_g(kv1[k - 6] + dpp<0x128>(kv1[k - 6])); pin(kv1); }
          if (k == 11) {
            pin(hres[0]); pin(hres[1]); pin(bb);
            const u32x2 o0 = pack4(kv0 + unpk(hres[0].x, hres[0].y) + bflo(bb)), o1 = pack4(kv1 + unpk(hres[1].x, hres[1].y) + bflo(bb));
            const bool hi = (lane & 8) != 0;
            asm volatile("s_mov_b64 exec, 0xffff\n\tglobal_store_dwordx2 %0, %1, %2\n\ts_mov_b64 exec, -1" ::"v"(oo + (lane & 8) * 4), "v"(hi ? o1 : o0), "s"(P.out) : "memory");
          }
          if (k == 14) oo = load_res(TF + kq, lane);
        }
        if (i == 8 && k == 2) {
          waitv<2>();
          pin(hres[0]); pin(hres[1]); pin(bb);
          kv0 = unpk(hres[0].x, hres[0].y) + bflo(bb); kv1 = unpk(hres[1].x, hres[1].y) + bflo(bb);
          pin(kv0); pin(kv1);
        }
        if (i == 8 && k >= 8 && k < 14) load_pv1(k - 8);
        SB();
      }
    PRIO(0);
    exch_p(4);
    lds_w128(exw, rx);
    bar();
    rx = lds_r128(exr);
    STAMP(17); FS(60);
    for (u32 spins = 0; spins < SPIN_CAP; ++spins) {
      waitv<0>();
      u32 bad = 0;
#pragma unroll
      for (int j = 0; j < 6; ++j) { pin(pv[j]); bad |= (pv[j].y ^ tag) | (pv[j].w ^ tag); }
      pin(bad);
      if (__all(bad == 0)) break;
      load_pv();
    }
    waitv<0>();
#pragma unroll
    for (int j = 0; j < 6; ++j) pin(pv[j]);
#pragma unroll
    for (int s = 0; s < 3; ++s) { kv0 += unpk(pv[2 * s].x, pv[2 * s].z); kv1 += unpk(pv[2 * s + 1].x, pv[2 * s + 1].z); }
    const u32x2 o0 = pack4(kv0 + cc[0][0][0] + unpk(rx.x, rx.y)), o1 = pack4(kv1 + cc[0][1][0] + unpk(rx.z, rx.w));
    auto s0 = __builtin_amdgcn_permlane16_swap(o0.x, o1.x, false, false);
    auto s1 = __builtin_amdgcn_permlane16_swap(o0.y, o1.y, false, false);
    asm volatile("global_store_dwordx4 %0, %1, %2\n\ts_nop 1" ::"v"(oo + (g & 1) * 24), "v"(u32x4{s0[0], s1[0], s0[1], s1[1]}), "s"(P.out) : "memory");
  }
  asm volatile(".Lfk_code_end:");
  if (bid == 0 && w == 0) st_flag(ws + W_EPOCH, tag);
}
}

extern "C" void kernel_launch(void* const* d_in, const int* in_sizes, int n_in, void* d_out, int out_size, void* d_ws,
                              size_t ws_size, hipStream_t stream) {
  Params p;
  p.x = (const char*)d_in[0]; p.wqkv = (const char*)d_in[1]; p.wphi = (const char*)d_in[2]; p.wo = (const char*)d_in[3];
  p.w1 = (const char*)d_in[5]; p.b1 = (const char*)d_in[6]; p.w2 = (const char*)d_in[7]; p.b2 = (const char*)d_in[8];
  p.out = (char*)d_out; p.ws = (char*)d_ws;
  (void)hipFuncSetAttribute((const void*)fused_kernel, hipFuncAttributeMaxDynamicSharedMemorySize, LDS_BYTES);
  fused_kernel<<<NWG, NT, LDS_BYTES, stream>>>(p);
}
```

```cpp
#include <hip/hip_runtime.h>
#include <hip/hip_bf16.h>

typedef unsigned u32;
typedef unsigned long long u64;
typedef u32 u32x2 __attribute__((ext_vector_type(2)));
typedef u32 u32x4 __attribute__((ext_vector_type(4)));
typedef float f32x2 __attribute__((ext_vector_type(2)));
typedef float f32x4 __attribute__((ext_vector_type(4)));
typedef float f32x16 __attribute__((ext_vector_type(16)));
typedef short s16x4 __attribute__((ext_vector_type(4)));
typedef __bf16 bf16x2 __attribute__((ext_vector_type(2)));
typedef __bf16 bf16x8 __attribute__((ext_vector_type(8)));
#define LAS __attribute__((address_space(3)))
#define DEVINL __device__ __forceinline__

namespace {
constexpr int S = 2048, D = 576, F = 4608;
constexpr int NWG = 256, NT = 512;
constexpr int LDS_BYTES = 163840;
constexpr u32 SPIN_CAP = 1u << 16;

constexpr u32 W_EPOCH = 0;
constexpr u32 W_FLAGB = 4096;
constexpr u32 W_FLAGD = W_FLAGB + 1024 * 128;
constexpr u32 W_FLAGE = W_FLAGD + 256 * 128;
constexpr u32 W_FLAGH = W_FLAGE + 18 * 36 * 128;
constexpr u32 W_KVFIN = W_FLAGH + 256 * 128;
constexpr u32 W_STATS = W_KVFIN + 8 * 96 * 128;
constexpr u32 W_KVPART = W_STATS + 32 * 4 * 8 * 256;
constexpr u32 W_O = W_KVPART + 8 * 96 * 32 * 128;
constexpr u32 W_H = W_O + 128 * 18 * 1024;
constexpr u32 W_HN = W_H + S * D * 2;
constexpr u32 W_U = W_HN + 18 * 128 * 1024;
constexpr u32 W_UP = W_U + 18 * 288 * 1024;
constexpr u32 W_YP = W_UP + 18 * 288 * 2048;
constexpr u32 W_YS = W_YP + 64 * 4 * 3 * 8 * 2048;
constexpr u32 W_END = W_YS + 32 * 8 * 8 * 2048;
constexpr u32 W_DBG = W_END;

constexpr u32 SLOT = 27648;
constexpr u32 L_XN = 3 * SLOT;
constexpr u32 L_PHI = 4 * SLOT;
constexpr u32 L_V = L_PHI + 64 * 328;
constexpr u32 L_KV = 147456;
constexpr u32 L_MISC = L_XN + 73728;
constexpr u32 L_WPHI = L_MISC;
constexpr u32 L_ZERO = L_MISC + 5760;
constexpr u32 L_CNT = L_MISC + 5824;
constexpr u32 L_HI = 5 * SLOT;
constexpr u32 L_HN = L_HI + 10240;

struct Params {
  const char* x; const char* wqkv; const char* wphi; const char* wo;
  const char* w1; const char* b1; const char* w2; const char* b2;
  char* out; char* ws;
};

DEVINL f32x4 mfma32(u32x4 a, u32x4 b, f32x4 c) {
  return __builtin_amdgcn_mfma_f32_16x16x32_bf16(__builtin_bit_cast(bf16x8, a), __builtin_bit_cast(bf16x8, b), c, 0, 0, 0);
}
DEVINL f32x16 mfmaB(u32x4 a, u32x4 b, f32x16 c) {
  return __builtin_amdgcn_mfma_f32_32x32x16_bf16(__builtin_bit_cast(bf16x8, a), __builtin_bit_cast(bf16x8, b), c, 0, 0, 0);
}
template <int Q> DEVINL f32x4 quad(const f32x16& v) { return f32x4{v[4 * Q], v[4 * Q + 1], v[4 * Q + 2], v[4 * Q + 3]}; }
DEVINL f32x4 mfma16(u32x2 a, u32x2 b, f32x4 c) {
  return mfma32(u32x4{a.x, a.y, 0u, 0u}, u32x4{b.x, b.y, 0u, 0u}, c);
}
DEVINL u32 pack2(float lo, float hi) {
  f32x2 p = {lo, hi};
  return __builtin_bit_cast(u32, __builtin_convertvector(p, bf16x2));
}
DEVINL u32x2 pack4(f32x4 v) { return u32x2{pack2(v.x, v.y), pack2(v.z, v.w)}; }
DEVINL float bflo(u32 v) { return __builtin_bit_cast(float, v << 16); }
DEVINL float bfhi(u32 v) { return __builtin_bit_cast(float, v & 0xffff0000u); }
DEVINL float asf(u32 v) { return __builtin_bit_cast(float, v); }
DEVINL u32 asu(float v) { return __builtin_bit_cast(u32, v); }
DEVINL f32x4 unpk(u32 a, u32 b) { return f32x4{bflo(a), bfhi(a), bflo(b), bfhi(b)}; }
DEVINL float dot2(u32 a, u32 b, float c) {
  return __builtin_amdgcn_fdot2_f32_bf16(__builtin_bit_cast(bf16x2, a), __builtin_bit_cast(bf16x2, b), c, false);
}
DEVINL u32x2 lds_tr(u32 addr) {
  return __builtin_bit_cast(u32x2, __builtin_amdgcn_ds_read_tr16_b64_v4i16((LAS s16x4*)addr));
}
DEVINL u32x4 lds_tr2(u32 a0, u32 a1) {
  u32x2 lo = lds_tr(a0), hi = lds_tr(a1);
  return u32x4{lo.x, lo.y, hi.x, hi.y};
}
DEVINL u32x4 lds_r128(u32 addr) { return *(LAS u32x4*)addr; }
DEVINL u32x2 lds_r64(u32 addr) { return *(LAS u32x2*)addr; }
DEVINL void lds_w128(u32 addr, u32x4 v) { *(LAS u32x4*)addr = v; }
DEVINL void lds_w64(u32 addr, u32x2 v) { *(LAS u32x2*)addr = v; }
template <int CTRL> DEVINL float dpp(float v) { return asf(__builtin_amdgcn_update_dpp(0u, asu(v), CTRL, 0xf, 0xf, true)); }
DEVINL float sum8(float v) { v += dpp<0xB1>(v); v += dpp<0x4E>(v); return v + dpp<0x141>(v); }
DEVINL float sum_g(float v) {
  auto a = __builtin_amdgcn_permlane16_swap(asu(v), asu(v), false, false);
  v = asf(a[0]) + asf(a[1]);
  auto b = __builtin_amdgcn_permlane32_swap(asu(v), asu(v), false, false);
  return asf(b[0]) + asf(b[1]);
}
#define SB() __builtin_amdgcn_sched_barrier(0)
#define PRIO_K1 2
#define PRIO_K2 2
#define PRIO(v)
DEVINL u32 fresh_lane() { u32 l; asm volatile("v_mbcnt_lo_u32_b32 %0, -1, 0\n\tv_mbcnt_hi_u32_b32 %0, -1, %0" : "=v"(l)); __builtin_assume(l < 64); return l; }
#define LANEVARS const u32 lane = fresh_lane(), l15 = lane & 15, g = lane >> 4, q4 = l15 >> 2, p4 = lane & 3; \
  const u32 trrow = (16 * (g >> 1) + 4 * (g & 1) + q4) * 288; (void)l15; (void)g; (void)q4; (void)p4; (void)trrow
struct LAddr {
  u32 b[3];
  DEVINL explicit LAddr(u32 a) { b[0] = a; b[1] = a + 65536u; b[2] = a + 131072u; asm volatile("" : "+v"(b[0]), "+v"(b[1]), "+v"(b[2])); }
  DEVINL u32 operator()(u32 c) const { return b[c >> 16] + (c & 0xffffu); }
};

#define POL_W ""
#define POL_T ""
#define POL_S ""
#define PFMASK 0xff
#define B1N 4
#define B3WAIT 0
#define POL_1 ""
#define ROT1 1
#define POL_X ""
template <int NL, bool SC1> DEVINL void dma(const char* base, u32 voff, u32 lbase, u32 loff) {
  if constexpr (NL == 64) {
    if constexpr (SC1) asm volatile("s_add_u32 m0, %2, %3\n\ts_nop 0\n\tglobal_load_lds_dwordx4 %0, %1 sc1" ::"v"(voff), "s"(base), "s"(lbase), "n"(loff) : "memory", "scc");
    else asm volatile("s_add_u32 m0, %2, %3\n\ts_nop 0\n\tglobal_load_lds_dwordx4 %0, %1" POL_W ::"v"(voff), "s"(base), "s"(lbase), "n"(loff) : "memory", "scc");
  } else {
    if constexpr (SC1) asm volatile("s_add_u32 m0, %2, %3\n\ts_mov_b32 exec_hi, %4\n\tglobal_load_lds_dwordx4 %0, %1 sc1\n\ts_mov_b32 exec_hi, -1" ::"v"(voff), "s"(base), "s"(lbase), "n"(loff), "n"((1u << (NL - 32)) - 1u) : "memory", "scc");
    else asm volatile("s_add_u32 m0, %2, %3\n\ts_mov_b32 exec_hi, %4\n\tglobal_load_lds_dwordx4 %0, %1" POL_X "\n\ts_mov_b32 exec_hi, -1" ::"v"(voff), "s"(base), "s"(lbase), "n"(loff), "n"((1u << (NL - 32)) - 1u) : "memory", "scc");
  }
}
DEVINL void dma_w1(const char* base, u32 voff, u32 lds) {
  asm volatile("s_mov_b32 m0, %2\n\ts_nop 0\n\tglobal_load_lds_dwordx4 %0, %1" POL_1 ::"v"(voff), "s"(base), "s"(lds) : "memory");
}
DEVINL void st128_sc1(char* base, u32 voff, u32x4 v) {
  asm volatile("global_store_dwordx4 %0, %1, %2 sc1\n\ts_nop 1" ::"v"(voff), "v"(v), "s"(base) : "memory");
}
DEVINL void st128_sc1_m(char* base, u32 voff, u32x4 v, u64 mask) {
  asm volatile("s_mov_b64 exec, %3\n\tglobal_store_dwordx4 %0, %1, %2 sc1\n\ts_mov_b64 exec, -1\n\ts_nop 0" ::"v"(voff), "v"(v), "s"(base), "s"(mask) : "memory");
}
DEVINL void st64_sc1(char* base, u32 voff, u32x2 v) {
  asm volatile("global_store_dwordx2 %0, %1, %2 sc1" ::"v"(voff), "v"(v), "s"(base) : "memory");
}
DEVINL void st64(char* base, u32 voff, u32x2 v) {
  asm volatile("global_store_dwordx2 %0, %1, %2" ::"v"(voff), "v"(v), "s"(base) : "memory");
}
DEVINL void st32_sc1(char* base, u32 voff, u32 v) {
  asm volatile("global_store_dword %0, %1, %2 sc1" ::"v"(voff), "v"(v), "s"(base) : "memory");
}
DEVINL void st_flag(char* addr, u32 tag) {
  u32 z, t;
  asm volatile("v_mov_b32 %0, 0\n\tv_mov_b32 %1, %3\n\ts_mov_b64 exec, 1\n\tglobal_store_dword %0, %1, %2 sc1\n\ts_mov_b64 exec, -1" : "=&v"(z), "=&v"(t) : "s"(addr), "s"(tag) : "memory");
}
DEVINL void ld128(u32x4& d, const char* base, u32 voff, int imm) {
  asm volatile("global_load_dwordx4 %0, %1, %2 offset:%3" POL_X : "=v"(d) : "v"(voff), "s"(base), "n"(imm) : "memory");
}
DEVINL void ld128_sc1(u32x4& d, const char* base, u32 voff, int imm = 0) {
  asm volatile("global_load_dwordx4 %0, %1, %2 offset:%3 sc1" : "=v"(d) : "v"(voff), "s"(base), "n"(imm) : "memory");
}
DEVINL void ld64(u32x2& d, const char* base, u32 voff, int imm = 0) {
  asm volatile("global_load_dwordx2 %0, %1, %2 offset:%3" : "=v"(d) : "v"(voff), "s"(base), "n"(imm) : "memory");
}
DEVINL void ld32(u32& d, const char* base, u32 voff) {
  asm volatile("global_load_dword %0, %1, %2" POL_T : "=v"(d) : "v"(voff), "s"(base) : "memory");
}
DEVINL void touch(u32& d, const char* base, u32 voff) {
  asm volatile("global_load_dword %0, %1, %2" POL_T : "+v"(d) : "v"(voff), "s"(base) : "memory");
}
DEVINL void ld64_sc1(u32x2& d, const char* base, u32 voff) {
  asm volatile("global_load_dwordx2 %0, %1, %2 sc1" : "=v"(d) : "v"(voff), "s"(base) : "memory");
}
DEVINL void ld16(u32& d, const char* base, u32 voff) {
  asm volatile("global_load_ushort %0, %1, %2" : "=v"(d) : "v"(voff), "s"(base) : "memory");
}
template <int N> DEVINL void waitv() { asm volatile("s_waitcnt vmcnt(%0)" ::"n"(N) : "memory"); }
template <typename T> DEVINL void pin(T& v) { asm volatile("" : "+v"(v)); }
template <int N> DEVINL void bar_v() { asm volatile("s_waitcnt vmcnt(%0) lgkmcnt(0)\n\ts_barrier" ::"n"(N) : "memory"); }
template <int N> DEVINL void bar_vo() { asm volatile("s_waitcnt vmcnt(%0)\n\ts_barrier" ::"n"(N) : "memory"); }
DEVINL void bar() { asm volatile("s_waitcnt lgkmcnt(0)\n\ts_barrier" ::: "memory"); }

DEVINL void cnt_add(u32 c) {
  u32 a, o;
  asm volatile("v_mov_b32 %0, %2\n\tv_mov_b32 %1, 1\n\ts_mov_b64 exec, 1\n\tds_add_u32 %0, %1\n\ts_mov_b64 exec, -1" : "=&v"(a), "=&v"(o) : "n"(L_CNT + 4 * c) : "memory");
}
DEVINL void cnt_wait(u32 c) {
  for (u32 spins = 0; spins < (SPIN_CAP << 4); ++spins) {
    u32 v;
    asm volatile("v_mov_b32 %0, %1\n\tds_read_b32 %0, %0\n\ts_waitcnt lgkmcnt(0)" : "=v"(v) : "n"(L_CNT + 4 * c) : "memory");
    if (__builtin_amdgcn_readfirstlane(v) >= 4u) break;
    __builtin_amdgcn_s_sleep(1);
  }
}
template <int N> DEVINL void sync4(u32 c) {
  asm volatile("s_waitcnt vmcnt(%0) lgkmcnt(0)" ::"n"(N) : "memory");
  cnt_add(c);
  cnt_wait(c);
}
DEVINL void poll_flags(const char* base, u32 voff, u32 tag) {
  for (u32 spins = 0; spins < SPIN_CAP; ++spins) {
    u32 v;
    asm volatile("global_load_dword %0, %1, %2 sc1\n\ts_waitcnt vmcnt(0)" : "=v"(v) : "v"(voff), "s"(base) : "memory");
    if (__all(v == tag)) break;
  }
}

#define STAMP(i)

#define FS(i)
DEVINL void feature_map2(const u32x4 (&wa)[5], const u32x2 (&wb)[5], const f32x4 (&t0)[2], const f32x4 (&t1)[2], const f32x4 (&t2)[2], u32 (&ph)[2][9]) {
  float p[2][20];
#pragma unroll
  for (int m = 0; m < 2; ++m) {
    const u32x2 p0 = pack4(t0[m]), p1 = pack4(t1[m]), p2 = pack4(t2[m]);
    const u32x4 b01 = {p0.x, p0.y, p1.x, p1.y};
    f32x4 a[5];
#pragma unroll
    for (int t = 0; t < 5; ++t) a[t] = mfma32(wa[t], b01, f32x4{0.f, 0.f, 0.f, 0.f});
#pragma unroll
    for (int t = 0; t < 5; ++t) {
      a[t] = mfma16(wb[t], p2, a[t]);
      p[m][4 * t] = a[t].x; p[m][4 * t + 1] = a[t].y; p[m][4 * t + 2] = a[t].z; p[m][4 * t + 3] = a[t].w;
    }
  }
  const float L2E = 1.44269504088896341f;
#pragma unroll
  for (int m = 0; m < 2; ++m) {
    float mx = p[m][0];
#pragma unroll
    for (int i = 1; i < 18; ++i) mx = fmaxf(mx, p[m][i]);
    const float nm = -mx * L2E;
    f32x2 sum = {0.f, 0.f};
#pragma unroll
    for (int i = 0; i < 18; ++i) p[m][i] = __builtin_amdgcn_exp2f(__builtin_fmaf(p[m][i], L2E, nm));
#pragma unroll
    for (int i = 0; i < 9; ++i) sum += f32x2{p[m][2 * i], p[m][2 * i + 1]};
    const float inv = __builtin_amdgcn_rcpf(sum.x + sum.y);
#pragma unroll
    for (int i = 0; i < 9; ++i) ph[m][i] = pack2(p[m][2 * i] * inv, p[m][2 * i + 1] * inv);
  }
}

DEVINL float gelu_tanh(float x) {
  const float C = -2.f * 0.7978845608028654f * 1.44269504088896341f;
  float y = x * __builtin_fmaf(x * x, 0.044715f * C, C);
  float e = __builtin_amdgcn_exp2f(y);
  return x * __builtin_amdgcn_rcpf(1.f + e);
}

__global__ __launch_bounds__(NT, 2) void fused_kernel(Params P) {
  extern __shared__ __attribute__((aligned(16))) char smem[];
  STAMP(0); FS(0);
  const u32 w = __builtin_amdgcn_readfirstlane(threadIdx.x >> 6);
  const u32 bid = blockIdx.x, xcd = bid & 7, idx = bid >> 3;
  char* const ws = P.ws;
  u64 pc0; asm volatile("s_memrealtime %0\n\ts_waitcnt lgkmcnt(0)" : "=s"(pc0));
  u64 pc; u32 len;
  asm volatile("s_getpc_b64 %0\n.Lfk_code_begin:\n\ts_mov_b32 %1, .Lfk_code_end-.Lfk_code_begin\n\ts_max_u32 %1, %1, .Lfk_copy_end-.Lfk_code_begin" : "=s"(pc), "=s"(len)::"scc");

  const u32 cg = (xcd & 1) * 4 + (idx & 3), rb = (xcd >> 1) * 8 + (idx >> 2);
  const u32 rot = (idx >> 2) % 6;
  const u32 w3 = w & 3, kh = w >> 2;
  const u32 kq = xcd >> 1, kp = xcd & 1;
  const bool g1 = idx < 27;
  const u32 ni = kq * 9 + (g1 ? idx / 3 : 0), mi = idx % 3;
  u32 tag, tag0;
  u32x4 wf[36];
  u32x4 xr[9];
  const u32 rgp = w ^ ((cg & 1) * 4);
  auto load_x = [&]() __attribute__((always_inline)) {
    LANEVARS;
    const u32 xo = (rb * 64 + rgp * 8 + (lane >> 3)) * 1152 + (lane & 7) * 16;
#pragma unroll
    for (int i = 0; i < 9; ++i) ld128(xr[i], P.x, xo, i * 128);
    asm volatile("s_load_dword %0, %1, 0x0" : "=s"(tag0) : "s"(ws) : "memory");
    FS(1);
  };
  auto layer_norm = [&]() __attribute__((always_inline)) {
    LANEVARS;
    f32x2 c[36];
    f32x2 s1 = {0.f, 0.f}, s2 = {0.f, 0.f};
#pragma unroll
    for (int i = 0; i < 9; ++i) {
#pragma unroll
      for (int j = 0; j < 4; ++j) {
        const f32x2 v = {bflo(xr[i][j]), bfhi(xr[i][j])};
        c[i * 4 + j] = v;
        s1 += v; s2 += v * v;
      }
    }
    const float mean = sum8(s1.x + s1.y) * (1.f / 576.f);
    const float var = fmaxf(sum8(s2.x + s2.y) * (1.f / 576.f) - mean * mean, 0.f);
    const float rs = __builtin_amdgcn_rsqf(var + 1e-5f), nb = -mean * rs;
    const u32 a0 = L_XN + ((rgp >> 1) * 18 + ((lane & 7) >> 2)) * 1024 + (lane & 3) * 256 + (((8 * (rgp & 1) + (lane >> 3)) ^ (lane & 7)) * 16);
#pragma unroll
    for (int i = 0; i < 9; ++i) {
      u32x4 o;
#pragma unroll
      for (int j = 0; j < 4; ++j) { const f32x2 v = c[i * 4 + j] * rs + nb; o[j] = pack2(v.x, v.y); }
      lds_w128(a0 + i * 2048, o);
    }
  };
#define WAIT_X(N) asm volatile("s_waitcnt vmcnt(" #N ")" : "+v"(xr[0]), "+v"(xr[1]), "+v"(xr[2]), "+v"(xr[3]), "+v"(xr[4]), "+v"(xr[5]), "+v"(xr[6]), "+v"(xr[7]), "+v"(xr[8])::"memory")
  auto unit_at = [](int u) -> u32 { return u < 6 ? 2 * SLOT + u * 8192 : (u < 9 ? (u - 6) * 8192 : (u < 12 ? SLOT + (u - 9) * 8192 : 2 * SLOT + (u - 6) * 8192)); };
  auto grab1 = [&](const LAddr& t0, const LAddr& t1, int ks, int u) __attribute__((always_inline)) {
    const u32 sl = unit_at(u);
    wf[2 * ks] = lds_tr2(t0(sl), t0(sl + 1024));
    wf[2 * ks + 1] = lds_tr2(t1(sl), t1(sl + 1024));
  };
#define T1W_DEF const u32 t1a = (8 * g + q4) * 256 + (((2 * w3) ^ (4 * (g & 1) + q4)) * 32) + p4 * 8; const LAddr t1w(t1a), t1x(t1a ^ 32)
  u32x4 hrow[3];
  const u32 wl = w3 * 3456;
  const char* wq = P.wqkv + cg * 144;
  auto stage = [&](u32 n) __attribute__((always_inline)) -> u32 { const u32 st = rot + n; return (st >= 6 ? st - 6 : st) * (96 * 3456); };
#define RING_DEF \
  const u32 r3 = lane / 18, c18 = lane % 18, seg = c18 / 9, ch9 = c18 % 9; \
  u32 wrow[4], voff[4]; \
  _Pragma("unroll") for (int i = 0; i < 4; ++i) { \
    const u32 r = 12 * w3 + 3 * i + r3; \
    wrow[i] = (r & ~12u) | ((r & 4u) << 1) | ((r & 8u) >> 1); \
    voff[i] = wrow[i] * 3456 + seg * 1152 + ch9 * 16; \
    pin(voff[i]); \
  } \
  auto issue1 = [&](int n, int j0, int j1) __attribute__((always_inline)) {   \
    constexpr u32 slot[9] = {0, 1, 2, 3, 4, 0, 1, 2, 3}; \
    const char* b = n < 6 ? wq + 1152 + stage(n) : wq; \
    const u32 qo = n < 6 ? 0u : (seg ? stage(2 * (n - 6) + 1) - 1152 : stage(2 * (n - 6))); \
    _Pragma("unroll") for (int j = j0; j < j1; ++j) \
      dma<54, false>(b + (j >> 2) * (48 * 3456), voff[j & 3] + qo, wl, slot[n] * SLOT + (j >> 2) * 13824 + (j & 3) * 864); \
  }

  if (__builtin_expect(w < 4, 1)) {
    const u32 mp = w >> 1, hp = w & 1;
    load_x();
    {
      LANEVARS;
      RING_DEF;
      WAIT_X(0);
      STAMP(1); FS(2);
      layer_norm();
      FS(3);
      issue1(1, 0, 8);
    }
    u32 ta0, ta1, xa[2];
    {
      LANEVARS;
      if (lane < 32) *(LAS u32*)(L_ZERO + lane * 4) = 0u;
      ta0 = trrow + hp * 72 + p4 * 8; ta1 = trrow + hp * 72 + 64 + (p4 & 1) * 144;
#pragma unroll
      for (int i = 0; i < 2; ++i) { xa[i] = L_XN + mp * 36864 + g * 256 + ((l15 ^ (((i + rot) & 1) * 4 + g)) * 16); pin(xa[i]); }
    }
    const LAddr trA(ta0);
    FS(4);
    asm volatile("s_waitcnt lgkmcnt(0)\n\ts_barrier\n\ts_add_u32 %0, %1, 1" : "=s"(tag) : "s"(tag0) : "memory", "scc");
    STAMP(2); FS(5);
    u32x4 xf[2][18];
    auto rdx = [&](int n) __attribute__((always_inline)) {
      u32 st = rot + n / 3; if (st >= 6) st -= 6;
      const u32 ad = xa[(n / 3 + n % 3) & 1] + st * 3072;
      xf[0][n] = lds_r128(ad + (n % 3) * 1024);
      xf[1][n] = lds_r128(ad + (n % 3) * 1024 + 18432);
    };
    f32x4 acc[5][2];
#pragma unroll
    for (int i = 0; i < 5; ++i) { acc[i][0] = f32x4{0.f, 0.f, 0.f, 0.f}; acc[i][1] = acc[i][0]; }
    u32x4 wa[5]; u32x2 wb[5];
    {
      const LAddr trM(ta1);
      u32x4 F[6];
      u32 ad[3];
      auto rd = [&](int u) __attribute__((always_inline)) {
        constexpr u32 toff[4] = {0, 32, 144, 176};
        const int n = u / 5, i = u % 5;
        const u32 b = ((n / 3) % 5) * SLOT + (n % 3) * 9216;
        F[u % 6] = i < 4 ? lds_tr2(trA(b + toff[i & 3]), trA(b + toff[i & 3] + 2304)) : lds_tr2(trM(b), trM(b + 2304));
      };
      rdx(0);
#pragma unroll
      for (int u = 0; u < 5; ++u) rd(u);
      rdx(1);
      SB();
#pragma unroll
      for (int n = 0; n < 18; ++n) {
        if (n == 14) {
          LANEVARS;
#pragma unroll
          for (int a = 0; a < 3; ++a) {
            const u32 ic = a < 2 ? 16 * a + 4 * g + q4 : 32 + q4;
            const bool ok = (ic >= 18 ? 1u : 0u) == (p4 >> 1) && (a < 2 || g == 0);
            ad[a] = ok ? L_WPHI + (p4 & 1) * 2880 + (hp * 36 + ic) * 40 : L_ZERO;
            pin(ad[a]);
          }
        }
        if (n % 3 == 2 && n < 17) { FS(6 + 2 * (n / 3)); bar_v<0>(); FS(7 + 2 * (n / 3)); }
#pragma unroll
        for (int i = 0; i < 5; ++i) {
          const int u = n * 5 + i;
          if (u + 5 < 90) rd(u + 5);
          else { wa[i] = lds_tr2(ad[0] + 8 * i, ad[1] + 8 * i); wb[i] = lds_tr(ad[2] + 8 * i); }
          if (u < 8) { rdx(2 + 2 * u); rdx(3 + 2 * u); }
          acc[i][0] = mfma32(F[u % 6], xf[0][n], acc[i][0]);
          acc[i][1] = mfma32(F[u % 6], xf[1][n], acc[i][1]);
          SB();
        }
      }
    }
    STAMP(3); FS(16);
    {
      LANEVARS;
      const u32 va = L_V + (mp * 32 + l15) * 152 + hp * 72 + g * 8;
#pragma unroll
      for (int m = 0; m < 2; ++m) {
        lds_w64(va + m * (16 * 152), pack4(acc[2][m]));
        lds_w64(va + m * (16 * 152) + 32, pack4(acc[3][m]));
        if (g == 1) lds_w64(va + m * (16 * 152) + 56, pack4(acc[4][m]));
      }
      u32 ph[2][9];
      feature_map2(wa, wb, acc[0], acc[1], acc[4], ph);
      const u32 pa = L_PHI + (mp * 32 + l15) * 328 + (hp * 4 + g) * 40;
#pragma unroll
      for (int m = 0; m < 2; ++m)
#pragma unroll
        for (int i = 0; i < 5; ++i) lds_w64(pa + m * (16 * 328) + 8 * i, u32x2{ph[m][2 * i], i < 4 ? ph[m][2 * i + 1] : 0u});
    }
    FS(17);
    bar();
    FS(18);
    bar();
    STAMP(4); FS(20);
#pragma unroll
    for (int i = 0; i < 3; ++i) { acc[i][0] = f32x4{0.f, 0.f, 0.f, 0.f}; acc[i][1] = acc[i][0]; }
    {
      u32 tq;
      { LANEVARS; tq = trrow + hp * 72 + 64; }
      const LAddr trQ(tq);
      u32x4 F[6];
      auto rd = [&](int u) __attribute__((always_inline)) {
        const int n = u / 3, i = u % 3;
        const u32 b = (1 + n / 6) * SLOT + (n % 3) * 9216 + ((n % 6) / 3) * 144;
        F[u % 6] = i < 2 ? lds_tr2(trA(b + 32 * i), trA(b + 32 * i + 2304)) : lds_tr2(trQ(b), trQ(b + 2304));
      };
#pragma unroll
      for (int u = 0; u < 5; ++u) rd(u);
      SB();
#pragma unroll
      for (int u = 0; u < 54; ++u) {
        if (u + 5 < 54) rd(u + 5);
        acc[u % 3][0] = mfma32(F[u % 6], xf[0][u / 3], acc[u % 3][0]);
        acc[u % 3][1] = mfma32(F[u % 6], xf[1][u / 3], acc[u % 3][1]);
        SB();
      }
    }
    STAMP(5); FS(21);
    u32 pq[2][9];
    feature_map2(wa, wb, acc[0], acc[1], acc[2], pq);
    LANEVARS;
    u32 oa[3];
    {
      const bool same = (q4 >> 1) == (g >> 1);
#pragma unroll
      for (int t = 0; t < 3; ++t) {
        const u32 slot = 4 * t + p4;
        const u32 cc = slot < 9 ? 9 * (q4 & 1) + slot : 18;
        oa[t] = same ? L_KV + ((hp * 2 + (g >> 1)) * 19 + cc) * 96 + (g & 1) * 48 : L_ZERO;
        pin(oa[t]);
      }
    }
    const char* w1b = P.w1 + (size_t)(kp * 1024 + w * 4) * 9216 + ni * 256;
    u32 w1off = g * 9216 + ((((l15 >> 1) ^ (g | ((w & 2) << 1))) << 1) | (l15 & 1)) * 16, lane16 = lane * 16;
    T1W_DEF;
    auto issue_w1 = [&](int k0, int u0, int n) __attribute__((always_inline)) {
#pragma unroll
      for (int j = 0; j < n; ++j) {
        u32 r = j + ROT1 * ((mi * n) / 3); if (r >= (u32)n) r -= n;
        const u32 u = u0 + r;
        const u32 lds = (u < 6 ? 2 * SLOT + u * 8192 : (u < 9 ? (u - 6) * 8192 : (u < 12 ? SLOT + (u - 9) * 8192 : 2 * SLOT + (u - 6) * 8192))) + w * 1024;
        const char* b = w1b + (size_t)(k0 + r) * (32 * 9216);
        dma_w1(b, w1off, lds);
        dma_w1(b + 16 * 9216, w1off, lds + 4096);
      }
    };
    pin(w1off); pin(lane16);
#pragma unroll
    for (int m = 0; m < 2; ++m)
#pragma unroll
      for (int i = 0; i < 9; ++i) pin(pq[m][i]);
    STAMP(6); FS(22);
    bar();
    STAMP(7); FS(23);
    {
      u32x4 k0[3], k1[3]; u32x2 k2[3];
#pragma unroll
      for (int t = 2; t >= 0; --t) { k0[t] = lds_r128(oa[t]); k1[t] = lds_r128(oa[t] + 16); k2[t] = lds_r64(oa[t] + 32); }
      SB();
      f32x4 nu[2][3];
#pragma unroll
      for (int t = 2; t >= 0; --t)
#pragma unroll
        for (int m = 0; m < 2; ++m) {
          f32x4 c = mfma32(k0[t], u32x4{pq[m][0], pq[m][1], pq[m][2], pq[m][3]}, f32x4{0.f, 0.f, 0.f, 0.f});
          c = mfma32(k1[t], u32x4{pq[m][4], pq[m][5], pq[m][6], pq[m][7]}, c);
          nu[m][t] = mfma16(k2[t], u32x2{pq[m][8], 0u}, c);
        }
      char* ob = ws + W_O + (rb * 4 + mp * 2) * 18432;
#pragma unroll
      for (int m = 0; m < 2; ++m) {
        const float inv = __builtin_amdgcn_rcpf(nu[m][2].y + 1e-6f);
        const u32x2 a = pack4(nu[m][0] * inv), b = pack4(nu[m][1] * inv);
        st128_sc1(ob + m * 18432 + (cg * 2 + hp) * 1024, lane16, u32x4{a.x, a.y, b.x, b.y});
        const u32 e0 = asu(nu[m][2].x * inv);
        auto t16 = __builtin_amdgcn_permlane16_swap(e0, e0, false, false);
        const u32 e01 = pack2(asf(t16[0]), asf(t16[1]));
        auto t32 = __builtin_amdgcn_permlane32_swap(e01, e01, false, false);
        asm volatile("s_mov_b64 exec, 0xffff\n\tglobal_store_dwordx2 %0, %1, %2 sc1\n\ts_mov_b64 exec, -1" ::"v"(lane16), "v"(u32x2{t32[0], t32[1]}), "s"(ob + m * 18432 + 16384 + cg * 256 + hp * 8) : "memory");
      }
    }
    FS(24);
    cnt_add(9);
    waitv<0>();
    st_flag(ws + W_FLAGB + ((rb * 2 + mp) * 16 + cg * 2 + hp) * 128, tag);
    STAMP(8); FS(25);
    cnt_wait(6);
    FS(42);
    issue_w1(0, 0, 9);
    sync4<0>(0);
#pragma unroll
    for (int ks = 0; ks < 9; ++ks) grab1(t1w, t1x, ks, ks);
    sync4<63>(1);
    FS(44);
    issue_w1(9, 0, 9);
    sync4<0>(2);
#pragma unroll
    for (int ks = 9; ks < 16; ++ks) grab1(t1w, t1x, ks, ks - 9);
    sync4<63>(3);
    FS(43);
    cnt_wait(7);
    issue_w1(18, 0, 7);
    issue_w1(25, 9, 7);
    FS(38);
    bar_vo<0>();
#pragma unroll
    for (int j = 0; j < 3; ++j) { const u32 it = min(j * 64 + lane, 143u); hrow[j] = lds_r128(L_HI + (w * 16 + it / 9) * 160 + (it % 9) * 16); }
#pragma unroll
    for (int j = 0; j < 3; ++j) {
      const u32 it = min(j * 64 + lane, 143u);
      st128_sc1(ws + W_H + (rb * 64 + w * 16) * 1152 + cg * 144, (it / 9) * 1152 + (it % 9) * 16, hrow[j]);
    }
    FS(40);
    bar();
  } else {
    LANEVARS;
    const u32 lane16 = lane * 16;
    u32 pfd = 0;
    asm volatile("global_load_dword %0, %1, %2" : "+v"(pfd) : "v"(min(((idx * 4 + w3) * 4 + p4) * 128u, len)), "s"(pc) : "memory");
    load_x();
    RING_DEF;
    u32 wp[3];
    {
      const u32 so = (min(lane, 59u) / 10) * 36 + min(lane % 10, 8u) * 4;
#pragma unroll
      for (int j = 0; j < 3; ++j) ld32(wp[j], P.wphi + cg * 2592 + w3 * 648 + j * 216, so);
    }
    issue1(0, 0, 8);
    WAIT_X(8);
    FS(2);
    pin(wp[0]); pin(wp[1]); pin(wp[2]);
    layer_norm();
    if (lane < 60) {
#pragma unroll
      for (int j = 0; j < 3; ++j) {
        *(LAS u32*)(L_WPHI + w3 * 720 + j * 240 + lane * 4) = wp[j];
        *(LAS u32*)(L_WPHI + 2880 + w3 * 720 + j * 240 + lane * 4) = wp[j] ^ 0x80008000u;
      }
    }
    FS(4);
    asm volatile("s_waitcnt vmcnt(0) lgkmcnt(0)\n\ts_barrier\n\ts_add_u32 %0, %1, 1" : "=s"(tag) : "s"(tag0) : "memory", "scc");
    issue1(2, 0, 8);
    FS(6);
    bar_vo<8>();
    issue1(3, 0, 8);
    FS(8);
    bar_vo<8>();
    issue1(4, 0, 8);
    FS(10);
    bar_vo<8>();
    issue1(5, 0, 8);
    FS(12);
    bar_vo<8>();
    {
      const u32 ln = min((idx * 4 + w3) * 23 + min(lane, 22u), 2879u);
      touch(pfd, P.wo + (xcd & 1) * 512, (ln / 5) * 1152 + (ln % 5) * 128);
    }
    issue1(6, 0, 4);
    FS(14);
    bar_vo<5>();
    issue1(6, 4, 8);
    issue1(7, 0, 8);
    const u32 srow = 8 * g + q4;
    u32 e_vb0 = L_V + srow * 152 + (w3 >> 1) * 72 + ((w3 & 1) ? 40 : 0) + p4 * 8, e_vb1 = L_V + srow * 152 + (w3 >> 1) * 72 + 32;
    u32 e_pb = L_PHI + srow * 328 + w3 * 80 + p4 * 8, e_pb2 = L_PHI + srow * 328 + w3 * 80 + 64 + (p4 & 1) * 8;
    u32 e_s0 = (lane >> 3) * 4096 + (lane & 7) * 16, e_s1[3];
    const u32 colp = l15 == 4 ? 2u : l15 - 2 * (w3 & 1);
#pragma unroll
    for (int i = 0; i < 3; ++i) {
      const u32 sidx = (160 + min(colp, 2u) * 10 + 4 * i + g);
      e_s1[i] = (sidx >> 3) * 4096 + (sidx & 7) * 16;
      pin(e_s1[i]);
    }
    const u64 e_m01 = __ballot(l15 == 4 || colp < 2), e_m2 = __ballot(l15 == 4 || (colp < 2 && g < 2));
    pin(e_vb0); pin(e_vb1); pin(e_pb); pin(e_pb2); pin(e_s0);
    issue1(8, 0, 8);
#pragma unroll
    for (int i = 0; i < 4; ++i) { voff[i] = (9 * (wrow[i] >> 3) + (wrow[i] & 7) + seg * 324) * 1152 + ch9 * 16; pin(voff[i]); }
    const char* wob = P.wo + cg * 144;
    FS(17);
    bar();
    FS(18);
    {
      u32x4 vf[2][2], pf_[2][3];
#pragma unroll
      for (int ks = 0; ks < 2; ++ks) {
        vf[ks][0] = lds_tr2(e_vb0 + ks * (32 * 152), e_vb0 + ks * (32 * 152) + 4 * 152);
        pf_[ks][0] = lds_tr2(e_pb + ks * (32 * 328), e_pb + ks * (32 * 328) + 4 * 328);
        pf_[ks][1] = lds_tr2(e_pb + ks * (32 * 328) + 32, e_pb + ks * (32 * 328) + 32 + 4 * 328);
        pf_[ks][2] = lds_tr2(e_pb2 + ks * (32 * 328), e_pb2 + ks * (32 * 328) + 4 * 328);
      }
#pragma unroll
      for (int ks = 0; ks < 2; ++ks) vf[ks][1] = lds_tr2(e_vb1 + ks * (32 * 152), e_vb1 + ks * (32 * 152) + 4 * 152);
      SB();
      f32x4 kv[2][3];
#pragma unroll
      for (int vt = 0; vt < 2; ++vt)
#pragma unroll
        for (int ks = 0; ks < 2; ++ks) {
          if (vt && l15 >= 4) vf[ks][1] = u32x4{0x3f803f80u, 0x3f803f80u, 0x3f803f80u, 0x3f803f80u};
#pragma unroll
          for (int i = 0; i < 3; ++i) kv[vt][i] = mfma32(pf_[ks][i], vf[ks][vt], ks ? kv[vt][i] : f32x4{0.f, 0.f, 0.f, 0.f});
        }
      char* kb = ws + W_KVPART + ((cg * 96 + w3 * 24) * 32 + rb) * 128;
#pragma unroll
      for (int i = 0; i < 3; ++i) {
        const u32x2 pk = pack4(kv[0][i]);
        st128_sc1_m(kb + i * 32768, e_s0, u32x4{pk.x, tag, pk.y, tag}, i < 2 ? ~0ull : 0xffffffffull);
      }
#pragma unroll
      for (int i = 0; i < 3; ++i) {
        const u32x2 pk = pack4(kv[1][i]);
        st128_sc1_m(kb, e_s1[i], u32x4{pk.x, tag, pk.y, tag}, i < 2 ? e_m01 : e_m2);
      }
    }
    FS(19);
    bar_v<6>();
#pragma unroll
    for (int j = 0; j < 8; ++j) dma<54, false>(wob + (j >> 2) * (54 * 1152), voff[j & 3], wl, (j >> 2) * 13824 + (j & 3) * 864);
    u32x2 xres[5];
    {
      const u32 xo = (rb * 64 + w3 * 16 + l15) * 1152 + cg * 144;
#pragma unroll
      for (int n = 0; n < 4; ++n) ld64(xres[n], P.x, xo + g * 8, n * 32);
      ld64(xres[4], P.x, xo + (g & 1) * 8, 128);
    }
    u32 la[3], vo2[8];
    {
#pragma unroll
      for (int a = 0; a < 3; ++a) {
        const u32 r = a * 64 + lane;
        const u32 rr = r - 160, cp = (rr * 205) >> 11;
        const u32 cc = r < 160 ? (r & 15) + 2 * (w3 & 1) : (cp >= 2 ? 18 : ((w3 & 1) ? cp : 16 + cp));
        const u32 piece = r < 160 ? (r >> 4) : rr - cp * 10;
        la[a] = r < 190 ? L_KV + (w3 * 19 + cc) * 96 + piece * 8 + (piece >= 5 ? 8 : 0) : L_KV + 40;
        pin(la[a]);
      }
#pragma unroll
      for (int j = 0; j < 8; ++j) {
        const u32 kap = 192 + (j >> 2) * 48 + wrow[j & 3] + seg * 288;
        vo2[j] = (kap < 512 ? 9 * (kap >> 3) + (kap & 7) : 9 * (kap - 512) + 8) * 1152 + ch9 * 16;
        pin(vo2[j]);
      }
    }
    FS(26);
    if (w3 < 3) {
      const char* pb = ws + W_KVPART + (cg * 96 + rb * 3 + w3) * 4096;
      f32x4 sum;
      for (u32 spins = 0; spins < SPIN_CAP; ++spins) {
        u32x4 v[4];
#pragma unroll
        for (int i = 0; i < 4; ++i) ld128_sc1(v[i], pb, lane16, i * 1024);
        waitv<0>();
        u32 bad = 0;
        sum = f32x4{0.f, 0.f, 0.f, 0.f};
#pragma unroll
        for (int i = 0; i < 4; ++i) {
          pin(v[i]);
          bad |= (v[i].y ^ tag) | (v[i].w ^ tag);
          sum += unpk(v[i].x, v[i].z);
        }
        pin(bad);
        if (__all(bad == 0)) break;
      }
#pragma unroll
      for (int j = 0; j < 4; ++j) sum[j] = sum_g(sum[j] + dpp<0x128>(sum[j]));
      const u32x2 pk = pack4(sum);
      st128_sc1_m(ws + W_KVFIN + (cg * 96 + rb * 3 + w3) * 128, lane16, u32x4{pk.x, tag, pk.y, tag}, 0xffull);
      cnt_add(8);
    } else {
      cnt_add(8); cnt_wait(8);
    }
    FS(27);
#pragma unroll
    for (int j = 0; j < 8; ++j) dma<54, false>(wob + (108 + (j >> 2) * 54) * 1152, voff[j & 3], wl, 4 * SLOT + (j >> 2) * 13824 + (j & 3) * 864);
    {
      FS(28);
      const char* fb = ws + W_KVFIN + (cg * 4 + w3) * 3072;
      u32x4 v[3];
      for (u32 spins = 0; spins < SPIN_CAP; ++spins) {
#pragma unroll
        for (int i = 0; i < 3; ++i) ld128_sc1(v[i], fb, lane16, i * 1024);
        waitv<0>();
        u32 bad = 0;
#pragma unroll
        for (int i = 0; i < 3; ++i) { pin(v[i]); bad |= (v[i].y ^ tag) | (v[i].w ^ tag); }
        pin(bad);
        if (__all(bad == 0)) break;
      }
#pragma unroll
      for (int i = 0; i < 3; ++i) lds_w64(la[i], u32x2{v[i].x, v[i].z});
    }
    FS(22);
    bar();
#pragma unroll
    for (int j = 0; j < 8; ++j) dma<54, false>(wob, vo2[j], wl, SLOT + (j >> 2) * 13824 + (j & 3) * 864);
    const LAddr wo0(trrow + p4 * 8), wo4(trrow + 128 + (p4 & 1) * 8);
    auto rdw = [&](u32 b, int n) __attribute__((always_inline)) -> u32x4 {
      return n < 4 ? lds_tr2(wo0(b + 32 * n), wo0(b + 32 * n + 2304)) : lds_tr2(wo4(b), wo4(b + 2304));
    };
    u32x4 wm[30];
#pragma unroll
    for (int u = 0; u < 30; ++u) wm[u] = rdw(((u / 5) % 3) * 9216 + (u / 15) * 144, u % 5);
    sync4<0>(5);
    f32x4 hc[5];
#pragma unroll
    for (int n = 0; n < 5; ++n) { pin(xres[n]); hc[n] = unpk(xres[n].x, xres[n].y); pin(hc[n]); }
    {
      u32x4 F[6];
      auto rd = [&](int u) __attribute__((always_inline)) {
        const int v = u / 5;
        F[u % 6] = rdw((v < 6 ? 4 * SLOT : SLOT) + (v % 3) * 9216 + ((v / 3) & 1) * 144, u % 5);
      };
#pragma unroll
      for (int u = 0; u < 5; ++u) rd(u);
      FS(29);
      poll_flags(ws + W_FLAGB + (rb * 2 + (w3 >> 1)) * 2048, l15 * 128, tag);
      STAMP(9); FS(30);
      constexpr int ord[18] = {3, 4, 5, 12, 13, 14, 6, 7, 8, 15, 16, 17, 0, 1, 2, 9, 10, 11};
      u32x4 of[18];
      const char* ob = ws + W_O + (rb * 4 + w3) * 18432;
#pragma unroll
      for (int j = 0; j < 18; ++j) ld128_sc1(of[j], ob + (ord[j] >> 2) * 4096, lane16, (ord[j] & 3) * 1024);
      cnt_add(6);
      FS(31);
#pragma unroll
      for (int j = 0; j < 18; ++j) {
        asm volatile("s_waitcnt vmcnt(%1)" : "+v"(of[j]) : "n"(17 - j) : "memory");
#pragma unroll
        for (int n = 0; n < 5; ++n) {
          const int u = j * 5 + n;
          if (u + 5 < 60) rd(u + 5);
          hc[n] = mfma32(u < 60 ? F[u % 6] : wm[u - 60], of[j], hc[n]);
          SB();
        }
      }
    }
    STAMP(10); FS(32);
    float s1 = 0.f, s2 = 0.f;
    u32x2 hr[5];
#pragma unroll
    for (int n = 0; n < 5; ++n) {
      hr[n] = pack4(hc[n]);
      if (n == 4 && g >= 2) hr[n] = u32x2{0u, 0u};
      s1 = dot2(hr[n].x, 0x3f803f80u, s1); s1 = dot2(hr[n].y, 0x3f803f80u, s1);
      s2 = dot2(hr[n].x, hr[n].x, s2); s2 = dot2(hr[n].y, hr[n].y, s2);
    }
    s1 = sum_g(s1); s2 = sum_g(s2);
    char* sb = ws + W_STATS + (rb * 4 + w3) * 2048;
    st128_sc1_m(sb + cg * 256, lane16, u32x4{asu(s1), tag, asu(s2), tag}, 0xffffull);
    FS(34);
    cnt_wait(9);
    u32 hw = L_HI + (w3 * 16 + l15) * 160 + g * 8;
#pragma unroll
    for (int n = 0; n < 5; ++n) {
      lds_w64(hw + n * 32, hr[n]);
      hc[n] = unpk(hr[n].x, hr[n].y);
      pin(hc[n]);
    }
    u32 hrd = L_HN + (w3 * 16 + 8 * (g & 1) + q4) * 160 + ((g >> 1) * 16 + p4 * 4) * 2;
    u32 hrx = L_HN + (w3 * 16 + 8 * (g & 1) + q4) * 160 + (64 + (p4 & 1) * 4) * 2;
    u32 ho[2];
#pragma unroll
    for (int j = 0; j < 2; ++j) {
      const u32 d = cg * 72 + (j ? 64 + (l15 & 7) : (g >> 1) * 16 + l15);
      ho[j] = (d >> 5) * 131072 + (g & 1) * 512 + (d & 31) * 16;
      pin(ho[j]);
    }
    T1W_DEF;
    pin(hw); pin(hrd); pin(hrx);
    char* hb = ws + W_HN + (rb * 4 + w3) * 1024;
    u32x4 v0, v1;
    FS(35);
    for (u32 spins = 0; spins < SPIN_CAP; ++spins) {
      ld128_sc1(v0, sb, lane16);
      ld128_sc1(v1, sb, lane16, 1024);
      waitv<0>();
      pin(v0); pin(v1);
      u32 bad = (v0.y ^ tag) | (v0.w ^ tag) | (v1.y ^ tag) | (v1.w ^ tag);
      pin(bad);
      if (__all(bad == 0)) break;
    }
    pin(pfd);
    cnt_add(7);
    FS(36);
    s1 = sum_g(asf(v0.x) + asf(v1.x));
    s2 = sum_g(asf(v0.z) + asf(v1.z));
    const float mean = s1 * (1.f / 576.f);
    const float var = fmaxf(s2 * (1.f / 576.f) - mean * mean, 0.f);
    const float r1 = __builtin_amdgcn_rsqf(var + 1e-5f);
    const float r2 = r1 * __builtin_amdgcn_rsqf(var * r1 * r1 + 1e-5f);
    const float nb = -mean * r2;
#pragma unroll
    for (int n = 0; n < 5; ++n) lds_w64(hw + 10240 + n * 32, pack4(hc[n] * r2 + nb));
    const u32x4 f0 = lds_tr2(hrd, hrd + 4 * 160), f1 = lds_tr2(hrd + 64, hrd + 64 + 4 * 160), f2 = lds_tr2(hrx, hrx + 4 * 160);
    STAMP(11); FS(37);
    st128_sc1(hb, ho[0], f0);
    st128_sc1(hb + 131072, ho[0], f1);
    st128_sc1_m(hb, ho[1], f2, 0x00ff00ffull);
    sync4<0>(10);
    if (w == 4) st_flag(ws + W_FLAGD + (cg * 32 + rb) * 128, tag);
    FS(38);
    asm volatile("s_barrier" ::: "memory");
#pragma unroll
    for (int ks = 0; ks < 16; ++ks) grab1(t1w, t1x, ks, ks < 2 ? ks + 7 : (ks < 9 ? ks - 2 : ks));
    FS(40);
    bar_v<0>();
    asm volatile(".Lfk_copy_end:");
  }
  STAMP(12); FS(39);
  LANEVARS;
  const u32 lane16 = lane * 16;
  const u32 wcol = ((((l15 >> 1) ^ ((w * 4 + g) & 7)) << 1) | (l15 & 1)) * 16;

  const u32 Mq = idx & 3, Nq = kp * 8 + (idx >> 2), odd = Mq & 1;
  const char* w2b = P.w2 + (size_t)(kq * 1152 + w * 4) * 4096 + Nq * 256;
  const u32 w2off = g * 4096 + wcol;
  u32 w1k = w * 1024;
  asm volatile("" : "+s"(w1k));
  auto issue_w2 = [&](int ps, u32 unit) __attribute__((always_inline)) {
    const char* b = w2b + (ps < 9 ? ps + 9 * odd : ps - 9 * odd) * (32 * 4096);
    dma<64, false>(b, w2off, w1k, unit * 16384);
    dma<64, false>(b + 18 * (32 * 4096), w2off, w1k, unit * 16384 + 8192);
  };
  u32 tw2;
  { LANEVARS; tw2 = kh * 8192 + (4 * g + q4) * 256 + (((2 * w3 + kh) ^ (4 * (g & 1) + q4)) * 32) + p4 * 8; }
#define GRAB2_DEF \
  const LAddr t2w(tw2), t2x(tw2 ^ 32); \
  auto grab = [&](int ps, u32 unit) __attribute__((always_inline)) { \
    const u32 sl = unit * 16384; \
    wf[2 * ps] = lds_tr2(t2w(sl), t2w(sl + 4096)); \
    wf[2 * ps + 1] = lds_tr2(t2x(sl), t2x(sl + 4096)); \
  }
  const f32x4 zero4 = {0.f, 0.f, 0.f, 0.f};
  if (g1) {
    const u32 ord = mi == 0 ? 0x321504u : (mi == 1 ? 0x251403u : 0x504321u);
    auto tile_of = [&](int t) __attribute__((always_inline)) -> u32 {
      const u32 pos = t < 3 ? 2 * t + (kp ^ 1) : 2 * (t - 3) + kp;
      return mi * 6 + ((ord >> (4 * pos)) & 15u);
    };
    const char* hb = ws + W_HN + kp * 65536;
    u32x2 bias[2];
    const u32 f16 = ni * 4 + w3 + kh * 144;
    ld64(bias[0], P.b1 + (ni * 4 + w3) * 64, g * 8);
    ld64(bias[1], P.b1 + (ni * 4 + w3) * 64, g * 8, 32);
    {
      const u32 cj = min((mi * 192) / 72 + ((lane >> 1) & 3), 7u);
      poll_flags(ws + W_FLAGD, (cj * 32 + kp * 16 + 2 * w + (lane & 1)) * 128, tag);
      FS(52);
      const char* tb = hb + tile_of(0) * 131072 + w * 8192;
      const u32 lb = ((w >> 1) & 1 ? 0u : 3 * 32768u) + (w >> 2) * 16384 + (w & 1) * 8192;
#pragma unroll
      for (int j = 0; j < 8; ++j) dma<64, true>(tb + j * 1024, lane16, lb, j * 1024);
    }
    STAMP(13); FS(54);
    bar_v<0>();
    if (w == 0) st_flag(ws + W_FLAGH + (rb * 8 + cg) * 128, tag);
    FS(55);
    auto issue_a = [&](int i, int j) __attribute__((always_inline)) {
      dma<64, true>(hb + tile_of(i >> 1) * 131072 + w1k + ((j >> 1) * 32 + (i & 1) * 16 + (j & 1) * 8) * 1024, lane16, w1k, ((i + 3) & 3) * 32768 + j * 8192);
    };
    const u32 aa = kh * (16384 + 256) + (g >> 1) * 1024 + (g & 1) * 512 + l15 * 16;
    const LAddr ar0(aa), ar1(aa ^ 256);
    u32 exw = 9 * 16384 + w * 1024 + lane16, exr = 9 * 16384 + (w ^ 4) * 1024 + lane16;
    pin(exw); pin(exr);
    f32x4 cc[2][2][2], ci[2];
    f32x4 kv0, kv1;
    u32x4 a[6], rx, pv[2], uo;
    pin(bias[0]); pin(bias[1]);
    auto set_init = [&](f32x4 lo, f32x4 hi) __attribute__((always_inline)) {
      ci[0] = lo; ci[1] = hi;
      pin(ci[0]); pin(ci[1]);
    };
    set_init(unpk(bias[0].x, bias[0].y), unpk(bias[1].x, bias[1].y));
    auto rd = [&](int n) __attribute__((always_inline)) {
      const u32 o = (((n >> 4) + 3) & 3) * 32768 + (n & 14) * 1024;
      a[n % 6] = lds_r128((n & 1) ? ar1(o) : ar0(o));
    };
    auto load_pv = [&](int t) __attribute__((always_inline)) {
      const char* pb = ws + W_UP + (tile_of(t) * 288 + f16) * 2048;
      ld128_sc1(pv[0], pb, lane16);
      ld128_sc1(pv[1], pb, lane16, 1024);
    };
    auto flag_u = [&](int t) __attribute__((always_inline)) {
      if (w == 0) st_flag(ws + W_FLAGE + (tile_of(t) * 36 + ni) * 128, tag);
    };
    auto exch_p = [&](int t) __attribute__((always_inline)) {
      const u32x2 p2 = pack4(cc[t & 1][1][0]), p3 = pack4(cc[t & 1][1][1]);
      rx = u32x4{p2.x, p2.y, p3.x, p3.y};
      pin(rx);
    };
    auto combine = [&](int t) __attribute__((always_inline)) {
      kv0 = cc[t & 1][0][0] + unpk(rx.x, rx.y);
      kv1 = cc[t & 1][0][1] + unpk(rx.z, rx.w);
      pin(kv0); pin(kv1);
    };
    auto send = [&](int t, int h) __attribute__((always_inline)) {
      const u32x2 p0 = pack4(h ? kv1 : kv0);
      st128_sc1(ws + W_UP + (tile_of(t) * 288 + f16) * 2048 + h * 1024, lane16, u32x4{p0.x, tag, p0.y, tag});
    };
    auto gather = [&](int t) __attribute__((always_inline)) {
      for (u32 spins = 0; spins < SPIN_CAP; ++spins) {
        pin(pv[0]); pin(pv[1]);
        u32 bad = (pv[0].y ^ tag) | (pv[0].w ^ tag) | (pv[1].y ^ tag) | (pv[1].w ^ tag);
        pin(bad);
        if (__builtin_expect(__all(bad == 0), 1)) break;
        load_pv(t);
        waitv<0>();
      }
      pin(pv[0]); pin(pv[1]);
      set_init(unpk(pv[0].x, pv[0].z), unpk(pv[1].x, pv[1].z));
    };
    auto act = [&](int j) __attribute__((always_inline)) {
      const f32x4 v = j < 2 ? kv0 : kv1;
      u32 t = (j & 1) ? pack2(gelu_tanh(v.z), gelu_tanh(v.w)) : pack2(gelu_tanh(v.x), gelu_tanh(v.y));
      pin(t);
      uo[j] = t;
    };
    auto store_u = [&](int t) __attribute__((always_inline)) { st128_sc1(ws + W_U + (tile_of(t) * 288 + f16) * 1024, lane16, uo); };
#pragma unroll
    for (int n = 0; n < 5; ++n) rd(n);
    SB();
#pragma unroll
    for (int i = 0; i < 12; ++i)
#pragma unroll
      for (int k = 0; k < 16; ++k) {
        const int n = i * 16 + k, t = i >> 1;
        if (n + 5 < 192) rd(n + 5);
#pragma unroll
        for (int e = 0; e < 2; ++e)
          cc[t & 1][n & 1][e] = mfma32(wf[(n & 30) + e], a[n % 6], (n & 30) ? cc[t & 1][n & 1][e] : ((n & 1) ? zero4 : ci[e]));
        SB();
        if (i == 0 && k < 4) issue_a(2, k);
        if (!(i & 1) && i > 0 && k == 2) exch_p(t - 1);
        if ((i & 1) && i > 1) {
          if (i < 9) { if (k == 1 || k == 4) send(t - 1, k >> 2); }
          else { if (k < 8 && !(k & 1)) act(k >> 1); if (k == 7) store_u(t - 1); }
          if (i >= 5 && i < 11 && k == 6) load_pv(t + 1);
        }
        if (k == PRIO_K1) PRIO(0);
        if (k == 8) {
          constexpr int vm[12] = {4, 4, 4, 6, 6, 8, 8, 8, 8, 9, 4, 63};
          if (!(i & 1) && i > 0) { lds_w128(exw, rx); asm volatile("s_waitcnt lgkmcnt(0)" ::: "memory"); }
          asm volatile("s_waitcnt vmcnt(%0)\n\ts_barrier" ::"n"(vm[i]) : "memory");
          PRIO(1);
          if (i == 10) flag_u(3);
          if (!(i & 1) && i > 0) rx = lds_r128(exr);
        }
        if (k >= 9 && k < 13) {
          if (i + 3 < 12) issue_a(i + 3, k - 9);
          else if (k & 1) issue_w2(2 * (i - 9) + 1 + ((k - 9) >> 1), ((i + 2) & 3) * 2 + ((k - 9) >> 1));
        }
        if (!(i & 1) && i > 0 && k == 14) combine(t - 1);
        if ((i & 1) && i >= 5 && i < 11 && k == 14) { if (i == 9) waitv<5>(); else waitv<4>(); gather(t + 1); }
        if (i == 8 && k == 13) issue_w2(0, 8);
        SB();
      }
    PRIO(0);
    STAMP(14); FS(57);
    GRAB2_DEF;
    exch_p(5);
    lds_w128(exw, rx);
    bar_v<4>();
    rx = lds_r128(exr);
    flag_u(4);
    issue_w2(7, 4); issue_w2(8, 5);
    combine(5);
#pragma unroll
    for (int j = 0; j < 4; ++j) act(j);
    store_u(5);
    grab(0, 8); grab(1, 6); grab(2, 7); grab(3, 0); grab(4, 1);
    bar_v<5>();
    {
      constexpr u32 un[6] = {8, 6, 7, 0, 1, 9};
#pragma unroll
      for (int ps = 9; ps < 15; ++ps) issue_w2(ps, un[ps - 9]);
    }
    grab(5, 2); grab(6, 3);
    bar_v<12>();
    flag_u(5);
  } else {
    if (w == 0) { poll_flags(ws + W_FLAGD, lane * 128, tag); __builtin_amdgcn_s_sleep(16); }
    bar();
    {
      const u32 part = (idx - 27) * 8 + w;
      const char* pb = P.w2 + (size_t)kq * 1152 * 4096 + kp * 2048;
      u32 d[8];
#pragma unroll
      for (int j = 0; j < 8; ++j)
        asm volatile("global_load_dword %0, %1, %2" POL_S : "=v"(d[j]) : "v"(min((j * 40 + part) * 4 + g, 1151u) * 4096 + l15 * 128), "s"(pb) : "memory");
      asm volatile("s_waitcnt vmcnt(0)" : "+v"(d[0]), "+v"(d[1]), "+v"(d[2]), "+v"(d[3]), "+v"(d[4]), "+v"(d[5]), "+v"(d[6]), "+v"(d[7])::"memory");
    }
    GRAB2_DEF;
    constexpr u32 un0[9] = {8, 6, 7, 0, 1, 2, 3, 4, 5};
    bar();
    if (w == 0) st_flag(ws + W_FLAGH + (rb * 8 + cg) * 128, tag);
#pragma unroll
    for (int ps = 0; ps < 9; ++ps) issue_w2(ps, un0[ps]);
    bar_v<0>();
    grab(0, 8); grab(1, 6); grab(2, 7); grab(3, 0); grab(4, 1);
    bar();
    {
      constexpr u32 un[6] = {8, 6, 7, 0, 1, 9};
#pragma unroll
      for (int ps = 9; ps < 15; ++ps) issue_w2(ps, un[ps - 9]);
    }
    grab(5, 2); grab(6, 3);
    bar();
  }
  STAMP(15); FS(58);

  {
    const u32 T0 = 9 * (Mq >> 1), TF = T0 + 5 * odd;
    auto owner_of = [&](int b) __attribute__((always_inline)) -> u32 { return b == 4 ? kq : (u32)(b - 1) + ((u32)(b - 1) >= kq ? 1u : 0u); };
    GRAB2_DEF;
    issue_w2(15, 2); issue_w2(16, 3);
    grab(7, 4); grab(8, 5);
    u32 fo;
    {
      const u32 b = min(lane / 9, 4u);
      fo = lane < 60 ? ((T0 + 4 * odd + b) * 36 + kq * 9 + lane % 9) * 128 : (W_FLAGH - W_FLAGE) + ((Nq * 2 + (lane & 1)) * 8 + Mq * 2 + ((lane >> 1) & 1)) * 128;
      if (lane == 58 || lane == 59) fo = (W_FLAGH - W_FLAGE) + ((Nq * 2 + (lane & 1)) * 8 + (Mq >> 1) * 4 + 2 - odd) * 128;
    }
    bar_v<4>();
    issue_w2(17, 4);
    {
      constexpr u32 un[6] = {8, 6, 7, 0, 1, 9};
#pragma unroll
      for (int ps = 9; ps < 15; ++ps) grab(ps, un[ps - 9]);
    }
    if (w == 0) poll_flags(ws + W_FLAGE, fo, tag);
    bar_v<0>();
    const char* ub = ws + W_U + kq * (36 * 1024);
    u32 uo_[5], lx = (32 + w3) * 1024;
#pragma unroll
    for (int j = 0; j < 5; ++j) {
      const u32 b = j < 4 ? 8 * j + w : 32 + w3;
      uo_[j] = (((b / 9) & 1) * 144 + (b / 18) * 18 + b % 9) * 1024;
      asm volatile("" : "+s"(uo_[j]));
    }
    asm volatile("" : "+s"(lx));
    auto issue_u = [&](int i, int j) __attribute__((always_inline)) {
      const u32 T = i == 0 ? T0 + 4 : TF + owner_of((i + 1) >> 1);
      const u32 part = (i == 0 ? 0u : (u32)((i + 1) & 1)) ^ odd;
      const char* b = ub + (T * 288 + part * 9) * 1024 + uo_[j];
      if (j < 4) dma<64, true>(b, lane16, w1k, ((i + 3) & 3) * 36864 + j * 8192);
      else dma<64, true>(b, lane16, lx, ((i + 3) & 3) * 36864);
    };
#pragma unroll
    for (int j = 0; j < 5; ++j) issue_u(0, j);
    grab(15, 2); grab(16, 3); grab(17, 4);
    bar();
    STAMP(16); FS(59);
#pragma unroll
    for (int j = 0; j < 5; ++j) issue_u(1, j);
    const u32 grp = Mq * 16 + Nq, me = odd * 4 + kq;
    u32 exw = 147456 + w * 1024 + lane16, exr = 147456 + (w ^ 4) * 1024 + lane16;
    pin(exw); pin(exr);
    const LAddr ar(kh * 18432 + lane16);
    f32x4 cc[2][2][2];
    f32x4 kv0, kv1;
    u32x4 a[6], rx, pv[6];
    u32x2 hres[2]; u32 bb;
    char* ysb = ws + W_YS + (((Mq >> 1) * 16 + Nq) * 8 + w) * 16384;
    auto rd = [&](int n) __attribute__((always_inline)) { a[n % 6] = lds_r128(ar(((n / 18 + 3) & 3) * 36864 + (n & 1) * 9216 + ((n % 18) >> 1) * 1024)); };
    auto exch_p = [&](int b) __attribute__((always_inline)) {
      const u32x2 p2 = pack4(cc[b & 1][0][1]), p3 = pack4(cc[b & 1][1][1]);
      rx = u32x4{p2.x, p2.y, p3.x, p3.y};
      pin(rx);
    };
    auto send = [&](int b, int h) __attribute__((always_inline)) {
      const u32x2 pk = pack4(h ? cc[b & 1][1][0] + unpk(rx.z, rx.w) : cc[b & 1][0][0] + unpk(rx.x, rx.y));
      const u32 o = owner_of(b ? b : 1);
      char* pb = b == 0 ? ysb + me * 2048 : ws + W_YP + (((grp * 4 + o) * 3 + ((kq - o - 1) & 3u)) * 8 + w) * 2048;
      st128_sc1(pb + h * 1024, lane16, u32x4{pk.x, tag, pk.y, tag});
    };
    auto load_res = [&](u32 T, u32 ln) __attribute__((always_inline)) -> u32 {
      const u32 srow = Nq * 128 + w3 * 32 + kh * 16 + (ln & 15), oo = srow * 1152 + T * 64 + (ln >> 4) * 8;
      ld64_sc1(hres[0], ws + W_H, oo);
      ld64_sc1(hres[1], ws + W_H + 32, oo);
      ld16(bb, P.b2, srow * 2);
      return oo;
    };
    u32 so, oo;
    auto load_ps = [&]() __attribute__((always_inline)) { ld128_sc1(pv[0], ysb, so); ld128_sc1(pv[1], ysb, so, 1024); };
    auto load_pv1 = [&](int j) __attribute__((always_inline)) {
      ld128_sc1(pv[j], ws + W_YP + ((grp * 4 + kq) * 3 * 8 + w) * 2048 + (j >> 1) * 16384, lane16, (j & 1) * 1024);
    };
    auto load_pv = [&]() __attribute__((always_inline)) {
#pragma unroll
      for (int j = 0; j < 6; ++j) load_pv1(j);
    };
    bar_vo<5>();
#pragma unroll
    for (int n = 0; n < 5; ++n) rd(n);
    SB();
#pragma unroll
    for (int i = 0; i < 9; ++i)
#pragma unroll
      for (int k = 0; k < 18; ++k) {
        const int n = i * 18 + k, b = (i + 1) >> 1, x = (i == 0 || (i & 1)) ? k : 18 + k;
        if (n + 5 < 162) rd(n + 5);
#pragma unroll
        for (int e = 0; e < 2; ++e)
          cc[b & 1][x & 1][e] = mfma32(a[n % 6], wf[(x & ~1) + e], x > 1 ? cc[b & 1][x & 1][e] : zero4);
        SB();
        if (i == 0 && k < 5) issue_u(2, k);
        if ((i & 1) && k == 2) exch_p(b - 1);
        if (k == PRIO_K2) PRIO(0);
        if (k == 5) {
          constexpr int vm[9] = {5, 5, 7, 5, 7, 10, 7, 4, 63};
          if (i & 1) { lds_w128(exw, rx); asm volatile("s_waitcnt lgkmcnt(0)" ::: "memory"); }
          asm volatile("s_waitcnt vmcnt(%0)\n\ts_barrier" ::"n"(vm[i]) : "memory");
          PRIO(1);
          if (i & 1) rx = lds_r128(exr);
        }
        if ((i & 1) && (k == 8 || k == 10)) send(b - 1, (k - 8) >> 1);
        if (k >= 11 && k < 16 && i + 3 < 9) issue_u(i + 3, k - 11);
        if (i == 4 && k == 6) { so = (lane >> 3) * 2048 + (me * 8 + (lane & 7)) * 16; load_ps(); }
        if (i == 4 && k == 8) oo = load_res(T0 + 4, me * 8 + (lane & 7));
        if (i == 6) {
          if (k == 0) {
            waitv<12>();
            for (u32 spins = 0; spins < SPIN_CAP; ++spins) {
              pin(pv[0]); pin(pv[1]);
              u32 bad = (pv[0].y ^ tag) | (pv[0].w ^ tag) | (pv[1].y ^ tag) | (pv[1].w ^ tag);
              pin(bad);
              if (__builtin_expect(__all(bad == 0), 1)) break;
              load_ps();
              waitv<0>();
            }
            pin(pv[0]); pin(pv[1]);
            kv0 = unpk(pv[0].x, pv[0].z); kv1 = unpk(pv[1].x, pv[1].z);
            pin(kv0); pin(kv1);
          }
          if (k >= 1 && k < 5) { kv0[k - 1] = sum_g(kv0[k - 1] + dpp<0x128>(kv0[k - 1])); pin(kv0); }
          if (k >= 6 && k < 10) { kv1[k - 6] = sum_g(kv1[k - 6] + dpp<0x128>(kv1[k - 6])); pin(kv1); }
          if (k == 11) {
            pin(hres[0]); pin(hres[1]); pin(bb);
            const u32x2 o0 = pack4(kv0 + unpk(hres[0].x, hres[0].y) + bflo(bb)), o1 = pack4(kv1 + unpk(hres[1].x, hres[1].y) + bflo(bb));
            const bool hi = (lane & 8) != 0;
            asm volatile("s_mov_b64 exec, 0xffff\n\tglobal_store_dwordx2 %0, %1, %2\n\ts_mov_b64 exec, -1" ::"v"(oo + (lane & 8) * 4), "v"(hi ? o1 : o0), "s"(P.out) : "memory");
          }
          if (k == 14) oo = load_res(TF + kq, lane);
        }
        if (i == 8 && k == 2) {
          waitv<2>();
          pin(hres[0]); pin(hres[1]); pin(bb);
          kv0 = unpk(hres[0].x, hres[0].y) + bflo(bb); kv1 = unpk(hres[1].x, hres[1].y) + bflo(bb);
          pin(kv0); pin(kv1);
        }
        if (i == 8 && k >= 8 && k < 14) load_pv1(k - 8);
        SB();
      }
    PRIO(0);
    exch_p(4);
    lds_w128(exw, rx);
    bar();
    rx = lds_r128(exr);
    STAMP(17); FS(60);
    for (u32 spins = 0; spins < SPIN_CAP; ++spins) {
      waitv<0>();
      u32 bad = 0;
#pragma unroll
      for (int j = 0; j < 6; ++j) { pin(pv[j]); bad |= (pv[j].y ^ tag) | (pv[j].w ^ tag); }
      pin(bad);
      if (__all(bad == 0)) break;
      load_pv();
    }
    waitv<0>();
#pragma unroll
    for (int j = 0; j < 6; ++j) pin(pv[j]);
#pragma unroll
    for (int s = 0; s < 3; ++s) { kv0 += unpk(pv[2 * s].x, pv[2 * s].z); kv1 += unpk(pv[2 * s + 1].x, pv[2 * s + 1].z); }
    const u32x2 o0 = pack4(kv0 + cc[0][0][0] + unpk(rx.x, rx.y)), o1 = pack4(kv1 + cc[0][1][0] + unpk(rx.z, rx.w));
    auto s0 = __builtin_amdgcn_permlane16_swap(o0.x, o1.x, false, false);
    auto s1 = __builtin_amdgcn_permlane16_swap(o0.y, o1.y, false, false);
    asm volatile("global_store_dwordx4 %0, %1, %2\n\ts_nop 1" ::"v"(oo + (g & 1) * 24), "v"(u32x4{s0[0], s1[0], s0[1], s1[1]}), "s"(P.out) : "memory");
  }
  asm volatile(".Lfk_code_end:");
  if (bid == 0 && threadIdx.x == 0) *(u64*)(ws + 256) = pc0;
  if (bid == 0 && w == 0) st_flag(ws + W_EPOCH, tag);
}
__global__ void k1(char* ws) { u64 t0 = *(volatile u64*)(ws + 256); u32 n = 0; while (__builtin_amdgcn_s_memrealtime() - t0 < 8000 && ++n < 100000) __builtin_amdgcn_s_sleep(2); }
}

extern "C" void kernel_launch(void* const* d_in, const int* in_sizes, int n_in, void* d_out, int out_size, void* d_ws,
                              size_t ws_size, hipStream_t stream) {
  Params p;
  p.x = (const char*)d_in[0]; p.wqkv = (const char*)d_in[1]; p.wphi = (const char*)d_in[2]; p.wo = (const char*)d_in[3];
  p.w1 = (const char*)d_in[5]; p.b1 = (const char*)d_in[6]; p.w2 = (const char*)d_in[7]; p.b2 = (const char*)d_in[8];
  p.out = (char*)d_out; p.ws = (char*)d_ws;
  (void)hipFuncSetAttribute((const void*)fused_kernel, hipFuncAttributeMaxDynamicSharedMemorySize, LDS_BYTES);
  fused_kernel<<<NWG, NT, LDS_BYTES, stream>>>(p);
  k1<<<1, 64, 0, stream>>>((char*)d_ws);
}
```

```cpp
#include <hip/hip_runtime.h>
#include <hip/hip_bf16.h>

typedef unsigned u32;
typedef unsigned long long u64;
typedef u32 u32x2 __attribute__((ext_vector_type(2)));
typedef u32 u32x4 __attribute__((ext_vector_type(4)));
typedef float f32x2 __attribute__((ext_vector_type(2)));
typedef float f32x4 __attribute__((ext_vector_type(4)));
typedef float f32x16 __attribute__((ext_vector_type(16)));
typedef short s16x4 __attribute__((ext_vector_type(4)));
typedef __bf16 bf16x2 __attribute__((ext_vector_type(2)));
typedef __bf16 bf16x8 __attribute__((ext_vector_type(8)));
#define LAS __attribute__((address_space(3)))
#define DEVINL __device__ __forceinline__

namespace {
constexpr int S = 2048, D = 576, F = 4608;
constexpr int NWG = 256, NT = 512;
constexpr int LDS_BYTES = 163840;
constexpr u32 SPIN_CAP = 1u << 16;

constexpr u32 W_EPOCH = 0;
constexpr u32 W_FLAGB = 4096;
constexpr u32 W_FLAGD = W_FLAGB + 1024 * 128;
constexpr u32 W_FLAGE = W_FLAGD + 256 * 128;
constexpr u32 W_FLAGH = W_FLAGE + 18 * 36 * 128;
constexpr u32 W_FLAGU = W_FLAGH + 256 * 128;
constexpr u32 W_FLAGY = W_FLAGU + 18 * 36 * 128;
constexpr u32 W_KVFIN = W_FLAGY + 1280 * 128;
constexpr u32 W_STATS = W_KVFIN + 8 * 96 * 128;
constexpr u32 W_KVPART = W_STATS + 32 * 4 * 8 * 256;
constexpr u32 W_O = W_KVPART + 8 * 96 * 32 * 128;
constexpr u32 W_H = W_O + 128 * 18 * 1024;
constexpr u32 W_HN = W_H + S * D * 2;
constexpr u32 W_U = W_HN + 18 * 128 * 1024;
constexpr u32 W_UP = W_U + 18 * 288 * 1024;
constexpr u32 W_YP = W_UP + 18 * 288 * 1024;
constexpr u32 W_YS = W_YP + 64 * 4 * 3 * 8 * 1024;
constexpr u32 W_END = W_YS + 32 * 8 * 8 * 1024;
constexpr u32 W_DBG = W_END;

constexpr u32 SLOT = 27648;
constexpr u32 L_XN = 3 * SLOT;
constexpr u32 L_PHI = 4 * SLOT;
constexpr u32 L_V = L_PHI + 64 * 328;
constexpr u32 L_KV = 147456;
constexpr u32 L_MISC = L_XN + 73728;
constexpr u32 L_WPHI = L_MISC;
constexpr u32 L_ZERO = L_MISC + 5760;
constexpr u32 L_CNT = L_MISC + 5824;
constexpr u32 L_HI = 5 * SLOT;
constexpr u32 L_HN = L_HI + 10240;

struct Params {
  const char* x; const char* wqkv; const char* wphi; const char* wo;
  const char* w1; const char* b1; const char* w2; const char* b2;
  char* out; char* ws;
};

DEVINL f32x4 mfma32(u32x4 a, u32x4 b, f32x4 c) {
  return __builtin_amdgcn_mfma_f32_16x16x32_bf16(__builtin_bit_cast(bf16x8, a), __builtin_bit_cast(bf16x8, b), c, 0, 0, 0);
}
DEVINL f32x16 mfmaB(u32x4 a, u32x4 b, f32x16 c) {
  return __builtin_amdgcn_mfma_f32_32x32x16_bf16(__builtin_bit_cast(bf16x8, a), __builtin_bit_cast(bf16x8, b), c, 0, 0, 0);
}
template <int Q> DEVINL f32x4 quad(const f32x16& v) { return f32x4{v[4 * Q], v[4 * Q + 1], v[4 * Q + 2], v[4 * Q + 3]}; }
DEVINL f32x4 mfma16(u32x2 a, u32x2 b, f32x4 c) {
  return mfma32(u32x4{a.x, a.y, 0u, 0u}, u32x4{b.x, b.y, 0u, 0u}, c);
}
DEVINL u32 pack2(float lo, float hi) {
  f32x2 p = {lo, hi};
  return __builtin_bit_cast(u32, __builtin_convertvector(p, bf16x2));
}
DEVINL u32x2 pack4(f32x4 v) { return u32x2{pack2(v.x, v.y), pack2(v.z, v.w)}; }
DEVINL float bflo(u32 v) { return __builtin_bit_cast(float, v << 16); }
DEVINL float bfhi(u32 v) { return __builtin_bit_cast(float, v & 0xffff0000u); }
DEVINL float asf(u32 v) { return __builtin_bit_cast(float, v); }
DEVINL u32 asu(float v) { return __builtin_bit_cast(u32, v); }
DEVINL f32x4 unpk(u32 a, u32 b) { return f32x4{bflo(a), bfhi(a), bflo(b), bfhi(b)}; }
DEVINL float dot2(u32 a, u32 b, float c) {
  return __builtin_amdgcn_fdot2_f32_bf16(__builtin_bit_cast(bf16x2, a), __builtin_bit_cast(bf16x2, b), c, false);
}
DEVINL u32x2 lds_tr(u32 addr) {
  return __builtin_bit_cast(u32x2, __builtin_amdgcn_ds_read_tr16_b64_v4i16((LAS s16x4*)addr));
}
DEVINL u32x4 lds_tr2(u32 a0, u32 a1) {
  u32x2 lo = lds_tr(a0), hi = lds_tr(a1);
  return u32x4{lo.x, lo.y, hi.x, hi.y};
}
DEVINL u32x4 lds_r128(u32 addr) { return *(LAS u32x4*)addr; }
DEVINL u32x2 lds_r64(u32 addr) { return *(LAS u32x2*)addr; }
DEVINL void lds_w128(u32 addr, u32x4 v) { *(LAS u32x4*)addr = v; }
DEVINL void lds_w64(u32 addr, u32x2 v) { *(LAS u32x2*)addr = v; }
template <int CTRL> DEVINL float dpp(float v) { return asf(__builtin_amdgcn_update_dpp(0u, asu(v), CTRL, 0xf, 0xf, true)); }
DEVINL float sum8(float v) { v += dpp<0xB1>(v); v += dpp<0x4E>(v); return v + dpp<0x141>(v); }
DEVINL float sum_g(float v) {
  auto a = __builtin_amdgcn_permlane16_swap(asu(v), asu(v), false, false);
  v = asf(a[0]) + asf(a[1]);
  auto b = __builtin_amdgcn_permlane32_swap(asu(v), asu(v), false, false);
  return asf(b[0]) + asf(b[1]);
}
#define SB() __builtin_amdgcn_sched_barrier(0)
template <int V> struct IC { static constexpr int value = V; };
template <int B, int E, class Fn> DEVINL void sfor(Fn&& f) {
  if constexpr (B < E) { f(IC<B>{}); sfor<B + 1, E>(f); }
}
#define PRIO_K1 2
#define PRIO_K2 2
#define PRIO(v)
DEVINL u32 fresh_lane() {
  u32 z = 0;
  asm volatile("" : "+v"(z));
  const u32 l = __builtin_amdgcn_mbcnt_hi(~0u, __builtin_amdgcn_mbcnt_lo(~0u, z));
  __builtin_assume(l < 64);
  return l;
}
#define LANEVARS const u32 lane = fresh_lane(), l15 = lane & 15, g = lane >> 4, q4 = l15 >> 2, p4 = lane & 3; \
  const u32 trrow = (16 * (g >> 1) + 4 * (g & 1) + q4) * 288; (void)l15; (void)g; (void)q4; (void)p4; (void)trrow
struct LAddr {
  u32 b[3];
  DEVINL explicit LAddr(u32 a) { b[0] = a; b[1] = a + 65536u; b[2] = a + 131072u; asm volatile("" : "+v"(b[0]), "+v"(b[1]), "+v"(b[2])); }
  DEVINL u32 operator()(u32 c) const { return b[c >> 16] + (c & 0xffffu); }
};

#define POL_W ""
#define POL_T ""
#define POL_S ""
#define PRETOUCH 1
#define TLBSTEP 1048576u
#define WO_BEHIND 1
#define SPJ0 4
#define RETOUCH 1
#define THR1 63
#define SEQ 0
#define MAP1 0
#define DLY_A1 12
#define DLY_A2 8
#define DLY_B 6
#define DLY_C 4
#define PF_A 0
#define PF_B 0
#define PF_C 0
#define PF_D 0
#define POL_1 ""
#define ROT1 1
#define POL_X ""
template <int NL, bool SC1> DEVINL void dma(const char* base, u32 voff, u32 lbase, u32 loff) {
  if constexpr (NL == 64) {
    if constexpr (SC1) asm volatile("s_add_u32 m0, %2, %3\n\ts_nop 0\n\tglobal_load_lds_dwordx4 %0, %1 sc1" ::"v"(voff), "s"(base), "s"(lbase), "n"(loff) : "memory", "scc");
    else asm volatile("s_add_u32 m0, %2, %3\n\ts_nop 0\n\tglobal_load_lds_dwordx4 %0, %1" POL_W ::"v"(voff), "s"(base), "s"(lbase), "n"(loff) : "memory", "scc");
  } else {
    if constexpr (SC1) asm volatile("s_add_u32 m0, %2, %3\n\ts_mov_b32 exec_hi, %4\n\tglobal_load_lds_dwordx4 %0, %1 sc1\n\ts_mov_b32 exec_hi, -1" ::"v"(voff), "s"(base), "s"(lbase), "n"(loff), "n"((1u << (NL - 32)) - 1u) : "memory", "scc");
    else asm volatile("s_add_u32 m0, %2, %3\n\ts_mov_b32 exec_hi, %4\n\tglobal_load_lds_dwordx4 %0, %1" POL_X "\n\ts_mov_b32 exec_hi, -1" ::"v"(voff), "s"(base), "s"(lbase), "n"(loff), "n"((1u << (NL - 32)) - 1u) : "memory", "scc");
  }
}
DEVINL void dma_w1(const char* base, u32 voff, u32 lds) {
  asm volatile("s_mov_b32 m0, %2\n\ts_nop 0\n\tglobal_load_lds_dwordx4 %0, %1" POL_1 ::"v"(voff), "s"(base), "s"(lds) : "memory");
}
DEVINL void st128_sc1(char* base, u32 voff, u32x4 v) {
  asm volatile("global_store_dwordx4 %0, %1, %2 sc1\n\ts_nop 1" ::"v"(voff), "v"(v), "s"(base) : "memory");
}
DEVINL void st128_sc1_m(char* base, u32 voff, u32x4 v, u64 mask) {
  asm volatile("s_mov_b64 exec, %3\n\tglobal_store_dwordx4 %0, %1, %2 sc1\n\ts_mov_b64 exec, -1\n\ts_nop 0" ::"v"(voff), "v"(v), "s"(base), "s"(mask) : "memory");
}
DEVINL void st64_sc1(char* base, u32 voff, u32x2 v) {
  asm volatile("global_store_dwordx2 %0, %1, %2 sc1" ::"v"(voff), "v"(v), "s"(base) : "memory");
}
DEVINL void st64(char* base, u32 voff, u32x2 v) {
  asm volatile("global_store_dwordx2 %0, %1, %2" ::"v"(voff), "v"(v), "s"(base) : "memory");
}
DEVINL void st32_sc1(char* base, u32 voff, u32 v) {
  asm volatile("global_store_dword %0, %1, %2 sc1" ::"v"(voff), "v"(v), "s"(base) : "memory");
}
DEVINL void st_flag(char* addr, u32 tag) {
  asm volatile("s_mov_b64 exec, 1\n\tglobal_store_dword %0, %1, %2 sc1\n\ts_mov_b64 exec, -1" ::"v"(0u), "v"(tag), "s"(addr) : "memory");
}
DEVINL void st_flag_if(u32 zero_to_store, char* addr, u32 tag) {
  asm volatile("s_cmp_lg_u32 %3, 0\n\ts_cbranch_scc1 .Lsf%=\n\ts_mov_b64 exec, 1\n\tglobal_store_dword %0, %1, %2 sc1\n\ts_mov_b64 exec, -1\n.Lsf%=:"
               ::"v"(0u), "v"(tag), "s"(addr), "s"(zero_to_store) : "memory", "scc");
}
DEVINL void flags_request(u32 sel, u32& v, const char* base, u32 voff) {
  asm volatile("s_cmp_lg_u32 %3, 0\n\ts_cbranch_scc1 .Lfq%=\n\tglobal_load_dword %0, %1, %2 sc1\n.Lfq%=:" : "+v"(v) : "v"(voff), "s"(base), "s"(sel) : "memory", "scc");
}
template <int N> DEVINL void flags_check(u32 sel, u32& v, const char* base, u32 voff, u32 tag) {
  u32 n;
  asm volatile(
      "s_cmp_lg_u32 %4, 0\n\ts_cbranch_scc1 .Lfe%=\n\ts_mov_b32 %1, %6\n\ts_waitcnt vmcnt(%7)\n"
      ".Lfl%=:\n\tv_cmp_ne_u32 vcc, %5, %0\n\ts_sub_u32 %1, %1, 1\n\ts_cbranch_vccz .Lfe%=\n\ts_cbranch_scc1 .Lfe%=\n\t"
      "global_load_dword %0, %2, %3 sc1\n\ts_waitcnt vmcnt(0)\n\ts_branch .Lfl%=\n"
      ".Lfe%=:"
      : "+v"(v), "=&s"(n) : "v"(voff), "s"(base), "s"(sel), "v"(tag), "n"(SPIN_CAP), "n"(N) : "memory", "vcc", "scc");
}
DEVINL void ld128(u32x4& d, const char* base, u32 voff, int imm) {
  asm volatile("global_load_dwordx4 %0, %1, %2 offset:%3" POL_X : "=v"(d) : "v"(voff), "s"(base), "n"(imm) : "memory");
}
DEVINL void ld128_sc1(u32x4& d, const char* base, u32 voff, int imm = 0) {
  asm volatile("global_load_dwordx4 %0, %1, %2 offset:%3 sc1" : "=v"(d) : "v"(voff), "s"(base), "n"(imm) : "memory");
}
DEVINL void ld64(u32x2& d, const char* base, u32 voff, int imm = 0) {
  asm volatile("global_load_dwordx2 %0, %1, %2 offset:%3" : "=v"(d) : "v"(voff), "s"(base), "n"(imm) : "memory");
}
DEVINL void ld32(u32& d, const char* base, u32 voff) {
  asm volatile("global_load_dword %0, %1, %2" POL_T : "=v"(d) : "v"(voff), "s"(base) : "memory");
}
DEVINL void touch(u32& d, const char* base, u32 voff) {
  asm volatile("global_load_dword %0, %1, %2" POL_T : "+v"(d) : "v"(voff), "s"(base) : "memory");
}
DEVINL void ld64_sc1(u32x2& d, const char* base, u32 voff) {
  asm volatile("global_load_dwordx2 %0, %1, %2 sc1" : "=v"(d) : "v"(voff), "s"(base) : "memory");
}
DEVINL void ld16(u32& d, const char* base, u32 voff) {
  asm volatile("global_load_ushort %0, %1, %2" : "=v"(d) : "v"(voff), "s"(base) : "memory");
}
template <int N> DEVINL void waitv() { asm volatile("s_waitcnt vmcnt(%0)" ::"n"(N) : "memory"); }
template <typename T> DEVINL void pin(T& v) { asm volatile("" : "+v"(v)); }
template <int N> DEVINL void bar_v() { asm volatile("s_waitcnt vmcnt(%0) lgkmcnt(0)\n\ts_barrier" ::"n"(N) : "memory"); }
template <int N> DEVINL void bar_vo() { asm volatile("s_waitcnt vmcnt(%0)\n\ts_barrier" ::"n"(N) : "memory"); }
DEVINL void bar() { asm volatile("s_waitcnt lgkmcnt(0)\n\ts_barrier" ::: "memory"); }

DEVINL void cnt_add(u32 c) {
  asm volatile("s_mov_b64 exec, 1\n\tds_add_u32 %0, %1\n\ts_mov_b64 exec, -1" ::"v"(L_CNT + 4 * c), "v"(1u) : "memory");
}
DEVINL void cnt_wait(u32 c) {
#pragma unroll 1
  for (u32 spins = 0; spins < (SPIN_CAP << 4); ++spins) {
    const u32 v = *(volatile LAS u32*)(L_CNT + 4 * c);
    if (__builtin_amdgcn_readfirstlane(v) >= 4u) break;
    __builtin_amdgcn_s_sleep(1);
  }
  asm volatile("" ::: "memory");
}
template <int N> DEVINL void sync4(u32 c) {
  asm volatile("s_waitcnt vmcnt(%0) lgkmcnt(0)" ::"n"(N) : "memory");
  cnt_add(c);
  cnt_wait(c);
}
DEVINL void poll_flags(const char* base, u32 voff, u32 tag) {
#pragma unroll 1
  for (u32 spins = 0; spins < SPIN_CAP; ++spins) {
    u32 v;
    asm volatile("global_load_dword %0, %1, %2 sc1\n\ts_waitcnt vmcnt(0)" : "=v"(v) : "v"(voff), "s"(base) : "memory");
    if (__all(v == tag)) break;
  }
}

#define STAMP(i)

#define FS(i)
DEVINL void feature_map2(const u32x4 (&wa)[5], const u32x2 (&wb)[5], const f32x4 (&t0)[2], const f32x4 (&t1)[2], const f32x4 (&t2)[2], u32 (&ph)[2][9]) {
  float p[2][20];
#pragma unroll
  for (int m = 0; m < 2; ++m) {
    const u32x2 p0 = pack4(t0[m]), p1 = pack4(t1[m]), p2 = pack4(t2[m]);
    const u32x4 b01 = {p0.x, p0.y, p1.x, p1.y};
    f32x4 a[5];
#pragma unroll
    for (int t = 0; t < 5; ++t) a[t] = mfma32(wa[t], b01, f32x4{0.f, 0.f, 0.f, 0.f});
#pragma unroll
    for (int t = 0; t < 5; ++t) {
      a[t] = mfma16(wb[t], p2, a[t]);
      p[m][4 * t] = a[t].x; p[m][4 * t + 1] = a[t].y; p[m][4 * t + 2] = a[t].z; p[m][4 * t + 3] = a[t].w;
    }
  }
  const float L2E = 1.44269504088896341f;
#pragma unroll
  for (int m = 0; m < 2; ++m) {
    float mx = p[m][0];
#pragma unroll
    for (int i = 1; i < 18; ++i) mx = fmaxf(mx, p[m][i]);
    const float nm = -mx * L2E;
    f32x2 sum = {0.f, 0.f};
#pragma unroll
    for (int i = 0; i < 18; ++i) p[m][i] = __builtin_amdgcn_exp2f(__builtin_fmaf(p[m][i], L2E, nm));
#pragma unroll
    for (int i = 0; i < 9; ++i) sum += f32x2{p[m][2 * i], p[m][2 * i + 1]};
    const float inv = __builtin_amdgcn_rcpf(sum.x + sum.y);
#pragma unroll
    for (int i = 0; i < 9; ++i) ph[m][i] = pack2(p[m][2 * i] * inv, p[m][2 * i + 1] * inv);
  }
}

DEVINL float gelu_tanh(float x) {
  const float C = -2.f * 0.7978845608028654f * 1.44269504088896341f;
  float y = x * __builtin_fmaf(x * x, 0.044715f * C, C);
  float e = __builtin_amdgcn_exp2f(y);
  return x * __builtin_amdgcn_rcpf(1.f + e);
}

__global__ __launch_bounds__(NT, 2) void fused_kernel(Params P) {
  extern __shared__ __attribute__((aligned(16))) char smem[];
  STAMP(0); FS(0);
  const u32 w = __builtin_amdgcn_readfirstlane(threadIdx.x >> 6);
  const u32 bid = blockIdx.x, xcd = bid & 7, idx = bid >> 3;
  char* const ws = P.ws;
  u64 pc; u32 len;
  asm volatile("s_getpc_b64 %0\n.Lfk_code_begin:\n\ts_mov_b32 %1, .Lfk_code_end-.Lfk_code_begin\n\ts_max_u32 %1, %1, .Lfk_copy_end-.Lfk_code_begin" : "=s"(pc), "=s"(len)::"scc");

  const u32 cg = (xcd & 1) * 4 + (idx & 3), rb = (xcd >> 1) * 8 + (idx >> 2);
  const u32 rot = (idx >> 2) % 6;
  const u32 rb2 = xcd * 4 + (idx >> 3), cj = idx & 7;
  const u32 w3 = w & 3, kh = w >> 2;
  const u32 kq = xcd >> 1, kp = xcd & 1;
  const bool g1 = idx < 27;
  const u32 ni = kq * 9 + (g1 ? idx / 3 : 0), mi = idx % 3;
  u32 tag, tag0;
  u32x4 wf[36];
  u32x4 xr[9];
  const u32 rgp = w ^ ((cg & 1) * 4);
  auto load_x = [&]() __attribute__((always_inline)) {
    LANEVARS;
    const u32 xo = (rb * 64 + rgp * 8 + (lane >> 3)) * 1152 + (lane & 7) * 16;
#pragma unroll
    for (int i = 0; i < 9; ++i) ld128(xr[i], P.x, xo, i * 128);
    asm volatile("s_load_dword %0, %1, 0x0" : "=s"(tag0) : "s"(ws) : "memory");
    FS(1);
  };
  auto layer_norm = [&]() __attribute__((always_inline)) {
    LANEVARS;
    f32x2 c[36];
    f32x2 s1 = {0.f, 0.f}, s2 = {0.f, 0.f};
#pragma unroll
    for (int i = 0; i < 9; ++i) {
#pragma unroll
      for (int j = 0; j < 4; ++j) {
        const f32x2 v = {bflo(xr[i][j]), bfhi(xr[i][j])};
        c[i * 4 + j] = v;
        s1 += v; s2 += v * v;
      }
    }
    const float mean = sum8(s1.x + s1.y) * (1.f / 576.f);
    const float var = fmaxf(sum8(s2.x + s2.y) * (1.f / 576.f) - mean * mean, 0.f);
    const float rs = __builtin_amdgcn_rsqf(var + 1e-5f), nb = -mean * rs;
    const u32 a0 = L_XN + ((rgp >> 1) * 18 + ((lane & 7) >> 2)) * 1024 + (lane & 3) * 256 + (((8 * (rgp & 1) + (lane >> 3)) ^ (lane & 7)) * 16);
#pragma unroll
    for (int i = 0; i < 9; ++i) {
      u32x4 o;
#pragma unroll
      for (int j = 0; j < 4; ++j) { const f32x2 v = c[i * 4 + j] * rs + nb; o[j] = pack2(v.x, v.y); }
      lds_w128(a0 + i * 2048, o);
    }
  };
#define WAIT_X(N) asm volatile("s_waitcnt vmcnt(" #N ")" : "+v"(xr[0]), "+v"(xr[1]), "+v"(xr[2]), "+v"(xr[3]), "+v"(xr[4]), "+v"(xr[5]), "+v"(xr[6]), "+v"(xr[7]), "+v"(xr[8])::"memory")
  auto unit_at = [](int u) -> u32 { return u < 6 ? 2 * SLOT + u * 8192 : (u < 9 ? (u - 6) * 8192 : (u < 12 ? SLOT + (u - 9) * 8192 : 2 * SLOT + (u - 6) * 8192)); };
  auto grab1 = [&](const LAddr& t0, const LAddr& t1, int ks, int u) __attribute__((always_inline)) {
    const u32 sl = unit_at(u);
    wf[2 * ks] = lds_tr2(t0(sl), t0(sl + 1024));
    wf[2 * ks + 1] = lds_tr2(t1(sl), t1(sl + 1024));
  };
#define T1W_DEF const u32 t1a = (8 * g + q4) * 256 + (((2 * w3) ^ (4 * (g & 1) + q4)) * 32) + p4 * 8; const LAddr t1w(t1a), t1x(t1a ^ 32)
  u32x4 hrow[2];
  u32x2 bias[2];
  const u32 wl = w3 * 3456;
  const char* wq = P.wqkv + cg * 144;
  auto stage = [&](u32 n) __attribute__((always_inline)) -> u32 { const u32 st = rot + n; return (st >= 6 ? st - 6 : st) * (96 * 3456); };
#define RING_DEF \
  const u32 r3 = lane / 18, c18 = lane % 18, seg = c18 / 9, ch9 = c18 % 9; \
  u32 wrow[4], voff[4]; \
  _Pragma("unroll") for (int i = 0; i < 4; ++i) { \
    const u32 r = 12 * w3 + 3 * i + r3; \
    wrow[i] = (r & ~12u) | ((r & 4u) << 1) | ((r & 8u) >> 1); \
    voff[i] = wrow[i] * 3456 + seg * 1152 + ch9 * 16; \
    pin(voff[i]); \
  } \
  auto issue1 = [&](int n, int j0, int j1) __attribute__((always_inline)) {   \
    constexpr u32 slot[9] = {0, 1, 2, 3, 4, 0, 1, 2, 3}; \
    const char* b = n < 6 ? wq + 1152 + stage(n) : wq; \
    const u32 qo = n < 6 ? 0u : (seg ? stage(2 * (n - 6) + 1) - 1152 : stage(2 * (n - 6))); \
    _Pragma("unroll") for (int j = j0; j < j1; ++j) \
      dma<54, false>(b + (j >> 2) * (48 * 3456), voff[j & 3] + qo, wl, slot[n] * SLOT + (j >> 2) * 13824 + (j & 3) * 864); \
  }

  if (__builtin_expect(w < 4, 1)) {
    const u32 mp = w >> 1, hp = w & 1;
    load_x();
    {
      LANEVARS;
      RING_DEF;
      WAIT_X(0);
      STAMP(1); FS(2);
      layer_norm();
      FS(3);
      issue1(1, 0, 8);
    }
    u32 ta0, ta1, xa[2];
    {
      LANEVARS;
      if (lane < 32) *(LAS u32*)(L_ZERO + lane * 4) = 0u;
      ta0 = trrow + hp * 72 + p4 * 8; ta1 = trrow + hp * 72 + 64 + (p4 & 1) * 144;
#pragma unroll
      for (int i = 0; i < 2; ++i) { xa[i] = L_XN + mp * 36864 + g * 256 + ((l15 ^ (((i + rot) & 1) * 4 + g)) * 16); pin(xa[i]); }
    }
    const LAddr trA(ta0);
    FS(4);
    asm volatile("s_waitcnt lgkmcnt(0)\n\ts_barrier\n\ts_add_u32 %0, %1, 1" : "=s"(tag) : "s"(tag0) : "memory", "scc");
    STAMP(2); FS(5);
    u32x4 xf[2][18];
    auto rdx = [&](int n) __attribute__((always_inline)) {
      u32 st = rot + n / 3; if (st >= 6) st -= 6;
      const u32 ad = xa[(n / 3 + n % 3) & 1] + st * 3072;
      xf[0][n] = lds_r128(ad + (n % 3) * 1024);
      xf[1][n] = lds_r128(ad + (n % 3) * 1024 + 18432);
    };
    f32x4 acc[5][2];
#pragma unroll
    for (int i = 0; i < 5; ++i) { acc[i][0] = f32x4{0.f, 0.f, 0.f, 0.f}; acc[i][1] = acc[i][0]; }
    u32x4 wa[5]; u32x2 wb[5];
    {
      const LAddr trM(ta1);
      u32x4 F[6];
      u32 ad[3];
      auto rd = [&](int u) __attribute__((always_inline)) {
        constexpr u32 toff[4] = {0, 32, 144, 176};
        const int n = u / 5, i = u % 5;
        const u32 b = ((n / 3) % 5) * SLOT + (n % 3) * 9216;
        F[u % 6] = i < 4 ? lds_tr2(trA(b + toff[i & 3]), trA(b + toff[i & 3] + 2304)) : lds_tr2(trM(b), trM(b + 2304));
      };
      rdx(0);
#pragma unroll
      for (int u = 0; u < 5; ++u) rd(u);
      rdx(1);
      SB();
#pragma unroll
      for (int n = 0; n < 18; ++n) {
        if (n == 14) {
          LANEVARS;
#pragma unroll
          for (int a = 0; a < 3; ++a) {
            const u32 ic = a < 2 ? 16 * a + 4 * g + q4 : 32 + q4;
            const bool ok = (ic >= 18 ? 1u : 0u) == (p4 >> 1) && (a < 2 || g == 0);
            ad[a] = ok ? L_WPHI + (p4 & 1) * 2880 + (hp * 36 + ic) * 40 : L_ZERO;
            pin(ad[a]);
          }
        }
        if (n % 3 == 2 && n < 17) { FS(6 + 2 * (n / 3)); bar_v<0>(); FS(7 + 2 * (n / 3)); }
#pragma unroll
        for (int i = 0; i < 5; ++i) {
          const int u = n * 5 + i;
          if (u + 5 < 90) rd(u + 5);
          else { wa[i] = lds_tr2(ad[0] + 8 * i, ad[1] + 8 * i); wb[i] = lds_tr(ad[2] + 8 * i); }
          if (u < 8) { rdx(2 + 2 * u); rdx(3 + 2 * u); }
          acc[i][0] = mfma32(F[u % 6], xf[0][n], acc[i][0]);
          acc[i][1] = mfma32(F[u % 6], xf[1][n], acc[i][1]);
          SB();
        }
      }
    }
    STAMP(3); FS(16);
    {
      LANEVARS;
      const u32 va = L_V + (mp * 32 + l15) * 152 + hp * 72 + g * 8;
#pragma unroll
      for (int m = 0; m < 2; ++m) {
        lds_w64(va + m * (16 * 152), pack4(acc[2][m]));
        lds_w64(va + m * (16 * 152) + 32, pack4(acc[3][m]));
        if (g == 1) lds_w64(va + m * (16 * 152) + 56, pack4(acc[4][m]));
      }
      u32 ph[2][9];
      feature_map2(wa, wb, acc[0], acc[1], acc[4], ph);
      const u32 pa = L_PHI + (mp * 32 + l15) * 328 + (hp * 4 + g) * 40;
#pragma unroll
      for (int m = 0; m < 2; ++m)
#pragma unroll
        for (int i = 0; i < 5; ++i) lds_w64(pa + m * (16 * 328) + 8 * i, u32x2{ph[m][2 * i], i < 4 ? ph[m][2 * i + 1] : 0u});
    }
    FS(17);
    bar();
    FS(18);
    bar();
    STAMP(4); FS(20);
#pragma unroll
    for (int i = 0; i < 3; ++i) { acc[i][0] = f32x4{0.f, 0.f, 0.f, 0.f}; acc[i][1] = acc[i][0]; }
    {
      u32 tq;
      { LANEVARS; tq = trrow + hp * 72 + 64; }
      const LAddr trQ(tq);
      u32x4 F[6];
      auto rd = [&](int u) __attribute__((always_inline)) {
        const int n = u / 3, i = u % 3;
        const u32 b = (1 + n / 6) * SLOT + (n % 3) * 9216 + ((n % 6) / 3) * 144;
        F[u % 6] = i < 2 ? lds_tr2(trA(b + 32 * i), trA(b + 32 * i + 2304)) : lds_tr2(trQ(b), trQ(b + 2304));
      };
#pragma unroll
      for (int u = 0; u < 5; ++u) rd(u);
      SB();
#pragma unroll
      for (int u = 0; u < 54; ++u) {
        if (u + 5 < 54) rd(u + 5);
        acc[u % 3][0] = mfma32(F[u % 6], xf[0][u / 3], acc[u % 3][0]);
        acc[u % 3][1] = mfma32(F[u % 6], xf[1][u / 3], acc[u % 3][1]);
        SB();
      }
    }
    STAMP(5); FS(21);
    if (SEQ == 2) { asm volatile("s_waitcnt lgkmcnt(0)" ::: "memory"); cnt_add(2); }
    u32 pq[2][9];
    feature_map2(wa, wb, acc[0], acc[1], acc[2], pq);
    LANEVARS;
    u32 oa[3];
    {
      const bool same = (q4 >> 1) == (g >> 1);
#pragma unroll
      for (int t = 0; t < 3; ++t) {
        const u32 slot = 4 * t + p4;
        const u32 cc = slot < 9 ? 9 * (q4 & 1) + slot : 18;
        oa[t] = same ? L_KV + ((hp * 2 + (g >> 1)) * 19 + cc) * 96 + (g & 1) * 48 : L_ZERO;
        pin(oa[t]);
      }
    }
    const char* w1b = P.w1 + (size_t)(kp * 1024 + w * 4) * 9216 + ni * 256;
    u32 w1off = g * 9216 + ((((l15 >> 1) ^ (g | ((w & 2) << 1))) << 1) | (l15 & 1)) * 16, lane16 = lane * 16;
    T1W_DEF;
    auto issue_w1 = [&](int k0, int u0, int n) __attribute__((always_inline)) {
      if (g1)
#pragma unroll
      for (int j = 0; j < n; ++j) {
        u32 r = j + ROT1 * ((mi * n) / 3); if (r >= (u32)n) r -= n;
        const u32 u = u0 + r;
        const u32 lds = (u < 6 ? 2 * SLOT + u * 8192 : (u < 9 ? (u - 6) * 8192 : (u < 12 ? SLOT + (u - 9) * 8192 : 2 * SLOT + (u - 6) * 8192))) + w * 1024;
        const char* b = w1b + (size_t)(k0 + r) * (32 * 9216);
        dma_w1(b, w1off, lds);
        dma_w1(b + 16 * 9216, w1off, lds + 4096);
        waitv<THR1>();
      }
    };
    pin(w1off); pin(lane16);
#pragma unroll
    for (int m = 0; m < 2; ++m)
#pragma unroll
      for (int i = 0; i < 9; ++i) pin(pq[m][i]);
    if (SEQ == 2) { cnt_wait(2); issue_w1(0, 0, 6); }
    u32 cpf = 0;
    if (RETOUCH & 1) touch(cpf, (const char*)pc, min(((idx * 4 + w) * 4 + p4) * 128u, len));
    STAMP(6); FS(22);
    bar();
    STAMP(7); FS(23);
    {
      u32x4 k0[3], k1[3]; u32x2 k2[3];
#pragma unroll
      for (int t = 2; t >= 0; --t) { k0[t] = lds_r128(oa[t]); k1[t] = lds_r128(oa[t] + 16); k2[t] = lds_r64(oa[t] + 32); }
      SB();
      f32x4 nu[2][3];
#pragma unroll
      for (int t = 2; t >= 0; --t)
#pragma unroll
        for (int m = 0; m < 2; ++m) {
          f32x4 c = mfma32(k0[t], u32x4{pq[m][0], pq[m][1], pq[m][2], pq[m][3]}, f32x4{0.f, 0.f, 0.f, 0.f});
          c = mfma32(k1[t], u32x4{pq[m][4], pq[m][5], pq[m][6], pq[m][7]}, c);
          nu[m][t] = mfma16(k2[t], u32x2{pq[m][8], 0u}, c);
        }
      char* ob = ws + W_O + (rb * 4 + mp * 2) * 18432;
#pragma unroll
      for (int m = 0; m < 2; ++m) {
        const float inv = __builtin_amdgcn_rcpf(nu[m][2].y + 1e-6f);
        const u32x2 a = pack4(nu[m][0] * inv), b = pack4(nu[m][1] * inv);
        st128_sc1(ob + m * 18432 + (cg * 2 + hp) * 1024, lane16, u32x4{a.x, a.y, b.x, b.y});
        const u32 e0 = asu(nu[m][2].x * inv);
        auto t16 = __builtin_amdgcn_permlane16_swap(e0, e0, false, false);
        const u32 e01 = pack2(asf(t16[0]), asf(t16[1]));
        auto t32 = __builtin_amdgcn_permlane32_swap(e01, e01, false, false);
        asm volatile("s_mov_b64 exec, 0xffff\n\tglobal_store_dwordx2 %0, %1, %2 sc1\n\ts_mov_b64 exec, -1" ::"v"(lane16), "v"(u32x2{t32[0], t32[1]}), "s"(ob + m * 18432 + 16384 + cg * 256 + hp * 8) : "memory");
      }
    }
    FS(24);
    waitv<0>();
    pin(cpf);
    st_flag(ws + W_FLAGB + ((rb * 2 + mp) * 16 + cg * 2 + hp) * 128, tag);
    cnt_add(11);
    STAMP(8); FS(25);
    cnt_wait(SEQ ? 5 : 6);
    FS(42);
    ld64(bias[0], P.b1 + (ni * 4 + w3) * 64, g * 8);
    ld64(bias[1], P.b1 + (ni * 4 + w3) * 64, g * 8, 32);
    if (!g1) {
      const char* pb = P.w1 + (size_t)kp * 1024 * 9216 + kq * 2304;
      u32 d = 0;
#pragma unroll 1
      for (u32 j = SPJ0; j < 9; ++j) {
        asm volatile("s_waitcnt vmcnt(0)" : "+v"(d)::"memory");
        const u32 r = min(j * 60 + ((idx - 27) * 4 + w) * 3 + min(lane / 18, 2u), 511u);
        const u32 row = r < 96 ? 288 + r : (r < 256 ? 640 + r : (r < 384 ? 128 + r : 512 + r));
        touch(d, pb, row * 9216 + (lane % 18) * 128);
      }
      asm volatile("s_waitcnt vmcnt(0)" : "+v"(d)::"memory");
    }
    if (SEQ == 2) issue_w1(6, 6, 3); else issue_w1(0, 0, 9);
    if (SEQ == 0) {
      cnt_wait(7);
      issue_w1(16, 9, 7);
    }
    sync4<SEQ ? 0 : 14>(0);
    pin(bias[0]); pin(bias[1]);
#pragma unroll
    for (int ks = 0; ks < 9; ++ks) grab1(t1w, t1x, ks, ks);
    sync4<63>(1);
    FS(44);
    if (RETOUCH & 2) touch(cpf, (const char*)pc, min(((idx * 4 + w) * 4 + p4) * 128u, len));
    issue_w1(9, 0, 3);
    issue_w1(23, 3, 5);
    if (SEQ) { cnt_wait(7); issue_w1(16, 9, 7); }
    FS(38);
    bar_vo<0>();
    pin(cpf);
#pragma unroll
    for (int ks = 9; ks < 12; ++ks) grab1(t1w, t1x, ks, ks - 9);
    hrow[0] = lds_r128(L_HI + (w * 8 + (lane >> 3)) * 160 + (lane & 7) * 16);
    hrow[1] = lds_r128(L_HI + (w * 8 + (lane & 7)) * 160 + 128);
    FS(40);
    bar();
  } else {
    LANEVARS;
    const u32 lane16 = lane * 16;
    u32 pfd = 0;
    asm volatile("global_load_dword %0, %1, %2" : "+v"(pfd) : "v"(min(((idx * 4 + w3) * 4 + p4) * 128u, len)), "s"(pc) : "memory");
    load_x();
    RING_DEF;
    u32 wp[3];
    {
      const u32 so = (min(lane, 59u) / 10) * 36 + min(lane % 10, 8u) * 4;
#pragma unroll
      for (int j = 0; j < 3; ++j) ld32(wp[j], P.wphi + cg * 2592 + w3 * 648 + j * 216, so);
    }
    issue1(0, 0, 8);
    WAIT_X(8);
    FS(2);
    pin(wp[0]); pin(wp[1]); pin(wp[2]);
    layer_norm();
    if (lane < 60) {
#pragma unroll
      for (int j = 0; j < 3; ++j) {
        *(LAS u32*)(L_WPHI + w3 * 720 + j * 240 + lane * 4) = wp[j];
        *(LAS u32*)(L_WPHI + 2880 + w3 * 720 + j * 240 + lane * 4) = wp[j] ^ 0x80008000u;
      }
    }
    const char* pw1 = P.w1 + (size_t)(kp * 1024 + idx * 4 + w3) * 9216 + kq * 2304;
    auto pf = [&](int r0, int n) __attribute__((always_inline)) {
      touch(pfd, pw1 + (size_t)r0 * (128 * 9216), n ? min(r3, (u32)n - 1) * (128 * 9216) + c18 * 128 : 0u - (u32)r0 * 0u);
    };
    FS(4);
    asm volatile("s_waitcnt vmcnt(0) lgkmcnt(0)\n\ts_barrier\n\ts_add_u32 %0, %1, 1" : "=s"(tag) : "s"(tag0) : "memory", "scc");
    __builtin_amdgcn_s_setprio(1);
    issue1(2, 0, 8);
    const u32 srow = 8 * g + q4;
    u32 e_vb0 = L_V + srow * 152 + (w3 >> 1) * 72 + ((w3 & 1) ? 40 : 0) + p4 * 8, e_vb1 = L_V + srow * 152 + (w3 >> 1) * 72 + 32;
    u32 e_pb = L_PHI + srow * 328 + w3 * 80 + p4 * 8, e_pb2 = L_PHI + srow * 328 + w3 * 80 + 64 + (p4 & 1) * 8;
    u32 e_s0 = (lane >> 3) * 4096 + (lane & 7) * 16, e_s1[3];
    const u32 colp = l15 == 4 ? 2u : l15 - 2 * (w3 & 1);
#pragma unroll
    for (int i = 0; i < 3; ++i) {
      const u32 sidx = (160 + min(colp, 2u) * 10 + 4 * i + g);
      e_s1[i] = (sidx >> 3) * 4096 + (sidx & 7) * 16;
      pin(e_s1[i]);
    }
    const u64 e_m01 = __ballot(l15 == 4 || colp < 2), e_m2 = __ballot(l15 == 4 || (colp < 2 && g < 2));
    pin(e_vb0); pin(e_vb1); pin(e_pb); pin(e_pb2); pin(e_s0);
    u32 vow[4];
#pragma unroll
    for (int i = 0; i < 4; ++i) { vow[i] = (9 * (wrow[i] >> 3) + (wrow[i] & 7) + seg * 324) * 1152 + ch9 * 16; pin(vow[i]); }
    FS(6);
    bar_vo<8>();
    issue1(3, 0, 8);
    u32 la[3], vo2[8];
    {
#pragma unroll
      for (int a = 0; a < 3; ++a) {
        const u32 r = a * 64 + lane;
        const u32 rr = r - 160, cp = (rr * 205) >> 11;
        const u32 cc = r < 160 ? (r & 15) + 2 * (w3 & 1) : (cp >= 2 ? 18 : ((w3 & 1) ? cp : 16 + cp));
        const u32 piece = r < 160 ? (r >> 4) : rr - cp * 10;
        la[a] = r < 190 ? L_KV + (w3 * 19 + cc) * 96 + piece * 8 + (piece >= 5 ? 8 : 0) : L_KV + 40;
        pin(la[a]);
      }
    }
    FS(8);
    bar_vo<8>();
    issue1(4, 0, 8);
    {
#pragma unroll
      for (int j = 0; j < 8; ++j) {
        const u32 kap = 192 + (j >> 2) * 48 + wrow[j & 3] + seg * 288;
        vo2[j] = (kap < 512 ? 9 * (kap >> 3) + (kap & 7) : 9 * (kap - 512) + 8) * 1152 + ch9 * 16;
        pin(vo2[j]);
      }
    }
    FS(10);
    bar_vo<8>();
    issue1(5, 0, 8);
    FS(12);
    bar_vo<8>();
    {
      touch(pfd, P.wo, min((idx * 4 + w3) * 41 + min(lane, 40u), 5183u) * 128);
    }
    {
      u32 pt = W_STATS + (rb2 * 4 + w3) * 2048 + cj * 256 + (lane & 1) * 128;
      if (lane == 2) pt = W_KVFIN + (cg * 96 + rb * 3 + min(w3, 2u)) * 128;
      if (lane == 3) pt = W_FLAGB + ((rb * 2 + (w3 >> 1)) * 16 + cg * 2 + (w3 & 1)) * 128;
      if (lane == 4) pt = W_FLAGD + (cj * 32 + rb2) * 128;
      if (lane >= 5 && lane < 8) pt = W_FLAGE + min((bid * 4 + w3) * 3 + lane - 5, (W_KVFIN - W_FLAGE) / 128 - 1) * 128;
      touch(pfd, ws, pt);
    }
    if (PF_A) pf(0, PF_A);
    FS(14);
    bar_vo<2 + (PF_A ? 1 : 0)>();
    issue1(6, 0, 8);
    if (PF_B) pf(PF_A, PF_B);
    issue1(7, 0, 8);
    issue1(8, 0, 8);
    const char* wob = P.wo + cj * 144;
    __builtin_amdgcn_s_setprio(3);
    FS(17);
    bar();
    FS(18);
    {
      u32x4 vf[2][2], pf_[2][3];
#pragma unroll
      for (int ks = 0; ks < 2; ++ks) {
        vf[ks][0] = lds_tr2(e_vb0 + ks * (32 * 152), e_vb0 + ks * (32 * 152) + 4 * 152);
        pf_[ks][0] = lds_tr2(e_pb + ks * (32 * 328), e_pb + ks * (32 * 328) + 4 * 328);
        pf_[ks][1] = lds_tr2(e_pb + ks * (32 * 328) + 32, e_pb + ks * (32 * 328) + 32 + 4 * 328);
        pf_[ks][2] = lds_tr2(e_pb2 + ks * (32 * 328), e_pb2 + ks * (32 * 328) + 4 * 328);
      }
#pragma unroll
      for (int ks = 0; ks < 2; ++ks) vf[ks][1] = lds_tr2(e_vb1 + ks * (32 * 152), e_vb1 + ks * (32 * 152) + 4 * 152);
      SB();
      f32x4 kv[2][3];
#pragma unroll
      for (int vt = 0; vt < 2; ++vt)
#pragma unroll
        for (int ks = 0; ks < 2; ++ks) {
          if (vt && l15 >= 4) vf[ks][1] = u32x4{0x3f803f80u, 0x3f803f80u, 0x3f803f80u, 0x3f803f80u};
#pragma unroll
          for (int i = 0; i < 3; ++i) kv[vt][i] = mfma32(pf_[ks][i], vf[ks][vt], ks ? kv[vt][i] : f32x4{0.f, 0.f, 0.f, 0.f});
        }
      char* kb = ws + W_KVPART + ((cg * 96 + w3 * 24) * 32 + rb) * 128;
#pragma unroll
      for (int i = 0; i < 3; ++i) {
        const u32x2 pk = pack4(kv[0][i]);
        st128_sc1_m(kb + i * 32768, e_s0, u32x4{pk.x, tag, pk.y, tag}, i < 2 ? ~0ull : 0xffffffffull);
      }
#pragma unroll
      for (int i = 0; i < 3; ++i) {
        const u32x2 pk = pack4(kv[1][i]);
        st128_sc1_m(kb, e_s1[i], u32x4{pk.x, tag, pk.y, tag}, i < 2 ? e_m01 : e_m2);
      }
    }
    FS(19);
    if (PF_C) pf(PF_A + PF_B, PF_C);
    bar_v<6 + (PF_C ? 1 : 0)>();
    u32x2 xres[5];
    auto issue_wo = [&](int part) __attribute__((always_inline)) {
#pragma unroll
      for (int j = 0; j < 8; ++j) dma<54, false>(wob + (part * 108 + (j >> 2) * 54) * 1152, vow[j & 3], wl, part * 4 * SLOT + (j >> 2) * 13824 + (j & 3) * 864);
    };
    issue_wo(0);
    if (DLY_A1) __builtin_amdgcn_s_sleep(DLY_A1);
    FS(26);
    {
      const u32 line = cg * 96 + rb * 3 + min(w3, 2u);
      const char* pb = ws + W_KVPART + line * 4096;
      u32x4 v[4];
#pragma unroll
      for (int i = 0; i < 4; ++i) ld128_sc1(v[i], pb, lane16, i * 1024);
      waitv<0>();
#pragma unroll 1
      for (u32 spins = 0; spins < SPIN_CAP; ++spins) {
        u32 bad = 0;
#pragma unroll
        for (int i = 0; i < 4; ++i) { pin(v[i]); bad |= (v[i].y ^ tag) | (v[i].w ^ tag); }
        pin(bad);
        if (__all(bad == 0)) break;
#pragma unroll
        for (int i = 0; i < 4; ++i) ld128_sc1(v[i], pb, lane16, i * 1024);
        waitv<0>();
      }
#pragma unroll
      for (int i = 0; i < 4; ++i) pin(v[i]);
      f32x4 sum = (unpk(v[0].x, v[0].z) + unpk(v[1].x, v[1].z)) + (unpk(v[2].x, v[2].z) + unpk(v[3].x, v[3].z));
#pragma unroll
      for (int j = 0; j < 4; ++j) sum[j] = sum_g(sum[j] + dpp<0x128>(sum[j]));
      const u32x2 pk = pack4(sum);
      if (w3 < 3) st128_sc1_m(ws + W_KVFIN + line * 128, lane16, u32x4{pk.x, tag, pk.y, tag}, 0xffull);
    }
    FS(27);
    if (PF_D) pf(PF_A + PF_B + PF_C, PF_D);
    {
      issue_wo(1);
      if (DLY_A2) __builtin_amdgcn_s_sleep(DLY_A2);
      FS(28);
      const char* fb = ws + W_KVFIN + (cg * 4 + w3) * 3072;
      u32x4 v[3];
#pragma unroll
      for (int i = 0; i < 3; ++i) ld128_sc1(v[i], fb, lane16, i * 1024);
      {
        const u32 xo = (rb2 * 64 + w3 * 16 + l15) * 1152 + cj * 144;
#pragma unroll
        for (int n = 0; n < 4; ++n) ld64(xres[n], P.x, xo + g * 8, n * 32);
        ld64(xres[4], P.x, xo + (g & 1) * 8, 128);
      }
      waitv<5>();
#pragma unroll 1
      for (u32 spins = 0; spins < SPIN_CAP; ++spins) {
        u32 bad = 0;
#pragma unroll
        for (int i = 0; i < 3; ++i) { pin(v[i]); bad |= (v[i].y ^ tag) | (v[i].w ^ tag); }
        pin(bad);
        if (__all(bad == 0)) break;
#pragma unroll
        for (int i = 0; i < 3; ++i) ld128_sc1(v[i], fb, lane16, i * 1024);
        waitv<0>();
      }
#pragma unroll
      for (int i = 0; i < 3; ++i) { pin(v[i]); lds_w64(la[i], u32x2{v[i].x, v[i].z}); }
    }
    FS(22);
    pin(pfd);
    __builtin_amdgcn_s_setprio(0);
    bar();
#pragma unroll
    for (int j = 0; j < 8; ++j) dma<54, false>(wob, vo2[j], wl, SLOT + (j >> 2) * 13824 + (j & 3) * 864);
    const LAddr wo0(trrow + p4 * 8), wo4(trrow + 128 + (p4 & 1) * 8);
    auto rdw = [&](u32 b, int n) __attribute__((always_inline)) -> u32x4 {
      return n < 4 ? lds_tr2(wo0(b + 32 * n), wo0(b + 32 * n + 2304)) : lds_tr2(wo4(b), wo4(b + 2304));
    };
    u32x4 wm[30];
#pragma unroll
    for (int u = 0; u < 30; ++u) wm[u] = rdw(((u / 5) % 3) * 9216 + (u / 15) * 144, u % 5);
    sync4<0>(5);
    f32x4 hc[5];
#pragma unroll
    for (int n = 0; n < 5; ++n) { pin(xres[n]); hc[n] = unpk(xres[n].x, xres[n].y); pin(hc[n]); }
    {
      u32x4 F[6];
      auto rd = [&](int u) __attribute__((always_inline)) {
        const int v = u / 5;
        F[u % 6] = rdw((v < 6 ? 4 * SLOT : SLOT) + (v % 3) * 9216 + ((v / 3) & 1) * 144, u % 5);
      };
#pragma unroll
      for (int u = 0; u < 5; ++u) rd(u);
      FS(29);
      __builtin_amdgcn_s_setprio(3);
      cnt_wait(11);
      if (DLY_B) __builtin_amdgcn_s_sleep(DLY_B);
      poll_flags(ws + W_FLAGB + (rb2 * 2 + (w3 >> 1)) * 2048, l15 * 128, tag);
      STAMP(9); FS(30);
      constexpr int ord[18] = {3, 4, 5, 12, 13, 14, 6, 7, 8, 15, 16, 17, 0, 1, 2, 9, 10, 11};
      u32x4 of[18];
      const char* ob = ws + W_O + (rb2 * 4 + w3) * 18432;
#pragma unroll
      for (int j = 0; j < 18; ++j) ld128_sc1(of[j], ob + (ord[j] >> 2) * 4096, lane16, (ord[j] & 3) * 1024);
      cnt_add(6);
      FS(31);
#pragma unroll
      for (int j = 0; j < 18; ++j) {
        asm volatile("s_waitcnt vmcnt(%1)" : "+v"(of[j]) : "n"(17 - j) : "memory");
#pragma unroll
        for (int n = 0; n < 5; ++n) {
          const int u = j * 5 + n;
          if (u + 5 < 60) rd(u + 5);
          hc[n] = mfma32(u < 60 ? F[u % 6] : wm[u - 60], of[j], hc[n]);
          SB();
        }
      }
    }
    STAMP(10); FS(32);
    {
    LANEVARS;
    const u32 lane16 = lane * 16;
    float s1 = 0.f, s2 = 0.f;
    u32x2 hr[5];
#pragma unroll
    for (int n = 0; n < 5; ++n) {
      hr[n] = pack4(hc[n]);
      if (n == 4 && g >= 2) hr[n] = u32x2{0u, 0u};
      s1 = dot2(hr[n].x, 0x3f803f80u, s1); s1 = dot2(hr[n].y, 0x3f803f80u, s1);
      s2 = dot2(hr[n].x, hr[n].x, s2); s2 = dot2(hr[n].y, hr[n].y, s2);
    }
    s1 = sum_g(s1); s2 = sum_g(s2);
    char* sb = ws + W_STATS + (rb2 * 4 + w3) * 2048;
    st128_sc1_m(sb + cj * 256, lane16, u32x4{asu(s1), tag, asu(s2), tag}, 0xffffull);
    cnt_add(7);
    FS(34);
    u32 hw = L_HI + (w3 * 16 + l15) * 160 + g * 8;
#pragma unroll
    for (int n = 0; n < 5; ++n) {
      lds_w64(hw + n * 32, hr[n]);
      hc[n] = unpk(hr[n].x, hr[n].y);
      pin(hc[n]);
    }
    u32 hrd = L_HN + (w3 * 16 + 8 * (g & 1) + q4) * 160 + ((g >> 1) * 16 + p4 * 4) * 2;
    u32 hrx = L_HN + (w3 * 16 + 8 * (g & 1) + q4) * 160 + (64 + (p4 & 1) * 4) * 2;
    u32 ho[2];
#pragma unroll
    for (int j = 0; j < 2; ++j) {
      const u32 d = cj * 72 + (j ? 64 + (l15 & 7) : (g >> 1) * 16 + l15);
      ho[j] = (d >> 5) * 131072 + (g & 1) * 512 + (d & 31) * 16;
      pin(ho[j]);
    }
    T1W_DEF;
    pin(hw); pin(hrd); pin(hrx);
    char* hb = ws + W_HN + (rb2 * 4 + w3) * 1024;
    u32x4 v0, v1;
    if (DLY_C) __builtin_amdgcn_s_sleep(DLY_C);
    FS(35);
#pragma unroll 1
    for (u32 spins = 0; spins < SPIN_CAP; ++spins) {
      ld128_sc1(v0, sb, lane16);
      ld128_sc1(v1, sb, lane16, 1024);
      waitv<0>();
      pin(v0); pin(v1);
      u32 bad = (v0.y ^ tag) | (v0.w ^ tag) | (v1.y ^ tag) | (v1.w ^ tag);
      pin(bad);
      if (__all(bad == 0)) break;
    }
    FS(36);
    s1 = sum_g(asf(v0.x) + asf(v1.x));
    s2 = sum_g(asf(v0.z) + asf(v1.z));
    const float mean = s1 * (1.f / 576.f);
    const float var = fmaxf(s2 * (1.f / 576.f) - mean * mean, 0.f);
    const float r1 = __builtin_amdgcn_rsqf(var + 1e-5f);
    const float r2 = r1 * __builtin_amdgcn_rsqf(var * r1 * r1 + 1e-5f);
    const float nb = -mean * r2;
#pragma unroll
    for (int n = 0; n < 5; ++n) lds_w64(hw + 10240 + n * 32, pack4(hc[n] * r2 + nb));
    const u32x4 f0 = lds_tr2(hrd, hrd + 4 * 160), f1 = lds_tr2(hrd + 64, hrd + 64 + 4 * 160), f2 = lds_tr2(hrx, hrx + 4 * 160);
    STAMP(11); FS(37);
    st128_sc1(hb, ho[0], f0);
    st128_sc1(hb + 131072, ho[0], f1);
    st128_sc1_m(hb, ho[1], f2, 0x00ff00ffull);
    ld64(bias[0], P.b1 + (ni * 4 + w3) * 64, g * 8);
    ld64(bias[1], P.b1 + (ni * 4 + w3) * 64, g * 8, 32);
    sync4<0>(10);
    pin(bias[0]); pin(bias[1]);
    if (w == 4) st_flag(ws + W_FLAGD + (cj * 32 + rb2) * 128, tag);
    __builtin_amdgcn_s_setprio(0);
    FS(38);
    asm volatile("s_barrier" ::: "memory");
#pragma unroll
    for (int ks = 0; ks < 12; ++ks) grab1(t1w, t1x, ks, ks < 7 ? ks + 9 : ks - 4);
    hrow[0] = lds_r128(L_HI + (w * 8 + (lane >> 3)) * 160 + (lane & 7) * 16);
    hrow[1] = lds_r128(L_HI + (w * 8 + (lane & 7)) * 160 + 128);
    FS(40);
    bar_v<0>();
    asm volatile(".Lfk_copy_end:");
    }
  }
  STAMP(12); FS(39);
  LANEVARS;
  const u32 lane16 = lane * 16;
  const u32 wcol = ((((l15 >> 1) ^ ((w * 4 + g) & 7)) << 1) | (l15 & 1)) * 16;

  const u32 Mq = idx & 3, Nq = kp * 8 + (idx >> 2), odd = Mq & 1;
  const char* w2b = P.w2 + (size_t)(kq * 1152 + w * 4) * 4096 + Nq * 256;
  const u32 w2off = g * 4096 + wcol;
  u32 w1k = w * 1024;
  asm volatile("" : "+s"(w1k));
  auto issue_w2 = [&](int ps, u32 unit) __attribute__((always_inline)) {
    const char* b = w2b + (ps < 9 ? ps + 9 * odd : ps - 9 * odd) * (32 * 4096);
    dma<64, false>(b, w2off, w1k, unit * 16384);
    dma<64, false>(b + 18 * (32 * 4096), w2off, w1k, unit * 16384 + 8192);
  };
  auto store_h = [&]() __attribute__((always_inline)) {
    char* hb = ws + W_H + (rb2 * 64 + w * 8) * 1152 + cj * 144;
    st128_sc1(hb, (lane >> 3) * 1152 + (lane & 7) * 16, hrow[0]);
    st128_sc1(hb + 128, (lane & 7) * 1152, hrow[1]);
  };
  u32 tw2;
  { LANEVARS; tw2 = kh * 8192 + (4 * g + q4) * 256 + (((2 * w3 + kh) ^ (4 * (g & 1) + q4)) * 32) + p4 * 8; }
#define GRAB2_DEF \
  const LAddr t2w(tw2), t2x(tw2 ^ 32); \
  auto grab = [&](int ps, u32 unit) __attribute__((always_inline)) { \
    const u32 sl = unit * 16384; \
    wf[2 * ps] = lds_tr2(t2w(sl), t2w(sl + 4096)); \
    wf[2 * ps + 1] = lds_tr2(t2x(sl), t2x(sl + 4096)); \
  }
  const f32x4 zero4 = {0.f, 0.f, 0.f, 0.f};
  const u32 ord = mi == 0 ? 0x321504u : (mi == 1 ? 0x251403u : 0x504321u);
  auto tile_of = [&](int t) __attribute__((always_inline)) -> u32 {
    const u32 pos = t < 3 ? 2 * t + (kp ^ 1) : 2 * (t - 3) + kp;
    return mi * 6 + ((ord >> (4 * pos)) & 15u);
  };
  const u32 f16 = ni * 4 + w3 + kh * 144;
  auto flag_u = [&](int t) __attribute__((always_inline)) {
    st_flag_if(w | (g1 ? 0u : 1u), ws + W_FLAGE + (tile_of(t) * 36 + ni) * 128, tag);
  };
  f32x4 kv0 = zero4, kv1 = zero4;
  u32x4 uo;
  auto act = [&](int j) __attribute__((always_inline)) {
    const f32x4 v = j < 2 ? kv0 : kv1;
    u32 t = (j & 1) ? pack2(gelu_tanh(v.z), gelu_tanh(v.w)) : pack2(gelu_tanh(v.x), gelu_tanh(v.y));
    pin(t);
    uo[j] = t;
  };
  auto store_u = [&](int t) __attribute__((always_inline)) { st128_sc1(ws + W_U + (tile_of(t) * 288 + f16) * 1024, lane16, uo); };
  const char* pw2 = P.w2 + (size_t)kq * 1152 * 4096 + kp * 2048;
  if (g1) {
    const char* hb = ws + W_HN + kp * 65536;
    static constexpr u32 reg1[12] = {2, 3, 1, 2, 3, 0, 1, 2, 3, 0, 1, 2};
    static constexpr int stile[12] = {0, 1, 0, 1, 2, 2, 3, 3, 4, 4, 5, 5}, spart[12] = {0, 0, 1, 1, 0, 1, 0, 1, 0, 1, 0, 1};
    {
      const u32 cp = min((mi * 192) / 72 + ((lane >> 1) & 3), 7u);
      poll_flags(ws + W_FLAGD, (cp * 32 + kp * 16 + kh * 8 + w3 + (lane & 1) * 4) * 128, tag);
      FS(52);
#pragma unroll
      for (int j = 0; j < 8; ++j)
        dma<64, true>(hb + tile_of(j >> 2) * 131072 + (kh * 32 + w3 * 4 + (j & 3)) * 1024, lane16, w * 4096, reg1[j >> 2] * 32768 + (j & 3) * 1024);
    }
    auto issue_a = [&](int i, int j) __attribute__((always_inline)) {
      dma<64, true>(hb + tile_of(stile[i]) * 131072 + w1k + ((j >> 1) * 32 + spart[i] * 16 + (j & 1) * 8) * 1024, lane16, w1k, reg1[i] * 32768 + j * 8192);
    };
    const char* w1l = P.w1 + (size_t)(kp * 1024 + 384 + w * 4) * 9216 + ni * 256;
    const u32 w1lo = g * 9216 + ((((l15 >> 1) ^ (g | ((w & 2) << 1))) << 1) | (l15 & 1)) * 16;
    static constexpr u32 lun[4] = {0, 1, 8, 9};
    auto issue_wl = [&](int j) __attribute__((always_inline)) {
      dma<64, false>(w1l + (size_t)((j & 1) * 512 + (j >> 1) * 32) * 9216, w1lo, w1k, lun[j >> 1] * 16384 + (j & 1) * 8192);
    };
    u32 t1l = kh * 8192 + (8 * g + q4) * 256 + (((2 * w3) ^ (4 * (g & 1) + q4)) * 32) + p4 * 8;
    pin(t1l);
    STAMP(13); FS(54);
    bar_vo<4>();
    FS(55);
    const u32 aa = kh * (16384 + 256) + (g >> 1) * 1024 + (g & 1) * 512 + l15 * 16;
    const LAddr ar0(aa), ar1(aa ^ 256);
    u32 exw = 9 * 16384 + w * 1024 + lane16, exr = 9 * 16384 + (w ^ 4) * 1024 + lane16;
    pin(exw); pin(exr);
    f32x4 cc[2][2][2], ci[2];
    u32x4 a[6], rx, pv;
    u32 pfd = 0, fv = 0, zo = 0;
    pin(zo);
    pin(bias[0]); pin(bias[1]);
    ci[0] = unpk(bias[0].x, bias[0].y); ci[1] = unpk(bias[1].x, bias[1].y);
    auto rd = [&](int n) __attribute__((always_inline)) {
      const u32 o = reg1[n >> 4] * 32768 + (n & 14) * 1024;
      a[n % 6] = lds_r128((n & 1) ? ar1(o) : ar0(o));
    };
    auto load_pv = [&](int t) __attribute__((always_inline)) { ld128_sc1(pv, ws + W_UP + (tile_of(t) * 288 + f16) * 1024, lane16); };
    auto flag_at = [&](int t) __attribute__((always_inline)) -> char* { return ws + W_FLAGU + (tile_of(t) * 36 + ni) * 128; };
    auto exch_p = [&](int t) __attribute__((always_inline)) {
      const u32x2 p2 = pack4(cc[t & 1][1][0]), p3 = pack4(cc[t & 1][1][1]);
      rx = u32x4{p2.x, p2.y, p3.x, p3.y};
      pin(rx);
    };
    auto combine = [&](int t) __attribute__((always_inline)) {
      kv0 = cc[t & 1][0][0] + unpk(rx.x, rx.y);
      kv1 = cc[t & 1][0][1] + unpk(rx.z, rx.w);
      if (t >= 3) { pin(pv); kv0 += unpk(pv.x, pv.y); kv1 += unpk(pv.z, pv.w); }
      pin(kv0); pin(kv1);
    };
    auto send = [&](int t) __attribute__((always_inline)) {
      const u32x2 p0 = pack4(kv0), p1 = pack4(kv1);
      st128_sc1(ws + W_UP + (tile_of(t) * 288 + f16) * 1024, lane16, u32x4{p0.x, p0.y, p1.x, p1.y});
    };
#pragma unroll
    for (int n = 0; n < 5; ++n) rd(n);
    SB();
    sfor<0, 192>([&](auto NN) __attribute__((always_inline)) {
        constexpr int n = decltype(NN)::value, i = n / 16, k = n % 16, t = stile[i], x = spart[i] * 16 + (k & 14);
        constexpr int d = i == 3 ? 0 : ((i >= 4 && !(i & 1)) ? (i >> 1) - 1 : -1);
        if (i == 2 && k == 0) bar_vo<4>();
        if (n + 5 < 192) rd(n + 5);
        if (i == 2 && k < 8) {
          const u32 la = (t1l ^ ((k & 1) * 32)) + lun[(k >> 1) & 3] * 16384;
          wf[24 + (k & 7)] = lds_tr2(la, la + 1024);
        }
#pragma unroll
        for (int e = 0; e < 2; ++e)
          cc[t & 1][k & 1][e] = mfma32(wf[x + e], a[n % 6], x ? cc[t & 1][k & 1][e] : (((k & 1) || t < 3) ? zero4 : ci[e]));
        SB();
        if (i == 0 && k < 4) issue_a(2, k);
        if (i == 0 && k >= 4 && k < 8) issue_wl(k - 4);
        if (i == 3 && k == 0) issue_a(5, 3);
        if (d >= 0 && k == 2) exch_p(d);
        if (i == 9 || i == 11) { if (k == 0 || k == 3 || k == 6) act(k / 3); if (k == 14) act(3); if (k == 15) store_u(t - 1); }
        if ((i == 7 || i == 8 || i == 10) && k == 7) flags_check<1>(w ^ 1, fv, flag_at(t), zo, tag);
        if (k == 8) {
          constexpr int vm[12] = {8, 8, 0, 4, 5, 7, 5, 6, 5, 8, 7, 5};
          if (d >= 0) lds_w128(exw + (d & 1) * 8192, rx);
          if (d >= 0 || i == 2) asm volatile("s_waitcnt lgkmcnt(0)" ::: "memory");
          asm volatile("s_waitcnt vmcnt(%0)\n\ts_barrier" ::"n"(vm[i]) : "memory");
          if (i == 5 || i == 6 || i == 8) st_flag_if(w, flag_at(i == 5 ? 0 : t - 2), tag);
          if (i == 6) st_flag_if(w ^ 2, ws + W_FLAGH + (rb2 * 8 + cj) * 128, tag);
          if (i == 11) flag_u(3);
          if (d >= 0) rx = lds_r128(exr + (d & 1) * 8192);
        }
        if (k >= 9 && k < 13) {
          if (i == 0) issue_wl(k - 5);
          else if (i < 3) issue_a(i + 2, k - 9);
          else if (i + 3 < 12) issue_a(i + 3, k - 9);
          else if (k & 1) issue_w2(2 * (i - 9) + 1 + ((k - 9) >> 1), reg1[i ? i - 1 : 0] * 2 + ((k - 9) >> 1));
        }
        if (i == 2 && k >= 13) issue_a(5, k - 13);
        if (i == 4 && k == 13) store_h();
        if ((i == 6 || i == 7 || i == 9) && k == 13) flags_request(w ^ 1, fv, flag_at(i == 6 ? 3 : t + 1), zo);
        if (d >= 0 && k == 14) { if (i == 8) waitv<4>(); combine(d); }
        if (k == 15) { if (i == 7 || i == 8 || i == 10) load_pv(t); if (d >= 0 && d < 3) send(d); }
        if (i == 8 && k == 15) issue_w2(0, 8);
        if (i == 5 && k == 14) touch(pfd, pw2 + (size_t)(936 + idx * 8 + w) * 4096, (lane16 & 0xf0) * 8);
        if ((RETOUCH & 4) && i == 5 && k == 15) touch(pfd, (const char*)pc, min(((idx * 8 + w) * 3 + min(lane, 2u)) * 128u, len));
        if (i == 8 && k == 9) pin(pfd);
        SB();
      });
    PRIO(0);
    STAMP(14); FS(57);
    exch_p(5);
    lds_w128(exw + 8192, rx);
    bar_v<5>();
    rx = lds_r128(exr + 8192);
    combine(5);
  } else {
    store_h();
    {
      const u32 part = (idx - 27) * 8 + w;
      u32 d[6];
#pragma unroll
      for (int j = 0; j < 6; ++j)
        asm volatile("global_load_dword %0, %1, %2" : "=v"(d[j]) : "v"(((j * 40 + part) * 4 + g) * 4096 + l15 * 128), "s"(pw2) : "memory");
      asm volatile("s_waitcnt vmcnt(0)" : "+v"(d[0]), "+v"(d[1]), "+v"(d[2]), "+v"(d[3]), "+v"(d[4]), "+v"(d[5])::"memory");
    }
    constexpr u32 un0[7] = {8, 6, 7, 0, 1, 2, 3};
    bar();
    if (w == 0) st_flag(ws + W_FLAGH + (rb2 * 8 + cj) * 128, tag);
#pragma unroll
    for (int ps = 0; ps < 7; ++ps) issue_w2(ps, un0[ps]);
    bar_v<0>();
  }
  STAMP(15); FS(58);

  {
    const u32 T0 = 9 * (Mq >> 1), TF = T0 + 5 * odd;
    u32 owners = (0x210310320321ull >> (12 * kq)) & 0xfffu;
    asm volatile("" : "+s"(owners));
    auto owner_of = [&](int b) __attribute__((always_inline)) -> u32 { return b == 4 ? kq : (owners >> (4 * (b - 1))) & 15u; };
    GRAB2_DEF;
    issue_w2(7, 4); issue_w2(8, 5);
    grab(0, 8); grab(1, 6); grab(2, 7); grab(3, 0); grab(4, 1);
    bar_v<4>();
    flag_u(4);
    issue_w2(9, 8); issue_w2(10, 6);
    grab(5, 2); grab(6, 3);
#pragma unroll
    for (int j = 0; j < 4; ++j) act(j);
    if (g1) store_u(5);
    issue_w2(11, 7); issue_w2(12, 0); issue_w2(13, 1); issue_w2(14, 9);
    bar_v<12>();
    issue_w2(15, 2); issue_w2(16, 3);
    grab(7, 4); grab(8, 5);
    u32 fo, fo2, fv = 0;
    {
      const u32 b = min(lane / 9, 4u);
      fo = ((b ? TF + owner_of(1) : T0 + 4) * 36 + kq * 9 + lane % 9) * 128;
      fo2 = lane < 60 ? ((TF + min(b, 3u)) * 36 + kq * 9 + lane % 9) * 128 : (W_FLAGH - W_FLAGE) + ((Nq * 2 + (lane & 1)) * 8 + Mq * 2 + ((lane >> 1) & 1)) * 128;
      if (lane == 58 || lane == 59) fo2 = (W_FLAGH - W_FLAGE) + ((Nq * 2 + (lane & 1)) * 8 + (Mq >> 1) * 4 + 2 - odd) * 128;
    }
    bar_v<12>();
    flag_u(5);
    issue_w2(17, 4);
    flags_request(w ^ 1, fv, ws + W_FLAGE, fo);
    grab(9, 8); grab(10, 6);
    bar_v<6>();
    grab(11, 7); grab(12, 0); grab(13, 1); grab(14, 9);
    flags_check<0>(w ^ 1, fv, ws + W_FLAGE, fo, tag);
    bar_v<0>();
    const char* ub = ws + W_U + kq * (36 * 1024);
    u32 uo_[5], lx = (32 + w3) * 1024;
#pragma unroll
    for (int j = 0; j < 5; ++j) {
      const u32 b = j < 4 ? 8 * j + w : 32 + w3;
      uo_[j] = (((b / 9) & 1) * 144 + (b / 18) * 18 + b % 9) * 1024;
      asm volatile("" : "+s"(uo_[j]));
    }
    asm volatile("" : "+s"(lx));
    auto issue_u = [&](int i, int j) __attribute__((always_inline)) {
      const u32 T = i == 0 ? T0 + 4 : TF + owner_of((i + 1) >> 1);
      const u32 part = (i == 0 ? 0u : (u32)((i + 1) & 1)) ^ odd;
      const char* b = ub + (T * 288 + part * 9) * 1024 + uo_[j];
      if (j < 4) dma<64, true>(b, lane16, w1k, ((i + 3) & 3) * 36864 + j * 8192);
      else dma<64, true>(b, lane16, lx, ((i + 3) & 3) * 36864);
    };
#pragma unroll
    for (int j = 0; j < 5; ++j) issue_u(0, j);
    grab(15, 2); grab(16, 3); grab(17, 4);
    bar();
    STAMP(16); FS(59);
#pragma unroll
    for (int j = 0; j < 5; ++j) issue_u(1, j);
    const u32 grp = Mq * 16 + Nq, me = odd * 4 + kq;
    u32 exw = 147456 + w * 1024 + lane16, exr = 147456 + (w ^ 4) * 1024 + lane16;
    pin(exw); pin(exr);
    const LAddr ar(kh * 18432 + lane16);
    f32x4 cc[2][2][2];
    u32x4 a[6], rx, pv[3];
    u32x2 hres[2]; u32 bb;
    const u32 pg = (Mq >> 1) * 16 + Nq;
    char* ysb = ws + W_YS + (pg * 8 + w) * 8192;
    auto flag_y = [&](int b) __attribute__((always_inline)) {
      st_flag_if(w, ws + W_FLAGY + (b == 0 ? 1024 + pg * 8 + me : (pg * 8 + me) * 4 + owner_of(b ? b : 1)) * 128, tag);
    };
    auto rd = [&](int n) __attribute__((always_inline)) { a[n % 6] = lds_r128(ar(((n / 18 + 3) & 3) * 36864 + (n & 1) * 9216 + ((n % 18) >> 1) * 1024)); };
    auto exch_p = [&](int b) __attribute__((always_inline)) {
      const u32x2 p2 = pack4(cc[b & 1][0][1]), p3 = pack4(cc[b & 1][1][1]);
      rx = u32x4{p2.x, p2.y, p3.x, p3.y};
      pin(rx);
    };
    auto send = [&](int b) __attribute__((always_inline)) {
      const u32x2 p0 = pack4(cc[b & 1][0][0] + unpk(rx.x, rx.y)), p1 = pack4(cc[b & 1][1][0] + unpk(rx.z, rx.w));
      const u32 o = owner_of(b ? b : 1);
      char* pb = b == 0 ? ysb + me * 1024 : ws + W_YP + (((grp * 4 + o) * 8 + w) * 3 + ((kq - o - 1) & 3u)) * 1024;
      st128_sc1(pb, lane16, u32x4{p0.x, p0.y, p1.x, p1.y});
    };
    auto load_res = [&](u32 T, u32 ln) __attribute__((always_inline)) -> u32 {
      const u32 srow = Nq * 128 + w3 * 32 + kh * 16 + (ln & 15), oo = srow * 1152 + T * 64 + (ln >> 4) * 8;
      ld64_sc1(hres[0], ws + W_H, oo);
      ld64_sc1(hres[1], ws + W_H + 32, oo);
      ld16(bb, P.b2, srow * 2);
      return oo;
    };
    u32 oo;
    bar_vo<5>();
#pragma unroll
    for (int n = 0; n < 5; ++n) rd(n);
    SB();
#pragma unroll
    for (int i = 0; i < 9; ++i)
#pragma unroll
      for (int k = 0; k < 18; ++k) {
        const int n = i * 18 + k, b = (i + 1) >> 1, x = (i == 0 || (i & 1)) ? k : 18 + k;
        if (n + 5 < 162) rd(n + 5);
#pragma unroll
        for (int e = 0; e < 2; ++e)
          cc[b & 1][x & 1][e] = mfma32(a[n % 6], wf[(x & ~1) + e], x > 1 ? cc[b & 1][x & 1][e] : zero4);
        SB();
        if (i == 0 && k < 5) issue_u(2, k);
        if ((i & 1) && k == 2) exch_p(b - 1);
        if (i == 1 && k == 3) flags_check<0>(w ^ 1, fv, ws + W_FLAGE, fo2, tag);
        if (i == 5 && k == 3) flags_check<0>(w ^ 1, fv, ws + W_FLAGY, fo, tag);
        if (k == 4) {
          constexpr int vm[9] = {4, 0, 6, 5, 6, 5, 10, 4, 0};
          if (i & 1) { lds_w128(exw, rx); asm volatile("s_waitcnt lgkmcnt(0)" ::: "memory"); }
          asm volatile("s_waitcnt vmcnt(%0)\n\ts_barrier" ::"n"(vm[i]) : "memory");
          if (i == 3 || i == 5 || i == 7) flag_y(b - 2);
          if (i == 8) flag_y(3);
          if (i & 1) rx = lds_r128(exr);
        }
        if (i == 1 && k >= 4 && k < 9) issue_u(3, k - 4);
        if (i == 1 ? (k >= 9 && k < 14) : (k >= 5 && k < 10 && i > 0 && i + 3 < 9)) issue_u(i + 3, i == 1 ? k - 9 : k - 5);
        if ((i & 1) && k == (i == 1 ? 15 : (i == 7 ? 7 : 12))) send(b - 1);
        if (i == 0 && k == 13) flags_request(w ^ 1, fv, ws + W_FLAGE, fo2);
        if (i == 4 && k == 12) { fo = (1024 + pg * 8 + (lane & 7)) * 128; flags_request(w ^ 1, fv, ws + W_FLAGY, fo); }
        if (i == 5 && k == 10) { ld128_sc1(pv[0], ysb, (lane >> 3) * 1024 + (me * 8 + (lane & 7)) * 16); oo = load_res(T0 + 4, me * 8 + (lane & 7)); }
        if (i == 6) {
          if (k == 5) {
            waitv<1>();
            pin(pv[0]);
            kv0 = unpk(pv[0].x, pv[0].y); kv1 = unpk(pv[0].z, pv[0].w);
            pin(kv0); pin(kv1);
          }
          if (k >= 6 && k < 10) { kv0[k - 6] = sum_g(kv0[k - 6] + dpp<0x128>(kv0[k - 6])); pin(kv0); }
          if (k >= 10 && k < 14) { kv1[k - 10] = sum_g(kv1[k - 10] + dpp<0x128>(kv1[k - 10])); pin(kv1); }
          if (k == 15) {
            pin(hres[0]); pin(hres[1]); pin(bb);
            const u32x2 o0 = pack4(kv0 + unpk(hres[0].x, hres[0].y) + bflo(bb)), o1 = pack4(kv1 + unpk(hres[1].x, hres[1].y) + bflo(bb));
            const bool hi = (lane & 8) != 0;
            asm volatile("s_mov_b64 exec, 0xffff\n\tglobal_store_dwordx2 %0, %1, %2 sc1\n\ts_mov_b64 exec, -1" ::"v"(oo + (lane & 8) * 4), "v"(hi ? o1 : o0), "s"(P.out) : "memory");
          }
          if (k == 17) oo = load_res(TF + kq, lane);
        }
        if (i == 8 && k == 6) {
          pin(hres[0]); pin(hres[1]); pin(bb);
          kv0 = unpk(hres[0].x, hres[0].y) + bflo(bb); kv1 = unpk(hres[1].x, hres[1].y) + bflo(bb);
          pin(kv0); pin(kv1);
          fo = ((pg * 8 + odd * 4 + ((kq + 1 + min(lane, 2u)) & 3u)) * 4 + kq) * 128;
        }
        if (i == 8 && k == 13) flags_request(w ^ 1, fv, ws + W_FLAGY, fo);
        SB();
      }
    exch_p(4);
    lds_w128(exw, rx);
    flags_check<0>(w ^ 1, fv, ws + W_FLAGY, fo, tag);
    bar();
    STAMP(17); FS(60);
#pragma unroll
    for (int j = 0; j < 3; ++j) ld128_sc1(pv[j], ws + W_YP + ((grp * 4 + kq) * 8 + w) * 3072, lane16, j * 1024);
    rx = lds_r128(exr);
    kv0 += cc[0][0][0] + unpk(rx.x, rx.y); kv1 += cc[0][1][0] + unpk(rx.z, rx.w);
    pin(kv0); pin(kv1);
    waitv<0>();
#pragma unroll
    for (int j = 0; j < 3; ++j) { pin(pv[j]); kv0 += unpk(pv[j].x, pv[j].y); kv1 += unpk(pv[j].z, pv[j].w); }
    const u32x2 o0 = pack4(kv0), o1 = pack4(kv1);
    auto s0 = __builtin_amdgcn_permlane16_swap(o0.x, o1.x, false, false);
    auto s1 = __builtin_amdgcn_permlane16_swap(o0.y, o1.y, false, false);
    asm volatile("global_store_dwordx4 %0, %1, %2 sc1\n\ts_nop 1" ::"v"(oo + (g & 1) * 24), "v"(u32x4{s0[0], s1[0], s0[1], s1[1]}), "s"(P.out) : "memory");
  }
  asm volatile(".Lfk_code_end:");
  if (bid == 0 && w == 0) st_flag(ws + W_EPOCH, tag);
}
}

extern "C" void kernel_launch(void* const* d_in, const int* in_sizes, int n_in, void* d_out, int out_size, void* d_ws,
                              size_t ws_size, hipStream_t stream) {
  Params p;
  p.x = (const char*)d_in[0]; p.wqkv = (const char*)d_in[1]; p.wphi = (const char*)d_in[2]; p.wo = (const char*)d_in[3];
  p.w1 = (const char*)d_in[5]; p.b1 = (const char*)d_in[6]; p.w2 = (const char*)d_in[7]; p.b2 = (const char*)d_in[8];
  p.out = (char*)d_out; p.ws = (char*)d_ws;
  (void)hipFuncSetAttribute((const void*)fused_kernel, hipFuncAttributeMaxDynamicSharedMemorySize, LDS_BYTES);
  fused_kernel<<<NWG, NT, LDS_BYTES, stream>>>(p);
}
```

```cpp
#include <hip/hip_runtime.h>
#include <hip/hip_bf16.h>

typedef unsigned u32;
typedef unsigned long long u64;
typedef u32 u32x2 __attribute__((ext_vector_type(2)));
typedef u32 u32x4 __attribute__((ext_vector_type(4)));
typedef float f32x2 __attribute__((ext_vector_type(2)));
typedef float f32x4 __attribute__((ext_vector_type(4)));
typedef float f32x16 __attribute__((ext_vector_type(16)));
typedef short s16x4 __attribute__((ext_vector_type(4)));
typedef __bf16 bf16x2 __attribute__((ext_vector_type(2)));
typedef __bf16 bf16x8 __attribute__((ext_vector_type(8)));
#define LAS __attribute__((address_space(3)))
#define DEVINL __device__ __forceinline__

namespace {
constexpr int S = 2048, D = 576, F = 4608;
constexpr int NWG = 256, NT = 512;
constexpr int LDS_BYTES = 163840;
constexpr u32 SPIN_CAP = 1u << 16;

constexpr u32 W_EPOCH = 0;
constexpr u32 W_FLAGB = 4096;
constexpr u32 W_FLAGD = W_FLAGB + 1024 * 128;
constexpr u32 W_FLAGE = W_FLAGD + 256 * 128;
constexpr u32 W_FLAGH = W_FLAGE + 18 * 36 * 128;
constexpr u32 W_FLAGU = W_FLAGH + 256 * 128;
constexpr u32 W_FLAGY = W_FLAGU + 216 * 8 * 128;
constexpr u32 W_KVFIN = W_FLAGY + 1280 * 128;
constexpr u32 W_STATS = W_KVFIN + 8 * 96 * 128;
constexpr u32 W_KVPART = W_STATS + 32 * 4 * 8 * 256;
constexpr u32 W_O = W_KVPART + 8 * 96 * 32 * 128;
constexpr u32 W_H = W_O + 128 * 18 * 1024;
constexpr u32 W_HN = W_H + S * D * 2;
constexpr u32 W_U = W_HN + 18 * 128 * 1024;
constexpr u32 W_UP = W_U + 18 * 288 * 1024;
constexpr u32 W_YP = W_UP + 18 * 288 * 1024;
constexpr u32 W_YS = W_YP + 64 * 4 * 3 * 8 * 1024;
constexpr u32 W_END = W_YS + 64 * 8 * 4 * 512;
constexpr u32 W_DBG = W_END;

constexpr u32 SLOT = 27648;
constexpr u32 L_XN = 3 * SLOT;
constexpr u32 L_PHI = 4 * SLOT;
constexpr u32 L_V = L_PHI + 64 * 328;
constexpr u32 L_KV = 147456;
constexpr u32 L_MISC = L_XN + 73728;
constexpr u32 L_WPHI = L_MISC;
constexpr u32 L_ZERO = L_MISC + 5760;
constexpr u32 L_CNT = L_MISC + 5824;
constexpr u32 L_HI = 5 * SLOT;
constexpr u32 L_HN = L_HI + 10240;

struct Params {
  const char* x; const char* wqkv; const char* wphi; const char* wo;
  const char* w1; const char* b1; const char* w2; const char* b2;
  char* out; char* ws;
};

DEVINL f32x4 mfma32(u32x4 a, u32x4 b, f32x4 c) {
  return __builtin_amdgcn_mfma_f32_16x16x32_bf16(__builtin_bit_cast(bf16x8, a), __builtin_bit_cast(bf16x8, b), c, 0, 0, 0);
}
DEVINL f32x16 mfmaB(u32x4 a, u32x4 b, f32x16 c) {
  return __builtin_amdgcn_mfma_f32_32x32x16_bf16(__builtin_bit_cast(bf16x8, a), __builtin_bit_cast(bf16x8, b), c, 0, 0, 0);
}
template <int Q> DEVINL f32x4 quad(const f32x16& v) { return f32x4{v[4 * Q], v[4 * Q + 1], v[4 * Q + 2], v[4 * Q + 3]}; }
DEVINL f32x4 mfma16(u32x2 a, u32x2 b, f32x4 c) {
  return mfma32(u32x4{a.x, a.y, 0u, 0u}, u32x4{b.x, b.y, 0u, 0u}, c);
}
DEVINL u32 pack2(float lo, float hi) {
  f32x2 p = {lo, hi};
  return __builtin_bit_cast(u32, __builtin_convertvector(p, bf16x2));
}
DEVINL u32x2 pack4(f32x4 v) { return u32x2{pack2(v.x, v.y), pack2(v.z, v.w)}; }
DEVINL float bflo(u32 v) { return __builtin_bit_cast(float, v << 16); }
DEVINL float bfhi(u32 v) { return __builtin_bit_cast(float, v & 0xffff0000u); }
DEVINL float asf(u32 v) { return __builtin_bit_cast(float, v); }
DEVINL u32 asu(float v) { return __builtin_bit_cast(u32, v); }
DEVINL f32x4 unpk(u32 a, u32 b) { return f32x4{bflo(a), bfhi(a), bflo(b), bfhi(b)}; }
DEVINL float dot2(u32 a, u32 b, float c) {
  return __builtin_amdgcn_fdot2_f32_bf16(__builtin_bit_cast(bf16x2, a), __builtin_bit_cast(bf16x2, b), c, false);
}
DEVINL u32x2 lds_tr(u32 addr) {
  return __builtin_bit_cast(u32x2, __builtin_amdgcn_ds_read_tr16_b64_v4i16((LAS s16x4*)addr));
}
DEVINL u32x4 lds_tr2(u32 a0, u32 a1) {
  u32x2 lo = lds_tr(a0), hi = lds_tr(a1);
  return u32x4{lo.x, lo.y, hi.x, hi.y};
}
DEVINL u32x4 lds_r128(u32 addr) { return *(LAS u32x4*)addr; }
DEVINL u32x2 lds_r64(u32 addr) { return *(LAS u32x2*)addr; }
DEVINL void lds_w128(u32 addr, u32x4 v) { *(LAS u32x4*)addr = v; }
DEVINL void lds_w64(u32 addr, u32x2 v) { *(LAS u32x2*)addr = v; }
template <int CTRL> DEVINL float dpp(float v) { return asf(__builtin_amdgcn_update_dpp(0u, asu(v), CTRL, 0xf, 0xf, true)); }
DEVINL float sum8(float v) { v += dpp<0xB1>(v); v += dpp<0x4E>(v); return v + dpp<0x141>(v); }
DEVINL float sum_g(float v) {
  auto a = __builtin_amdgcn_permlane16_swap(asu(v), asu(v), false, false);
  v = asf(a[0]) + asf(a[1]);
  auto b = __builtin_amdgcn_permlane32_swap(asu(v), asu(v), false, false);
  return asf(b[0]) + asf(b[1]);
}
#define SB() __builtin_amdgcn_sched_barrier(0)
template <int V> struct IC { static constexpr int value = V; };
template <int B, int E, class Fn> DEVINL void sfor(Fn&& f) {
  if constexpr (B < E) { f(IC<B>{}); sfor<B + 1, E>(f); }
}
#define PRIO_K1 2
#define PRIO_K2 2
#define PRIO(v)
DEVINL u32 fresh_lane() {
  u32 z = 0;
  asm volatile("" : "+v"(z));
  const u32 l = __builtin_amdgcn_mbcnt_hi(~0u, __builtin_amdgcn_mbcnt_lo(~0u, z));
  __builtin_assume(l < 64);
  return l;
}
#define LANEVARS const u32 lane = fresh_lane(), l15 = lane & 15, g = lane >> 4, q4 = l15 >> 2, p4 = lane & 3; \
  const u32 trrow = (16 * (g >> 1) + 4 * (g & 1) + q4) * 288; (void)l15; (void)g; (void)q4; (void)p4; (void)trrow
struct LAddr {
  u32 b[3];
  DEVINL explicit LAddr(u32 a) { b[0] = a; b[1] = a + 65536u; b[2] = a + 131072u; asm volatile("" : "+v"(b[0]), "+v"(b[1]), "+v"(b[2])); }
  DEVINL u32 operator()(u32 c) const { return b[c >> 16] + (c & 0xffffu); }
};

#define POL_W ""
#define POL_T ""
#define POL_S ""
#define PRETOUCH 1
#define TLBSTEP 1048576u
#define WO_BEHIND 1
#define SPJ0 4
#define RETOUCH 1
#define THR1 63
#define TPOS 91
#define SPW 4
#define T3R 0
#define PFV 0
#define HPOS 0
#define MAP1 0
#define DLY_A1 8
#define DLY_A2 8
#define DLY_B 6
#define DLY_C 4
#define PF_A 0
#define PF_B 0
#define PF_C 0
#define PF_D 0
#define POL_1 ""
#define ROT1 1
#define POL_X ""
template <int NL, bool SC1> DEVINL void dma(const char* base, u32 voff, u32 lbase, u32 loff) {
  if constexpr (NL == 64) {
    if constexpr (SC1) asm volatile("s_add_u32 m0, %2, %3\n\ts_nop 0\n\tglobal_load_lds_dwordx4 %0, %1 sc1" ::"v"(voff), "s"(base), "s"(lbase), "n"(loff) : "memory", "scc");
    else asm volatile("s_add_u32 m0, %2, %3\n\ts_nop 0\n\tglobal_load_lds_dwordx4 %0, %1" POL_W ::"v"(voff), "s"(base), "s"(lbase), "n"(loff) : "memory", "scc");
  } else {
    if constexpr (SC1) asm volatile("s_add_u32 m0, %2, %3\n\ts_mov_b32 exec_hi, %4\n\tglobal_load_lds_dwordx4 %0, %1 sc1\n\ts_mov_b32 exec_hi, -1" ::"v"(voff), "s"(base), "s"(lbase), "n"(loff), "n"((1u << (NL - 32)) - 1u) : "memory", "scc");
    else asm volatile("s_add_u32 m0, %2, %3\n\ts_mov_b32 exec_hi, %4\n\tglobal_load_lds_dwordx4 %0, %1" POL_X "\n\ts_mov_b32 exec_hi, -1" ::"v"(voff), "s"(base), "s"(lbase), "n"(loff), "n"((1u << (NL - 32)) - 1u) : "memory", "scc");
  }
}
DEVINL void dma_w1(const char* base, u32 voff, u32 lds) {
  asm volatile("s_mov_b32 m0, %2\n\ts_nop 0\n\tglobal_load_lds_dwordx4 %0, %1" POL_1 ::"v"(voff), "s"(base), "s"(lds) : "memory");
}
DEVINL void st128_sc1(char* base, u32 voff, u32x4 v) {
  asm volatile("global_store_dwordx4 %0, %1, %2 sc1\n\ts_nop 1" ::"v"(voff), "v"(v), "s"(base) : "memory");
}
DEVINL void st128_sc1_m(char* base, u32 voff, u32x4 v, u64 mask) {
  asm volatile("s_mov_b64 exec, %3\n\tglobal_store_dwordx4 %0, %1, %2 sc1\n\ts_mov_b64 exec, -1\n\ts_nop 0" ::"v"(voff), "v"(v), "s"(base), "s"(mask) : "memory");
}
DEVINL void st64_sc1(char* base, u32 voff, u32x2 v) {
  asm volatile("global_store_dwordx2 %0, %1, %2 sc1" ::"v"(voff), "v"(v), "s"(base) : "memory");
}
DEVINL void st64(char* base, u32 voff, u32x2 v) {
  asm volatile("global_store_dwordx2 %0, %1, %2" ::"v"(voff), "v"(v), "s"(base) : "memory");
}
DEVINL void st32_sc1(char* base, u32 voff, u32 v) {
  asm volatile("global_store_dword %0, %1, %2 sc1" ::"v"(voff), "v"(v), "s"(base) : "memory");
}
DEVINL void st_flag(char* addr, u32 tag) {
  asm volatile("s_mov_b64 exec, 1\n\tglobal_store_dword %0, %1, %2 sc1\n\ts_mov_b64 exec, -1" ::"v"(0u), "v"(tag), "s"(addr) : "memory");
}
DEVINL void st_flag_if(u32 zero_to_store, char* addr, u32 tag) {
  asm volatile("s_cmp_lg_u32 %3, 0\n\ts_cbranch_scc1 .Lsf%=\n\ts_mov_b64 exec, 1\n\tglobal_store_dword %0, %1, %2 sc1\n\ts_mov_b64 exec, -1\n.Lsf%=:"
               ::"v"(0u), "v"(tag), "s"(addr), "s"(zero_to_store) : "memory", "scc");
}
DEVINL void flags_request(u32 sel, u32& v, const char* base, u32 voff) {
  asm volatile("s_cmp_lg_u32 %3, 0\n\ts_cbranch_scc1 .Lfq%=\n\tglobal_load_dword %0, %1, %2 sc1\n.Lfq%=:" : "+v"(v) : "v"(voff), "s"(base), "s"(sel) : "memory", "scc");
}
template <int N> DEVINL void flags_check(u32 sel, u32& v, const char* base, u32 voff, u32 tag) {
  u32 n;
  asm volatile(
      "s_cmp_lg_u32 %4, 0\n\ts_cbranch_scc1 .Lfe%=\n\ts_mov_b32 %1, %6\n\ts_waitcnt vmcnt(%7)\n"
      ".Lfl%=:\n\tv_cmp_ne_u32 vcc, %5, %0\n\ts_sub_u32 %1, %1, 1\n\ts_cbranch_vccz .Lfe%=\n\ts_cbranch_scc1 .Lfe%=\n\t"
      "global_load_dword %0, %2, %3 sc1\n\ts_waitcnt vmcnt(0)\n\ts_branch .Lfl%=\n"
      ".Lfe%=:"
      : "+v"(v), "=&s"(n) : "v"(voff), "s"(base), "s"(sel), "v"(tag), "n"(SPIN_CAP), "n"(N) : "memory", "vcc", "scc");
}
DEVINL void ld128(u32x4& d, const char* base, u32 voff, int imm) {
  asm volatile("global_load_dwordx4 %0, %1, %2 offset:%3" POL_X : "=v"(d) : "v"(voff), "s"(base), "n"(imm) : "memory");
}
DEVINL void ld128_sc1(u32x4& d, const char* base, u32 voff, int imm = 0) {
  asm volatile("global_load_dwordx4 %0, %1, %2 offset:%3 sc1" : "=v"(d) : "v"(voff), "s"(base), "n"(imm) : "memory");
}
DEVINL void ld64(u32x2& d, const char* base, u32 voff, int imm = 0) {
  asm volatile("global_load_dwordx2 %0, %1, %2 offset:%3" : "=v"(d) : "v"(voff), "s"(base), "n"(imm) : "memory");
}
DEVINL void ld32(u32& d, const char* base, u32 voff) {
  asm volatile("global_load_dword %0, %1, %2" POL_T : "=v"(d) : "v"(voff), "s"(base) : "memory");
}
DEVINL void touch(u32& d, const char* base, u32 voff) {
  asm volatile("global_load_dword %0, %1, %2" POL_T : "+v"(d) : "v"(voff), "s"(base) : "memory");
}
DEVINL void ld64_sc1(u32x2& d, const char* base, u32 voff) {
  asm volatile("global_load_dwordx2 %0, %1, %2 sc1" : "=v"(d) : "v"(voff), "s"(base) : "memory");
}
DEVINL void ld16(u32& d, const char* base, u32 voff) {
  asm volatile("global_load_ushort %0, %1, %2" : "=v"(d) : "v"(voff), "s"(base) : "memory");
}
template <int N> DEVINL void waitv() { asm volatile("s_waitcnt vmcnt(%0)" ::"n"(N) : "memory"); }
template <typename T> DEVINL void pin(T& v) { asm volatile("" : "+v"(v)); }
template <int N> DEVINL void bar_v() { asm volatile("s_waitcnt vmcnt(%0) lgkmcnt(0)\n\ts_barrier" ::"n"(N) : "memory"); }
template <int N> DEVINL void bar_vo() { asm volatile("s_waitcnt vmcnt(%0)\n\ts_barrier" ::"n"(N) : "memory"); }
DEVINL void bar() { asm volatile("s_waitcnt lgkmcnt(0)\n\ts_barrier" ::: "memory"); }

DEVINL void cnt_add(u32 c) {
  asm volatile("s_mov_b64 exec, 1\n\tds_add_u32 %0, %1\n\ts_mov_b64 exec, -1" ::"v"(L_CNT + 4 * c), "v"(1u) : "memory");
}
DEVINL void cnt_wait(u32 c) {
#pragma unroll 1
  for (u32 spins = 0; spins < (SPIN_CAP << 4); ++spins) {
    const u32 v = *(volatile LAS u32*)(L_CNT + 4 * c);
    if (__builtin_amdgcn_readfirstlane(v) >= 4u) break;
    __builtin_amdgcn_s_sleep(1);
  }
  asm volatile("" ::: "memory");
}
template <int N> DEVINL void sync4(u32 c) {
  asm volatile("s_waitcnt vmcnt(%0) lgkmcnt(0)" ::"n"(N) : "memory");
  cnt_add(c);
  cnt_wait(c);
}
DEVINL void poll_flags(const char* base, u32 voff, u32 tag) {
#pragma unroll 1
  for (u32 spins = 0; spins < SPIN_CAP; ++spins) {
    u32 v;
    asm volatile("global_load_dword %0, %1, %2 sc1\n\ts_waitcnt vmcnt(0)" : "=v"(v) : "v"(voff), "s"(base) : "memory");
    if (__all(v == tag)) break;
  }
}

#define STAMP(i)

#define FS(i)
DEVINL void feature_map2(const u32x4 (&wa)[5], const u32x2 (&wb)[5], const f32x4 (&t0)[2], const f32x4 (&t1)[2], const f32x4 (&t2)[2], u32 (&ph)[2][9]) {
  float p[2][20];
#pragma unroll
  for (int m = 0; m < 2; ++m) {
    const u32x2 p0 = pack4(t0[m]), p1 = pack4(t1[m]), p2 = pack4(t2[m]);
    const u32x4 b01 = {p0.x, p0.y, p1.x, p1.y};
    f32x4 a[5];
#pragma unroll
    for (int t = 0; t < 5; ++t) a[t] = mfma32(wa[t], b01, f32x4{0.f, 0.f, 0.f, 0.f});
#pragma unroll
    for (int t = 0; t < 5; ++t) {
      a[t] = mfma16(wb[t], p2, a[t]);
      p[m][4 * t] = a[t].x; p[m][4 * t + 1] = a[t].y; p[m][4 * t + 2] = a[t].z; p[m][4 * t + 3] = a[t].w;
    }
  }
  const float L2E = 1.44269504088896341f;
#pragma unroll
  for (int m = 0; m < 2; ++m) {
    float mx = p[m][0];
#pragma unroll
    for (int i = 1; i < 18; ++i) mx = fmaxf(mx, p[m][i]);
    const float nm = -mx * L2E;
    f32x2 sum = {0.f, 0.f};
#pragma unroll
    for (int i = 0; i < 18; ++i) p[m][i] = __builtin_amdgcn_exp2f(__builtin_fmaf(p[m][i], L2E, nm));
#pragma unroll
    for (int i = 0; i < 9; ++i) sum += f32x2{p[m][2 * i], p[m][2 * i + 1]};
    const float inv = __builtin_amdgcn_rcpf(sum.x + sum.y);
#pragma unroll
    for (int i = 0; i < 9; ++i) ph[m][i] = pack2(p[m][2 * i] * inv, p[m][2 * i + 1] * inv);
  }
}

DEVINL float gelu_tanh(float x) {
  const float C = -2.f * 0.7978845608028654f * 1.44269504088896341f;
  float y = x * __builtin_fmaf(x * x, 0.044715f * C, C);
  float e = __builtin_amdgcn_exp2f(y);
  return x * __builtin_amdgcn_rcpf(1.f + e);
}

__global__ __launch_bounds__(NT, 2) void fused_kernel(Params P) {
  extern __shared__ __attribute__((aligned(16))) char smem[];
  STAMP(0); FS(0);
  const u32 w = __builtin_amdgcn_readfirstlane(threadIdx.x >> 6);
  const u32 bid = blockIdx.x, xcd = bid & 7, idx = bid >> 3;
  char* const ws = P.ws;
  u64 pc; u32 len;
  asm volatile("s_getpc_b64 %0\n.Lfk_code_begin:\n\ts_mov_b32 %1, .Lfk_code_end-.Lfk_code_begin\n\ts_max_u32 %1, %1, .Lfk_copy_end-.Lfk_code_begin" : "=s"(pc), "=s"(len)::"scc");

  const u32 cg = (xcd & 1) * 4 + (idx & 3), rb = (xcd >> 1) * 8 + (idx >> 2);
  const u32 rot = (idx >> 2) % 6;
  const u32 rb2 = xcd * 4 + (idx >> 3), cj = idx & 7;
  const u32 w3 = w & 3, kh = w >> 2;
  const u32 kq = xcd >> 1, kp = xcd & 1;
  const bool g1 = idx < 27;
  const u32 ni = kq * 9 + (g1 ? idx / 3 : 0), mi = idx % 3;
  u32 tag, tag0;
  u32x4 wf[36];
  u32x4 xr[9];
  const u32 rgp = w ^ ((cg & 1) * 4);
  auto load_x = [&]() __attribute__((always_inline)) {
    LANEVARS;
    const u32 xo = (rb * 64 + rgp * 8 + (lane >> 3)) * 1152 + (lane & 7) * 16;
#pragma unroll
    for (int i = 0; i < 9; ++i) ld128(xr[i], P.x, xo, i * 128);
    asm volatile("s_load_dword %0, %1, 0x0" : "=s"(tag0) : "s"(ws) : "memory");
    FS(1);
  };
  auto layer_norm = [&]() __attribute__((always_inline)) {
    LANEVARS;
    f32x2 c[36];
    f32x2 s1 = {0.f, 0.f}, s2 = {0.f, 0.f};
#pragma unroll
    for (int i = 0; i < 9; ++i) {
#pragma unroll
      for (int j = 0; j < 4; ++j) {
        const f32x2 v = {bflo(xr[i][j]), bfhi(xr[i][j])};
        c[i * 4 + j] = v;
        s1 += v; s2 += v * v;
      }
    }
    const float mean = sum8(s1.x + s1.y) * (1.f / 576.f);
    const float var = fmaxf(sum8(s2.x + s2.y) * (1.f / 576.f) - mean * mean, 0.f);
    const float rs = __builtin_amdgcn_rsqf(var + 1e-5f), nb = -mean * rs;
    const u32 a0 = L_XN + ((rgp >> 1) * 18 + ((lane & 7) >> 2)) * 1024 + (lane & 3) * 256 + (((8 * (rgp & 1) + (lane >> 3)) ^ (lane & 7)) * 16);
#pragma unroll
    for (int i = 0; i < 9; ++i) {
      u32x4 o;
#pragma unroll
      for (int j = 0; j < 4; ++j) { const f32x2 v = c[i * 4 + j] * rs + nb; o[j] = pack2(v.x, v.y); }
      lds_w128(a0 + i * 2048, o);
    }
  };
#define WAIT_X(N) asm volatile("s_waitcnt vmcnt(" #N ")" : "+v"(xr[0]), "+v"(xr[1]), "+v"(xr[2]), "+v"(xr[3]), "+v"(xr[4]), "+v"(xr[5]), "+v"(xr[6]), "+v"(xr[7]), "+v"(xr[8])::"memory")
  const char* pw1 = P.w1 + (size_t)kp * 1024 * 9216 + kq * 2304;
  auto w1_line = [](u32 r0, u32 lane) -> u32 {
    const u32 r = min(r0 + min(lane / 18, 2u), 1023u);
    return (((r >> 5) & 1) * 512 + (r >> 6) * 32 + (r & 31)) * 9216 + (lane % 18) * 128;
  };
  u32x2 bias[2];
  const u32 wl = w3 * 3456;
  const char* wq = P.wqkv + cg * 144;
  auto stage = [&](u32 n) __attribute__((always_inline)) -> u32 { const u32 st = rot + n; return (st >= 6 ? st - 6 : st) * (96 * 3456); };
#define RING_DEF \
  const u32 r3 = lane / 18, c18 = lane % 18, seg = c18 / 9, ch9 = c18 % 9; \
  u32 wrow[4], voff[4]; \
  _Pragma("unroll") for (int i = 0; i < 4; ++i) { \
    const u32 r = 12 * w3 + 3 * i + r3; \
    wrow[i] = (r & ~12u) | ((r & 4u) << 1) | ((r & 8u) >> 1); \
    voff[i] = wrow[i] * 3456 + seg * 1152 + ch9 * 16; \
    pin(voff[i]); \
  } \
  auto issue1 = [&](int n, int j0, int j1) __attribute__((always_inline)) {   \
    constexpr u32 slot[9] = {0, 1, 2, 3, 4, 0, 1, 2, 3}; \
    const char* b = n < 6 ? wq + 1152 + stage(n) : wq; \
    const u32 qo = n < 6 ? 0u : (seg ? stage(2 * (n - 6) + 1) - 1152 : stage(2 * (n - 6))); \
    _Pragma("unroll") for (int j = j0; j < j1; ++j) \
      dma<54, false>(b + (j >> 2) * (48 * 3456), voff[j & 3] + qo, wl, slot[n] * SLOT + (j >> 2) * 13824 + (j & 3) * 864); \
  }

  if (__builtin_expect(w < 4, 1)) {
    const u32 mp = w >> 1, hp = w & 1;
    u32 adp = 0;
    load_x();
    {
      LANEVARS;
      RING_DEF;
#pragma unroll
      for (int a = 0; a < 3; ++a) {
        const u32 ic = a < 2 ? 16 * a + 4 * g + q4 : 32 + q4;
        const bool ok = (ic >= 18 ? 1u : 0u) == (p4 >> 1) && (a < 2 || g == 0);
        adp |= (ok ? (p4 & 1) * 360 + (hp * 36 + ic) * 5 : (L_ZERO - L_WPHI) / 8) << (10 * a);
      }
      pin(adp);
      WAIT_X(0);
      STAMP(1); FS(2);
      layer_norm();
      FS(3);
      issue1(1, 0, 8);
    }
    u32 ta0, ta1, xa[2];
    {
      LANEVARS;
      if (lane < 32) *(LAS u32*)(L_ZERO + lane * 4) = 0u;
      ta0 = trrow + hp * 72 + p4 * 8; ta1 = trrow + hp * 72 + 64 + (p4 & 1) * 144;
#pragma unroll
      for (int i = 0; i < 2; ++i) { xa[i] = L_XN + mp * 36864 + g * 256 + ((l15 ^ (((i + rot) & 1) * 4 + g)) * 16); pin(xa[i]); }
    }
    const LAddr trA(ta0);
    FS(4);
    asm volatile("s_waitcnt lgkmcnt(0)\n\ts_barrier\n\ts_add_u32 %0, %1, 1" : "=s"(tag) : "s"(tag0) : "memory", "scc");
    STAMP(2); FS(5);
    u32x4 xf[2][18];
    auto rdx = [&](int n) __attribute__((always_inline)) {
      u32 st = rot + n / 3; if (st >= 6) st -= 6;
      const u32 ad = xa[(n / 3 + n % 3) & 1] + st * 3072;
      xf[0][n] = lds_r128(ad + (n % 3) * 1024);
      xf[1][n] = lds_r128(ad + (n % 3) * 1024 + 18432);
    };
    f32x4 acc[5][2];
#pragma unroll
    for (int i = 0; i < 5; ++i) { acc[i][0] = f32x4{0.f, 0.f, 0.f, 0.f}; acc[i][1] = acc[i][0]; }
    u32x4 wa[5]; u32x2 wb[5];
    {
      const LAddr trM(ta1);
      u32x4 F[6];
      u32 ad[3];
      auto rd = [&](int u) __attribute__((always_inline)) {
        constexpr u32 toff[4] = {0, 32, 144, 176};
        const int n = u / 5, i = u % 5;
        const u32 b = ((n / 3) % 5) * SLOT + (n % 3) * 9216;
        F[u % 6] = i < 4 ? lds_tr2(trA(b + toff[i & 3]), trA(b + toff[i & 3] + 2304)) : lds_tr2(trM(b), trM(b + 2304));
      };
      rdx(0);
#pragma unroll
      for (int u = 0; u < 5; ++u) rd(u);
      rdx(1);
      SB();
#pragma unroll
      for (int n = 0; n < 18; ++n) {
        if (n == 16) {
#pragma unroll
          for (int a = 0; a < 3; ++a) { ad[a] = L_WPHI + ((adp >> (10 * a)) & 1023u) * 8; pin(ad[a]); }
        }
        if (n % 3 == 2 && n < 17) { FS(6 + 2 * (n / 3)); bar_v<0>(); FS(7 + 2 * (n / 3)); }
#pragma unroll
        for (int i = 0; i < 5; ++i) {
          const int u = n * 5 + i;
          if (u + 5 < 90) rd(u + 5);
          else { wa[i] = lds_tr2(ad[0] + 8 * i, ad[1] + 8 * i); wb[i] = lds_tr(ad[2] + 8 * i); }
          if (u < 8) { rdx(2 + 2 * u); rdx(3 + 2 * u); }
          acc[i][0] = mfma32(F[u % 6], xf[0][n], acc[i][0]);
          acc[i][1] = mfma32(F[u % 6], xf[1][n], acc[i][1]);
          SB();
        }
      }
    }
    STAMP(3); FS(16);
    {
      LANEVARS;
      const u32 va = L_V + (mp * 32 + l15) * 152 + hp * 72 + g * 8;
#pragma unroll
      for (int m = 0; m < 2; ++m) {
        lds_w64(va + m * (16 * 152), pack4(acc[2][m]));
        lds_w64(va + m * (16 * 152) + 32, pack4(acc[3][m]));
        if (g == 1) lds_w64(va + m * (16 * 152) + 56, pack4(acc[4][m]));
      }
      u32 ph[2][9];
      feature_map2(wa, wb, acc[0], acc[1], acc[4], ph);
      const u32 pa = L_PHI + (mp * 32 + l15) * 328 + (hp * 4 + g) * 40;
#pragma unroll
      for (int m = 0; m < 2; ++m)
#pragma unroll
        for (int i = 0; i < 5; ++i) lds_w64(pa + m * (16 * 328) + 8 * i, u32x2{ph[m][2 * i], i < 4 ? ph[m][2 * i + 1] : 0u});
    }
    FS(17);
    bar();
    FS(18);
    bar();
    STAMP(4); FS(20);
#pragma unroll
    for (int i = 0; i < 3; ++i) { acc[i][0] = f32x4{0.f, 0.f, 0.f, 0.f}; acc[i][1] = acc[i][0]; }
    {
      u32 tq;
      { LANEVARS; tq = trrow + hp * 72 + 64; }
      const LAddr trQ(tq);
      u32x4 F[6];
      auto rd = [&](int u) __attribute__((always_inline)) {
        const int n = u / 3, i = u % 3;
        const u32 b = (1 + n / 6) * SLOT + (n % 3) * 9216 + ((n % 6) / 3) * 144;
        F[u % 6] = i < 2 ? lds_tr2(trA(b + 32 * i), trA(b + 32 * i + 2304)) : lds_tr2(trQ(b), trQ(b + 2304));
      };
#pragma unroll
      for (int u = 0; u < 5; ++u) rd(u);
      SB();
#pragma unroll
      for (int u = 0; u < 54; ++u) {
        if (u + 5 < 54) rd(u + 5);
        acc[u % 3][0] = mfma32(F[u % 6], xf[0][u / 3], acc[u % 3][0]);
        acc[u % 3][1] = mfma32(F[u % 6], xf[1][u / 3], acc[u % 3][1]);
        SB();
      }
    }
    STAMP(5); FS(21);
    u32 cpf = 0;

    u32 pq[2][9];
    feature_map2(wa, wb, acc[0], acc[1], acc[2], pq);
    LANEVARS;
    u32 oa[3];
    {
      const bool same = (q4 >> 1) == (g >> 1);
#pragma unroll
      for (int t = 0; t < 3; ++t) {
        const u32 slot = 4 * t + p4;
        const u32 cc = slot < 9 ? 9 * (q4 & 1) + slot : 18;
        oa[t] = same ? L_KV + ((hp * 2 + (g >> 1)) * 19 + cc) * 96 + (g & 1) * 48 : L_ZERO;
        pin(oa[t]);
      }
    }
    const char* w1b = P.w1 + (size_t)(kp * 1024 + w * 4) * 9216 + ni * 256;
    u32 w1off = g * 9216 + ((((l15 >> 1) ^ (g | ((w & 2) << 1))) << 1) | (l15 & 1)) * 16, lane16 = lane * 16;
    pin(w1off); pin(lane16);
#pragma unroll
    for (int m = 0; m < 2; ++m)
#pragma unroll
      for (int i = 0; i < 9; ++i) pin(pq[m][i]);
    if (PFV == 1) touch(cpf, pw1, w1_line((idx * 4 + w) * 3, lane));
    if (RETOUCH & 1) touch(cpf, (const char*)pc, min(((idx * 4 + w) * 4 + p4) * 128u, len));
    STAMP(6); FS(22);
    bar();
    STAMP(7); FS(23);
    {
      u32x4 k0[3], k1[3]; u32x2 k2[3];
#pragma unroll
      for (int t = 2; t >= 0; --t) { k0[t] = lds_r128(oa[t]); k1[t] = lds_r128(oa[t] + 16); k2[t] = lds_r64(oa[t] + 32); }
      SB();
      f32x4 nu[2][3];
#pragma unroll
      for (int t = 2; t >= 0; --t)
#pragma unroll
        for (int m = 0; m < 2; ++m) {
          f32x4 c = mfma32(k0[t], u32x4{pq[m][0], pq[m][1], pq[m][2], pq[m][3]}, f32x4{0.f, 0.f, 0.f, 0.f});
          c = mfma32(k1[t], u32x4{pq[m][4], pq[m][5], pq[m][6], pq[m][7]}, c);
          nu[m][t] = mfma16(k2[t], u32x2{pq[m][8], 0u}, c);
        }
      char* ob = ws + W_O + (rb * 4 + mp * 2) * 18432;
#pragma unroll
      for (int m = 0; m < 2; ++m) {
        const float inv = __builtin_amdgcn_rcpf(nu[m][2].y + 1e-6f);
        const u32x2 a = pack4(nu[m][0] * inv), b = pack4(nu[m][1] * inv);
        st128_sc1(ob + m * 18432 + (cg * 2 + hp) * 1024, lane16, u32x4{a.x, a.y, b.x, b.y});
        const u32 e0 = asu(nu[m][2].x * inv);
        auto t16 = __builtin_amdgcn_permlane16_swap(e0, e0, false, false);
        const u32 e01 = pack2(asf(t16[0]), asf(t16[1]));
        auto t32 = __builtin_amdgcn_permlane32_swap(e01, e01, false, false);
        asm volatile("s_mov_b64 exec, 0xffff\n\tglobal_store_dwordx2 %0, %1, %2 sc1\n\ts_mov_b64 exec, -1" ::"v"(lane16), "v"(u32x2{t32[0], t32[1]}), "s"(ob + m * 18432 + 16384 + cg * 256 + hp * 8) : "memory");
      }
    }
    FS(24);
    waitv<0>();
    pin(cpf);
    st_flag(ws + W_FLAGB + ((rb * 2 + mp) * 16 + cg * 2 + hp) * 128, tag);
    cnt_add(11);
    STAMP(8); FS(25);
    u32 po = w1_line((idx * 4 + w) * 3, lane);
    pin(po);
    if (PFV == 2) touch(cpf, pw1, po);
    ld64(bias[0], P.b1 + (ni * 4 + w3) * 64, g * 8);
    ld64(bias[1], P.b1 + (ni * 4 + w3) * 64, g * 8, 32);
    cnt_wait(6);
    FS(42);
    if (PFV == 0) touch(cpf, pw1, po);
    asm volatile("s_waitcnt vmcnt(0)" : "+v"(cpf), "+v"(bias[0]), "+v"(bias[1])::"memory");
    FS(44);
    if (PFV == 0) cnt_wait(7);
    if (g1) {
#pragma unroll
      for (int j = 0; j < 20; ++j)
        dma<64, false>(w1b + (size_t)(((j >> 1) & 1) * 512 + (j >> 2) * 32 + (j & 1) * 16) * 9216, w1off, w * 1024, (((j >> 2) + 1) % 5) * 16384 + (j & 3) * 4096);
    }
    FS(38);
    asm volatile("s_barrier" ::: "memory");
  } else {
    LANEVARS;
    const u32 lane16 = lane * 16;
    u32 pfd = 0;
    asm volatile("global_load_dword %0, %1, %2" : "+v"(pfd) : "v"(min(((idx * 4 + w3) * 4 + p4) * 128u, len)), "s"(pc) : "memory");
    load_x();
    RING_DEF;
    u32 wp[3];
    {
      const u32 so = (min(lane, 59u) / 10) * 36 + min(lane % 10, 8u) * 4;
#pragma unroll
      for (int j = 0; j < 3; ++j) ld32(wp[j], P.wphi + cg * 2592 + w3 * 648 + j * 216, so);
    }
    issue1(0, 0, 8);
    WAIT_X(8);
    FS(2);
    pin(wp[0]); pin(wp[1]); pin(wp[2]);
    layer_norm();
    if (lane < 60) {
#pragma unroll
      for (int j = 0; j < 3; ++j) {
        *(LAS u32*)(L_WPHI + w3 * 720 + j * 240 + lane * 4) = wp[j];
        *(LAS u32*)(L_WPHI + 2880 + w3 * 720 + j * 240 + lane * 4) = wp[j] ^ 0x80008000u;
      }
    }
    FS(4);
    asm volatile("s_waitcnt vmcnt(0) lgkmcnt(0)\n\ts_barrier\n\ts_add_u32 %0, %1, 1" : "=s"(tag) : "s"(tag0) : "memory", "scc");
    __builtin_amdgcn_s_setprio(1);
    issue1(2, 0, 8);
    const u32 srow = 8 * g + q4;
    u32 e_vb0 = L_V + srow * 152 + (w3 >> 1) * 72 + ((w3 & 1) ? 40 : 0) + p4 * 8, e_vb1 = L_V + srow * 152 + (w3 >> 1) * 72 + 32;
    u32 e_pb = L_PHI + srow * 328 + w3 * 80 + p4 * 8, e_pb2 = L_PHI + srow * 328 + w3 * 80 + 64 + (p4 & 1) * 8;
    u32 e_s0 = (lane >> 3) * 4096 + (lane & 7) * 16, e_s1[3];
    const u32 colp = l15 == 4 ? 2u : l15 - 2 * (w3 & 1);
#pragma unroll
    for (int i = 0; i < 3; ++i) {
      const u32 sidx = (160 + min(colp, 2u) * 10 + 4 * i + g);
      e_s1[i] = (sidx >> 3) * 4096 + (sidx & 7) * 16;
      pin(e_s1[i]);
    }
    const u64 e_m01 = __ballot(l15 == 4 || colp < 2), e_m2 = __ballot(l15 == 4 || (colp < 2 && g < 2));
    pin(e_vb0); pin(e_vb1); pin(e_pb); pin(e_pb2); pin(e_s0);
    u32 vow[4];
#pragma unroll
    for (int i = 0; i < 4; ++i) { vow[i] = (9 * (wrow[i] >> 3) + (wrow[i] & 7) + seg * 324) * 1152 + ch9 * 16; pin(vow[i]); }
    FS(6);
    bar_vo<8>();
    issue1(3, 0, 8);
    FS(8);
    bar_vo<8>();
    issue1(4, 0, 8);
    FS(10);
    bar_vo<8>();
    issue1(5, 0, 8);
    FS(12);
    bar_vo<8>();
    {
      touch(pfd, P.wo, min((idx * 4 + w3) * 41 + min(lane, 40u), 5183u) * 128);
    }
    {
      u32 pt = W_STATS + (rb2 * 4 + w3) * 2048 + cj * 256 + (lane & 1) * 128;
      if (lane == 2) pt = W_KVFIN + (cg * 96 + rb * 3 + min(w3, 2u)) * 128;
      if (lane == 3) pt = W_FLAGB + ((rb * 2 + (w3 >> 1)) * 16 + cg * 2 + (w3 & 1)) * 128;
      if (lane == 4) pt = W_FLAGD + (cj * 32 + rb2) * 128;
      if (lane >= 5 && lane < 9) pt = W_FLAGE + min((bid * 4 + w3) * 4 + lane - 5, (W_KVFIN - W_FLAGE) / 128 - 1) * 128;
      touch(pfd, ws, pt);
    }
    FS(14);
    bar_vo<2>();
    issue1(6, 0, 8);
    issue1(7, 0, 8);
    issue1(8, 0, 8);
    const char* wob = P.wo + cj * 144;
    __builtin_amdgcn_s_setprio(3);
    FS(17);
    bar();
    FS(18);
    {
      u32x4 vf[2][2], pf_[2][3];
#pragma unroll
      for (int ks = 0; ks < 2; ++ks) {
        vf[ks][0] = lds_tr2(e_vb0 + ks * (32 * 152), e_vb0 + ks * (32 * 152) + 4 * 152);
        pf_[ks][0] = lds_tr2(e_pb + ks * (32 * 328), e_pb + ks * (32 * 328) + 4 * 328);
        pf_[ks][1] = lds_tr2(e_pb + ks * (32 * 328) + 32, e_pb + ks * (32 * 328) + 32 + 4 * 328);
        pf_[ks][2] = lds_tr2(e_pb2 + ks * (32 * 328), e_pb2 + ks * (32 * 328) + 4 * 328);
      }
#pragma unroll
      for (int ks = 0; ks < 2; ++ks) vf[ks][1] = lds_tr2(e_vb1 + ks * (32 * 152), e_vb1 + ks * (32 * 152) + 4 * 152);
      SB();
      f32x4 kv[2][3];
#pragma unroll
      for (int vt = 0; vt < 2; ++vt)
#pragma unroll
        for (int ks = 0; ks < 2; ++ks) {
          if (vt && l15 >= 4) vf[ks][1] = u32x4{0x3f803f80u, 0x3f803f80u, 0x3f803f80u, 0x3f803f80u};
#pragma unroll
          for (int i = 0; i < 3; ++i) kv[vt][i] = mfma32(pf_[ks][i], vf[ks][vt], ks ? kv[vt][i] : f32x4{0.f, 0.f, 0.f, 0.f});
        }
      char* kb = ws + W_KVPART + ((cg * 96 + w3 * 24) * 32 + rb) * 128;
#pragma unroll
      for (int i = 0; i < 3; ++i) {
        const u32x2 pk = pack4(kv[0][i]);
        st128_sc1_m(kb + i * 32768, e_s0, u32x4{pk.x, tag, pk.y, tag}, i < 2 ? ~0ull : 0xffffffffull);
      }
#pragma unroll
      for (int i = 0; i < 3; ++i) {
        const u32x2 pk = pack4(kv[1][i]);
        st128_sc1_m(kb, e_s1[i], u32x4{pk.x, tag, pk.y, tag}, i < 2 ? e_m01 : e_m2);
      }
    }
    FS(19);
    bar_v<6>();
    u32x2 xres[5];
    auto issue_wo = [&](int part) __attribute__((always_inline)) {
#pragma unroll
      for (int j = 0; j < 8; ++j) dma<54, false>(wob + (part * 108 + (j >> 2) * 54) * 1152, vow[j & 3], wl, part * 4 * SLOT + (j >> 2) * 13824 + (j & 3) * 864);
    };
    issue_wo(0);
    u32 la[3], vo2[8];
    {
#pragma unroll
      for (int a = 0; a < 3; ++a) {
        const u32 r = a * 64 + lane;
        const u32 rr = r - 160, cp = (rr * 205) >> 11;
        const u32 cc = r < 160 ? (r & 15) + 2 * (w3 & 1) : (cp >= 2 ? 18 : ((w3 & 1) ? cp : 16 + cp));
        const u32 piece = r < 160 ? (r >> 4) : rr - cp * 10;
        la[a] = r < 190 ? L_KV + (w3 * 19 + cc) * 96 + piece * 8 + (piece >= 5 ? 8 : 0) : L_KV + 40;
        pin(la[a]);
      }
    }
    {
#pragma unroll
      for (int j = 0; j < 8; ++j) {
        const u32 kap = 192 + (j >> 2) * 48 + wrow[j & 3] + seg * 288;
        vo2[j] = (kap < 512 ? 9 * (kap >> 3) + (kap & 7) : 9 * (kap - 512) + 8) * 1152 + ch9 * 16;
        pin(vo2[j]);
      }
    }
    if (DLY_A1) __builtin_amdgcn_s_sleep(DLY_A1);
    FS(26);
    {
      const u32 line = cg * 96 + rb * 3 + min(w3, 2u);
      const char* pb = ws + W_KVPART + line * 4096;
      u32x4 v[4];
#pragma unroll
      for (int i = 0; i < 4; ++i) ld128_sc1(v[i], pb, lane16, i * 1024);
      waitv<0>();
#pragma unroll 1
      for (u32 spins = 0; spins < SPIN_CAP; ++spins) {
        u32 bad = 0;
#pragma unroll
        for (int i = 0; i < 4; ++i) { pin(v[i]); bad |= (v[i].y ^ tag) | (v[i].w ^ tag); }
        pin(bad);
        if (__all(bad == 0)) break;
#pragma unroll
        for (int i = 0; i < 4; ++i) ld128_sc1(v[i], pb, lane16, i * 1024);
        waitv<0>();
      }
#pragma unroll
      for (int i = 0; i < 4; ++i) pin(v[i]);
      f32x4 sum = (unpk(v[0].x, v[0].z) + unpk(v[1].x, v[1].z)) + (unpk(v[2].x, v[2].z) + unpk(v[3].x, v[3].z));
#pragma unroll
      for (int j = 0; j < 4; ++j) sum[j] = sum_g(sum[j] + dpp<0x128>(sum[j]));
      const u32x2 pk = pack4(sum);
      if (w3 < 3) st128_sc1_m(ws + W_KVFIN + line * 128, lane16, u32x4{pk.x, tag, pk.y, tag}, 0xffull);
    }
    FS(27);
    {
      issue_wo(1);
      if (DLY_A2) __builtin_amdgcn_s_sleep(DLY_A2);
      FS(28);
      const char* fb = ws + W_KVFIN + (cg * 4 + w3) * 3072;
      u32x4 v[3];
#pragma unroll
      for (int i = 0; i < 3; ++i) ld128_sc1(v[i], fb, lane16, i * 1024);
      {
        const u32 xo = (rb2 * 64 + w3 * 16 + l15) * 1152 + cj * 144;
#pragma unroll
        for (int n = 0; n < 4; ++n) ld64(xres[n], P.x, xo + g * 8, n * 32);
        ld64(xres[4], P.x, xo + (g & 1) * 8, 128);
      }
      waitv<5>();
#pragma unroll 1
      for (u32 spins = 0; spins < SPIN_CAP; ++spins) {
        u32 bad = 0;
#pragma unroll
        for (int i = 0; i < 3; ++i) { pin(v[i]); bad |= (v[i].y ^ tag) | (v[i].w ^ tag); }
        pin(bad);
        if (__all(bad == 0)) break;
#pragma unroll
        for (int i = 0; i < 3; ++i) ld128_sc1(v[i], fb, lane16, i * 1024);
        waitv<0>();
      }
#pragma unroll
      for (int i = 0; i < 3; ++i) { pin(v[i]); lds_w64(la[i], u32x2{v[i].x, v[i].z}); }
    }
    FS(22);
    pin(pfd);
    __builtin_amdgcn_s_setprio(0);
    bar();
#pragma unroll
    for (int j = 0; j < 8; ++j) dma<54, false>(wob, vo2[j], wl, 3 * SLOT + (j >> 2) * 13824 + (j & 3) * 864);
    const LAddr wo0(trrow + p4 * 8), wo4(trrow + 128 + (p4 & 1) * 8);
    auto rdw = [&](u32 b, int n) __attribute__((always_inline)) -> u32x4 {
      return n < 4 ? lds_tr2(wo0(b + 32 * n), wo0(b + 32 * n + 2304)) : lds_tr2(wo4(b), wo4(b + 2304));
    };
    u32x4 wm[30];
#pragma unroll
    for (int u = 0; u < 30; ++u) wm[u] = rdw(((u / 5) % 3) * 9216 + (u / 15) * 144, u % 5);
    sync4<0>(5);
    f32x4 hc[5];
#pragma unroll
    for (int n = 0; n < 5; ++n) { pin(xres[n]); hc[n] = unpk(xres[n].x, xres[n].y); pin(hc[n]); }
    {
      u32x4 F[6];
      auto rd = [&](int u) __attribute__((always_inline)) {
        const int v = u / 5;
        F[u % 6] = rdw((v < 6 ? 4 * SLOT : 3 * SLOT) + (v % 3) * 9216 + ((v / 3) & 1) * 144, u % 5);
      };
#pragma unroll
      for (int u = 0; u < 5; ++u) rd(u);
      FS(29);
      __builtin_amdgcn_s_setprio(3);
      cnt_wait(11);
      if (DLY_B) __builtin_amdgcn_s_sleep(DLY_B);
      poll_flags(ws + W_FLAGB + (rb2 * 2 + (w3 >> 1)) * 2048, l15 * 128, tag);
      STAMP(9); FS(30);
      constexpr int ord[18] = {3, 4, 5, 12, 13, 14, 6, 7, 8, 15, 16, 17, 0, 1, 2, 9, 10, 11};
      u32x4 of[18];
      const char* ob = ws + W_O + (rb2 * 4 + w3) * 18432;
#pragma unroll
      for (int j = 0; j < 18; ++j) ld128_sc1(of[j], ob + (ord[j] >> 2) * 4096, lane16, (ord[j] & 3) * 1024);
      cnt_add(6);
      FS(31);
#pragma unroll
      for (int j = 0; j < 18; ++j) {
        asm volatile("s_waitcnt vmcnt(%1)" : "+v"(of[j]) : "n"(17 - j) : "memory");
#pragma unroll
        for (int n = 0; n < 5; ++n) {
          const int u = j * 5 + n;
          if (u + 5 < 60) rd(u + 5);
          hc[n] = mfma32(u < 60 ? F[u % 6] : wm[u - 60], of[j], hc[n]);
          SB();
        }
      }
    }
    STAMP(10); FS(32);
    {
    LANEVARS;
    const u32 lane16 = lane * 16;
    float s1 = 0.f, s2 = 0.f;
    u32x2 hr[5];
#pragma unroll
    for (int n = 0; n < 5; ++n) {
      hr[n] = pack4(hc[n]);
      if (n == 4 && g >= 2) hr[n] = u32x2{0u, 0u};
      s1 = dot2(hr[n].x, 0x3f803f80u, s1); s1 = dot2(hr[n].y, 0x3f803f80u, s1);
      s2 = dot2(hr[n].x, hr[n].x, s2); s2 = dot2(hr[n].y, hr[n].y, s2);
    }
    s1 = sum_g(s1); s2 = sum_g(s2);
    char* sb = ws + W_STATS + (rb2 * 4 + w3) * 2048;
    st128_sc1_m(sb + cj * 256, lane16, u32x4{asu(s1), tag, asu(s2), tag}, 0xffffull);
    FS(34);
    u32 hw = L_HI + (w3 * 16 + l15) * 160 + g * 8;
#pragma unroll
    for (int n = 0; n < 5; ++n) {
      lds_w64(hw + n * 32, hr[n]);
      hc[n] = unpk(hr[n].x, hr[n].y);
      pin(hc[n]);
    }
    u32 hrd = L_HN + (w3 * 16 + 8 * (g & 1) + q4) * 160 + ((g >> 1) * 16 + p4 * 4) * 2;
    u32 hrx = L_HN + (w3 * 16 + 8 * (g & 1) + q4) * 160 + (64 + (p4 & 1) * 4) * 2;
    u32 ho[2];
#pragma unroll
    for (int j = 0; j < 2; ++j) {
      const u32 d = cj * 72 + (j ? 64 + (l15 & 7) : (g >> 1) * 16 + l15);
      ho[j] = (d >> 5) * 131072 + (g & 1) * 512 + (d & 31) * 16;
      pin(ho[j]);
    }
    u32x4 hrow[3];
    u32 hso[3];
#pragma unroll
    for (int j = 0; j < 3; ++j) {
      const u32 it = min(j * 64 + lane, 143u);
      hrow[j] = lds_r128(L_HI + (w3 * 16 + it / 9) * 160 + (it % 9) * 16);
      hso[j] = it * 16;
      pin(hso[j]);
    }
    pin(hw); pin(hrd); pin(hrx);
    char* hb = ws + W_HN + (rb2 * 4 + w3) * 1024;
    u32x4 v0, v1;
    if (HPOS == 1) {
#pragma unroll
    for (int j = 0; j < 3; ++j) st128_sc1(ws + W_H + (cj * 2048 + rb2 * 64 + w3 * 16) * 144, hso[j], hrow[j]);
    }
    if (DLY_C) __builtin_amdgcn_s_sleep(DLY_C);
    FS(35);
#pragma unroll 1
    for (u32 spins = 0; spins < SPIN_CAP; ++spins) {
      ld128_sc1(v0, sb, lane16);
      ld128_sc1(v1, sb, lane16, 1024);
      waitv<0>();
      pin(v0); pin(v1);
      u32 bad = (v0.y ^ tag) | (v0.w ^ tag) | (v1.y ^ tag) | (v1.w ^ tag);
      pin(bad);
      if (__all(bad == 0)) break;
    }
    FS(36);
    s1 = sum_g(asf(v0.x) + asf(v1.x));
    s2 = sum_g(asf(v0.z) + asf(v1.z));
    const float mean = s1 * (1.f / 576.f);
    const float var = fmaxf(s2 * (1.f / 576.f) - mean * mean, 0.f);
    const float r1 = __builtin_amdgcn_rsqf(var + 1e-5f);
    const float r2 = r1 * __builtin_amdgcn_rsqf(var * r1 * r1 + 1e-5f);
    const float nb = -mean * r2;
#pragma unroll
    for (int n = 0; n < 5; ++n) lds_w64(hw + 10240 + n * 32, pack4(hc[n] * r2 + nb));
    const u32x4 f0 = lds_tr2(hrd, hrd + 4 * 160), f1 = lds_tr2(hrd + 64, hrd + 64 + 4 * 160), f2 = lds_tr2(hrx, hrx + 4 * 160);
    STAMP(11); FS(37);
    st128_sc1(hb, ho[0], f0);
    st128_sc1(hb + 131072, ho[0], f1);
    st128_sc1_m(hb, ho[1], f2, 0x00ff00ffull);
    ld64(bias[0], P.b1 + (ni * 4 + w3) * 64, g * 8);
    ld64(bias[1], P.b1 + (ni * 4 + w3) * 64, g * 8, 32);
    cnt_add(7);
    sync4<0>(10);
    pin(bias[0]); pin(bias[1]);
    st_flag_if(w3, ws + W_FLAGD + (cj * 32 + rb2) * 128, tag);
    if (HPOS == 0) {
#pragma unroll
    for (int j = 0; j < 3; ++j) st128_sc1(ws + W_H + (cj * 2048 + rb2 * 64 + w3 * 16) * 144, hso[j], hrow[j]);
    }
    __builtin_amdgcn_s_setprio(0);
    FS(38);
    asm volatile("s_barrier" ::: "memory");
    asm volatile(".Lfk_copy_end:");
    }
  }
  STAMP(12); FS(39);
  LANEVARS;
  const u32 lane16 = lane * 16;
  const u32 wcol = ((((l15 >> 1) ^ ((w * 4 + g) & 7)) << 1) | (l15 & 1)) * 16;

  const u32 Mq = idx & 3, Nq = kp * 8 + (idx >> 2), odd = Mq & 1;
  const char* w2b = P.w2 + (size_t)(kq * 1152 + w * 4) * 4096 + Nq * 256;
  const u32 w2off = g * 4096 + wcol;
  u32 w1k = w * 1024;
  asm volatile("" : "+s"(w1k));
  auto issue_w2 = [&](int ps, u32 unit) __attribute__((always_inline)) {
    const char* b = w2b + ps * (32 * 4096);
    dma<64, false>(b, w2off, w1k, unit * 16384);
    dma<64, false>(b + 18 * (32 * 4096), w2off, w1k, unit * 16384 + 8192);
  };
  static constexpr u32 un2[18] = {8, 6, 7, 0, 1, 2, 3, 8, 6, 7, 0, 1, 4, 5, 9, 2, 3, 0};
  u32 tw2;
  { LANEVARS; tw2 = kh * 8192 + (4 * g + q4) * 256 + (((2 * w3 + kh) ^ (4 * (g & 1) + q4)) * 32) + p4 * 8; }
#define GRAB2_DEF \
  const LAddr t2w(tw2), t2x(tw2 ^ 32); \
  auto grab = [&](int ps, u32 unit) __attribute__((always_inline)) { \
    const u32 sl = unit * 16384; \
    wf[2 * ps] = lds_tr2(t2w(sl), t2w(sl + 4096)); \
    wf[2 * ps + 1] = lds_tr2(t2x(sl), t2x(sl + 4096)); \
  }
  const f32x4 zero4 = {0.f, 0.f, 0.f, 0.f};
  const u32 ord = mi == 0 ? 0x321504u : (mi == 1 ? 0x251403u : 0x504321u);
  auto tile_of = [&](int t) __attribute__((always_inline)) -> u32 {
    const u32 pos = t < 3 ? 2 * t + (kp ^ 1) : 2 * (t - 3) + kp;
    return mi * 6 + ((ord >> (4 * pos)) & 15u);
  };
  const u32 f16 = ni * 4 + w3 + kh * 144;
  u32 usel = w | ((idx + 5) >> 5);
  asm volatile("" : "+s"(usel));
  auto flag_u = [&](int t) __attribute__((always_inline)) {
    st_flag_if(usel, ws + W_FLAGE + (tile_of(t) * 36 + ni) * 128, tag);
  };
  float pre[3][8];
  auto finish_u = [&](int q) __attribute__((always_inline)) {
    u32x4 uo;
#pragma unroll
    for (int j = 0; j < 4; ++j) uo[j] = pack2(gelu_tanh(pre[q][2 * j]), gelu_tanh(pre[q][2 * j + 1]));
    st128_sc1(ws + W_U + (tile_of(3 + q) * 288 + f16) * 1024, lane16, uo);
  };
  const char* pw2 = P.w2 + (size_t)kq * 1152 * 4096 + kp * 2048;
  if (g1) {
    const char* hb = ws + W_HN + kp * 65536;
    u32 la24 = kh * 12288 + w3 * 1024;
    asm volatile("" : "+s"(la24));
    auto issue_a = [&](int d, int j) __attribute__((always_inline)) {
      dma<64, true>(hb + tile_of(j) * 131072 + (kh * 32 + w3 + d * 4) * 1024, lane16, la24, 81920 + ((d + 1) % 3) * 24576 + j * 4096);
    };
    const char* w1s = P.w1 + (size_t)(kp * 1024 + w * 4) * 9216 + ni * 256;
    const u32 w1o = g * 9216 + ((((l15 >> 1) ^ (g | ((w & 2) << 1))) << 1) | (l15 & 1)) * 16;
    auto issue_w = [&](int st, int j) __attribute__((always_inline)) {
      dma<64, false>(w1s + (size_t)(j * 512 + st * 32) * 9216, w1o, w1k, ((st + 1) % 5) * 16384 + j * 8192);
    };
    static constexpr u32 regb[6] = {1, 2, 3, 0, 1, 2};
    auto issue_b = [&](int i, int j) __attribute__((always_inline)) {
      dma<64, true>(hb + tile_of(3 + (i >> 1)) * 131072 + w1k + ((j >> 1) * 32 + (i & 1) * 16 + (j & 1) * 8) * 1024, lane16, w1k, regb[i] * 32768 + j * 8192);
    };
    u32 tw0 = kh * 8192 + (8 * g + q4) * 256 + (((2 * w3) ^ (4 * (g & 1) + q4)) * 32) + p4 * 8, tw1 = tw0 ^ 32;
    u32 tw0h = tw0 + 65536, tw1h = tw1 + 65536;
    const u32 aa = kh * 256 + (g >> 1) * 1024 + (g & 1) * 512 + l15 * 16;
    u32 pa0 = 81920 + 24576 + kh * 12288 + aa, pa1 = pa0 ^ 256, pa0l = pa0 - 24576, pa1l = pa1 - 24576;
    pin(tw0); pin(tw1); pin(tw0h); pin(tw1h); pin(pa0); pin(pa1); pin(pa0l); pin(pa1l);
    {
      const u32 cp = min((mi * 192) / 72 + q4, 7u);
      poll_flags(ws + W_FLAGD, (cp * 32 + kp * 16 + kh * 8 + (p4 >> 1) * 2 * w3 + (p4 & 1)) * 128, tag);
      FS(52);
#pragma unroll
      for (int j = 0; j < 6; ++j) issue_a(j / 3, j % 3);
    }
    STAMP(13); FS(54);
    bar_vo<3>();
    FS(55);
    st_flag_if(w ^ 2, ws + W_FLAGH + (rb2 * 8 + cj) * 128, tag);
    const LAddr ar0(kh * 16384 + aa), ar1((kh * 16384 + aa) ^ 256);
    u32 exw = w * 1024 + lane16, exr = (w ^ 4) * 1024 + lane16;
    f32x4 ca[3][2][2], cc[2][2][2], ci[2];
    u32x4 a[6], rx, r3[3];
    u32 pfd = 0, fv = 0, zo = 0;
    pin(zo);
    auto rdw = [&](int st, int e) __attribute__((always_inline)) {
      const u32 o = ((st + 1) % 5) * 16384;
      const u32 b = o < 65536 ? (e ? tw1 : tw0) + o : (e ? tw1h : tw0h);
      wf[2 * st + e] = lds_tr2(b, b + 1024);
    };
    auto rd = [&](int n) __attribute__((always_inline)) {
      if (n < 96) {
        const int st = n / 6, j = n % 6, sl = (st / 2 + 1) % 3;
        const u32 o = (j >> 1) * 4096 + (st & 1) * 2048;
        a[n % 6] = lds_r128(sl ? ((j & 1) ? pa1 : pa0) + (sl - 1) * 24576 + o : ((j & 1) ? pa1l : pa0l) + o);
      } else {
        const u32 o = regb[((n - 96) >> 4) % 6] * 32768 + (n & 14) * 1024;
        a[n % 6] = lds_r128((n & 1) ? ar1(o) : ar0(o));
      }
    };
    char* fl_own = ws + W_FLAGU + (((ni * 3 + mi) * 2 + kp) * 8 + w) * 128;
    const char* fl_par = ws + W_FLAGU + (((ni * 3 + mi) * 2 + (kp ^ 1)) * 8 + w) * 128;
    auto pack8 = [&](f32x4 lo, f32x4 hi) __attribute__((always_inline)) -> u32x4 {
      const u32x2 p0 = pack4(lo), p1 = pack4(hi);
      return u32x4{p0.x, p0.y, p1.x, p1.y};
    };
    rdw(0, 0); rdw(0, 1);
#pragma unroll
    for (int n = 0; n < 5; ++n) rd(n);
    SB();
    sfor<0, 96>([&](auto NN) __attribute__((always_inline)) {
      constexpr int n = decltype(NN)::value, st = n / 6, j = n % 6;
      rd(n + 5);
      if (st < 15 && (j == 1 || j == 2)) rdw(st + 1, j - 1);
#pragma unroll
      for (int e = 0; e < 2; ++e) ca[j >> 1][j & 1][e] = mfma32(wf[2 * st + e], a[j], st ? ca[j >> 1][j & 1][e] : zero4);
      SB();
      if (j == 0) {
        constexpr int vm[16] = {63, 5, 63, 7, 63, 7, 63, 7, 63, 7, 63, TPOS == 65 ? 8 : 7, 63, TPOS == 65 ? 5 : 4, 63, 8};
        asm volatile("s_waitcnt vmcnt(%0)\n\ts_barrier" ::"n"(vm[st]) : "memory");
      }
      if (st <= 10) {
        if (st & 1) { if (j == 1 || j == 3) issue_w(st + 5, j >> 1); }
        else if (j < 2) issue_w(st + 5, j);
        else if (j < 5) issue_a(st / 2 + 2, j - 2);
      } else if (st >= 12 && st < 15 && j < 4) issue_b(st - 12, j);
      if (n == TPOS) touch(pfd, pw2, ((T3R && g >= 2) ? (w & 1) * 576 + 216 + idx * 4 + (w >> 1) : (g & 1) * 576 + idx * 8 + w) * 4096 + (lane16 & 0xf0) * 8);
      if (st == 15 && j == 3) { ci[0] = unpk(bias[0].x, bias[0].y); ci[1] = unpk(bias[1].x, bias[1].y); pin(ci[0]); pin(ci[1]); pin(exw); pin(exr); }
      SB();
    });
    FS(56);
    auto grab_h = [&](int f) __attribute__((always_inline)) {
      const u32 la = (tw2 ^ ((f & 1) * 32)) + un2[f >> 1] * 16384;
      wf[f] = lds_tr2(la, la + 4096);
    };
    float v8[8];
    u32x4 uo;
    auto gelu_op = [&](int q) __attribute__((always_inline)) {
      const float C = -2.f * 0.7978845608028654f * 1.44269504088896341f;
      if (q < 12 || (q >= 20 && q < 24) || (q >= 32 && q < 36)) {
#pragma unroll
        for (int h = 0; h < 2; ++h) {
          const int j = (q & 3) * 2 + h;
          v8[j] = q < 4 ? pre[0][j] * pre[0][j] : (q < 8 ? __builtin_fmaf(v8[j], 0.044715f * C, C) : (q >= 20 && q < 24 ? v8[j] + 1.f : pre[0][j] * v8[j]));
          pin(v8[j]);
        }
      } else if (q < 20) { v8[q - 12] = __builtin_amdgcn_exp2f(v8[q - 12]); pin(v8[q - 12]); }
      else if (q < 32) { v8[q - 24] = __builtin_amdgcn_rcpf(v8[q - 24]); pin(v8[q - 24]); }
      else {
        const int j = (q - 36) * 4;
        u32 x = pack2(v8[j], v8[j + 1]), y = pack2(v8[j + 2], v8[j + 3]);
        pin(x); pin(y);
        uo[j >> 1] = x; uo[(j >> 1) + 1] = y;
      }
    };
    auto acc8 = [&](const f32x4 (&c)[2], int j) __attribute__((always_inline)) -> float { return c[j >> 2][j & 3]; };
    auto half = [&](const u32x4& p, int j) __attribute__((always_inline)) -> float { return (j & 1) ? bfhi(p[j >> 1]) : bflo(p[j >> 1]); };
    auto pack_op = [&](u32x4& d, const f32x4 (&c)[2], int q) __attribute__((always_inline)) {
      u32 x = pack2(c[q][0], c[q][1]), y = pack2(c[q][2], c[q][3]);
      pin(x); pin(y);
      d[2 * q] = x; d[2 * q + 1] = y;
    };
    sfor<0, 192>([&](auto NN) __attribute__((always_inline)) {
      constexpr int G = decltype(NN)::value, m = G / 2, e = G & 1, n = 96 + m, i = m / 16, k = m % 16, t = 3 + i / 2, x = (i & 1) * 16 + (k & 14);
      if (e == 0 && n + 5 < 192) rd(n + 5);
      cc[t & 1][k & 1][e] = mfma32(wf[x + e], a[n % 6], x ? cc[t & 1][k & 1][e] : ((k & 1) ? zero4 : ci[e]));
      SB();
      if (G >= 2 && G < 8) pack_op(r3[((G - 2) >> 1) % 3], ca[((G - 2) >> 1) % 3][1], G & 1);
      if (G >= 13 && G < 13 + 33) {
        constexpr int q = ((G - 13) / 11) % 3, o = (G - 13) % 11;
        if (o < 8) { v8[o] = acc8(ca[q][0], o) + half(r3[q], o); pin(v8[o]); }
        else if (o < 10) { u32 px = pack2(v8[(o - 8) * 4], v8[(o - 8) * 4 + 1]), py = pack2(v8[(o - 8) * 4 + 2], v8[(o - 8) * 4 + 3]); pin(px); pin(py); r3[q][(o - 8) * 2] = px; r3[q][(o - 8) * 2 + 1] = py; }
        else st128_sc1(ws + W_UP + (tile_of(q) * 288 + f16) * 1024, lane16, r3[q]);
      }
      if ((i == 2 || i == 4) && k == 2) pack_op(rx, cc[(t - 1) & 1][1], e);
      if (G == 73) { waitv<8>(); st_flag(fl_own, tag); }
      if (G >= 90 && G < 98) { constexpr int j = (G - 90) & 7; pre[0][j] = acc8(cc[1][0], j) + half(rx, j); pin(pre[0][j]); }
      if (G == 95) flags_request(0u, fv, fl_par, zo);
      if (G == 109) flags_check<0>(0u, fv, fl_par, zo, tag);
      if (G >= 109 && G < 112) ld128_sc1(r3[(G - 109) % 3], ws + W_UP + (tile_of(3 + (G - 109) % 3) * 288 + f16) * 1024, lane16);
      if (G == 126) { waitv<4>(); pin(r3[0]); pin(r3[1]); pin(r3[2]); }
      if (G >= 126 && G < 136 && (G < 132 || G >= 134)) { constexpr int j = (G < 132 ? G - 126 : G - 128) & 7; pre[0][j] += half(r3[0], j); pin(pre[0][j]); }
      if (G >= 136 && G < 174) gelu_op(G - 136);
      if (G == 175) st128_sc1(ws + W_U + (tile_of(3) * 288 + f16) * 1024, lane16, uo);
      if (G >= 176 && G < 184) { constexpr int j = (G - 176) & 7; pre[1][j] = acc8(cc[0][0], j) + half(rx, j); pin(pre[1][j]); }
      if (G >= 184) { constexpr int j = (G - 184) & 7; pre[1][j] += half(r3[1], j); pin(pre[1][j]); }
      if (e == 1) {
        if (k == (i == 0 ? 4 : 8)) {
          constexpr int vm[6] = {TPOS == 91 ? 5 : 4, TPOS == 91 ? 4 : 3, 5, 63, 0, 1};
          if (i == 0) { lds_w128(exw, r3[0]); lds_w128(exw + 8192, r3[1]); lds_w128(exw + 16384, r3[2]); exw += 9 * 16384; }
          if (i == 2 || i == 4) lds_w128(exw, rx);
          if (!(i & 1)) asm volatile("s_waitcnt lgkmcnt(0)" ::: "memory");
          asm volatile("s_waitcnt vmcnt(%0)\n\ts_barrier" ::"n"(vm[i]) : "memory");
          if (i == 0) { r3[0] = lds_r128(exr); r3[1] = lds_r128(exr + 8192); r3[2] = lds_r128(exr + 16384); exr += 9 * 16384; }
          if (i == 2) pin(pfd);
          if (i == 2 || i == 4) rx = lds_r128(exr);
        }
        if (i == 1 && k >= 9 && k < 13) issue_b(3, k - 9);
        if (i == 2 && k < 4) issue_b(4, k);
        if (i == 2 && k >= 9 && k < 13) issue_b(5, k - 9);
        if (i == 2 && k == 14) issue_w2(0, 8);
        if ((i == 3 || i == 4) && (k == 9 || k == 11)) issue_w2(2 * (i - 3) + 1 + ((k >> 1) & 1), un2[(2 * (i - 3) + 1 + ((k >> 1) & 1)) % 18]);
        if (i == 5 && k >= 9 && k < 14) issue_w2(k - 4, un2[(k - 4) % 18]);
      } else {
        if (i == 4 && k >= 10) grab_h((k - 10) % 10);
        if (i == 5 && k >= 9 && k < 13) grab_h((k - 3) % 10);
      }
      SB();
    });
    STAMP(14); FS(57);
    lds_w128(exw, pack8(cc[1][1][0], cc[1][1][1]));
    finish_u(1);
    bar_v<11>();
    flag_u(3);
    rx = lds_r128(exr);
#pragma unroll
    for (int j = 0; j < 8; ++j) pre[2][j] = acc8(cc[1][0], j) + half(r3[2], j) + half(rx, j);
    finish_u(2);
#pragma unroll
    for (int ps = 10; ps < 14; ++ps) issue_w2(ps, un2[ps]);
  } else {
    {
      const u32 part = (idx - 27) * 8 + w;
      u32 d = 0;
#pragma unroll 1
      for (u32 r = 384; r < 1024; r += 120) {
        touch(d, pw1, w1_line(r + part * 3, lane));
        asm volatile("s_barrier" ::: "memory");
      }
#pragma unroll
      for (int j = 0; j < SPW; ++j) {
        touch(d, pw2, ((g & 1) * 576 + min((TPOS < 96 ? 216 : 0) + T3R * 108 + (j * 40 + part) * 2 + (g >> 1), 575u)) * 4096 + l15 * 128);
        asm volatile("s_barrier" ::: "memory");
      }
      asm volatile("s_waitcnt vmcnt(0)" : "+v"(d)::"memory");
    }
    GRAB2_DEF;
    bar();
    if (w == 0) st_flag(ws + W_FLAGH + (rb2 * 8 + cj) * 128, tag);
#pragma unroll
    for (int ps = 0; ps < 7; ++ps) issue_w2(ps, un2[ps]);
    bar_v<4>();
#pragma unroll
    for (int ps = 0; ps < 5; ++ps) grab(ps, un2[ps]);
    bar();
#pragma unroll
    for (int ps = 7; ps < 14; ++ps) issue_w2(ps, un2[ps]);
  }
  STAMP(15); FS(58);

  {
    const u32 T0 = 9 * (Mq >> 1), TF = T0 + 5 * odd;
    u32 owners = (0x210310320321ull >> (12 * kq)) & 0xfffu;
    asm volatile("" : "+s"(owners));
    auto owner_of = [&](int b) __attribute__((always_inline)) -> u32 { return b == 4 ? kq : (owners >> (4 * (b - 1))) & 15u; };
    GRAB2_DEF;
    bar_v<8>();
    flag_u(4); flag_u(5);
    issue_w2(14, un2[14]);
#pragma unroll
    for (int ps = 5; ps < 10; ++ps) grab(ps, un2[ps]);
    u32 fo, fo2, fv = 0;
    {
      const u32 b = min(lane / 9, 4u);
      fo = ((T0 + 4) * 36 + kq * 9 + lane % 9) * 128;
      fo2 = lane < 60 ? ((TF + min(b, 3u)) * 36 + kq * 9 + lane % 9) * 128 : (W_FLAGH - W_FLAGE) + ((Nq * 2 + (lane & 1)) * 8 + Mq * 2 + ((lane >> 1) & 1)) * 128;
    }
    flags_request(w ^ 1, fv, ws + W_FLAGE, fo);
    bar_v<2>();
    issue_w2(15, un2[15]); issue_w2(16, un2[16]);
#pragma unroll
    for (int ps = 10; ps < 14; ++ps) grab(ps, un2[ps]);
    flags_check<4>(w ^ 1, fv, ws + W_FLAGE, fo, tag);
    const char* ub = ws + W_U + kq * (36 * 1024);
    u32 uo_[5], lx = (32 + w3) * 1024;
#pragma unroll
    for (int j = 0; j < 5; ++j) {
      const u32 b = j < 4 ? 8 * j + w : 32 + w3;
      uo_[j] = (((b / 9) & 1) * 144 + (b / 18) * 18 + b % 9) * 1024;
      asm volatile("" : "+s"(uo_[j]));
    }
    asm volatile("" : "+s"(lx));
    auto issue_u = [&](int i, int j) __attribute__((always_inline)) {
      const char* b = i == 0 ? ub + ((T0 + 4) * 2 + odd) * (144 * 1024) + (j < 4 ? w1k + j * 8192 : lx)
                             : ub + ((TF + owner_of((i + 1) >> 1)) * 288 + ((i + 1) & 1) * 9) * 1024 + uo_[j];
      if (j < 4) dma<64, true>(b, lane16, w1k, ((11 - i) & 3) * 36864 + j * 8192);
      else dma<64, true>(b, lane16, lx, ((11 - i) & 3) * 36864);
    };
    bar();
#pragma unroll
    for (int j = 0; j < 5; ++j) issue_u(0, j);
    issue_w2(17, un2[17]);
    fo += (TF + owner_of(1) - (T0 + 4)) * (36 * 128);
    flags_request(w ^ 1, fv, ws + W_FLAGE, fo);
    STAMP(16); FS(59);
    const u32 grp = Mq * 16 + Nq;
    u32 exw = 147456 + w * 1024 + lane16, exr = 147456 + (w ^ 4) * 1024 + lane16;
    pin(exw); pin(exr);
    const LAddr ar(kh * 18432 + lane16);
    f32x4 cc[2][2][2];
    float v8[8], k8[8];
    u32x4 a[6], rx, pv[3];
    u32x2 hres[3], ps_[4]; u32 bb;
    char* ysb = ws + W_YS + (grp * 8 + w) * 2048;
    auto flag_y = [&](int b) __attribute__((always_inline)) {
      st_flag_if(w, ws + W_FLAGY + (b == 0 ? 1024 + grp * 4 + kq : (grp * 4 + kq) * 4 + owner_of(b ? b : 1)) * 128, tag);
    };
    auto rd = [&](int n) __attribute__((always_inline)) {
      a[n % 6] = lds_r128(ar(((11 - n / 18) & 3) * 36864 + (n < 18 ? n * 1024 : (n & 1) * 9216 + ((n % 18) >> 1) * 1024)));
    };
    auto half = [&](const u32x4& p, int j) __attribute__((always_inline)) -> float { return (j & 1) ? bfhi(p[j >> 1]) : bflo(p[j >> 1]); };
    auto half2 = [&](const u32x2& p, int j) __attribute__((always_inline)) -> float { return (j & 1) ? bfhi(p[j >> 1]) : bflo(p[j >> 1]); };
    const u32 srow = Nq * 128 + w3 * 32 + kh * 16 + l15;
    u32 oo = srow * 1152 + g * 8, l8 = lane * 8;
    pin(oo); pin(l8);
    flags_check<0>(w ^ 1, fv, ws + W_FLAGE, fo, tag);
    bar_vo<0>();
#pragma unroll
    for (int n = 0; n < 5; ++n) rd(n);
    SB();
    sfor<0, 324>([&](auto NN) __attribute__((always_inline)) {
      constexpr int G = decltype(NN)::value, n = G / 2, e = G & 1, i = n / 18, k = n % 18, g_ = G % 36, b = (i + 1) >> 1, x = (i & 1) ? k : 18 + k, pb = (b - 1) & 1;
      if (e == 0 && n + 5 < 162) rd(n + 5);
      if (i == 0 && e == 0 && k < 4) grab(14 + (k & 3), un2[14 + (k & 3)]);
      if (i == 0) cc[0][0][e] = mfma32(a[n % 6], wf[2 * k + e], k ? cc[0][0][e] : zero4);
      else cc[b & 1][x & 1][e] = mfma32(a[n % 6], wf[(x & ~1) + e], x > 1 ? cc[b & 1][x & 1][e] : zero4);
      SB();
      if (i & 1) {
        if (g_ == 4 || (g_ == 5 && i > 1)) { u32 px = pack2(cc[pb][e][1][0], cc[pb][e][1][1]), py = pack2(cc[pb][e][1][2], cc[pb][e][1][3]); pin(px); pin(py); rx[2 * e] = px; rx[2 * e + 1] = py; }
        constexpr int s0 = i == 7 ? 13 : 21, nv = i == 1 ? 4 : 8;
        if (g_ >= s0 && g_ < s0 + nv) { constexpr int j = (g_ - s0) & 7; v8[j] = cc[pb][j >> 2][0][j & 3] + half(rx, j); pin(v8[j]); }
        if (g_ >= s0 + nv && g_ < s0 + nv + nv / 4) { constexpr int q = (g_ - s0 - nv) & 1; u32 px = pack2(v8[4 * q], v8[4 * q + 1]), py = pack2(v8[4 * q + 2], v8[4 * q + 3]); pin(px); pin(py); rx[2 * q] = px; rx[2 * q + 1] = py; }
        if (g_ == s0 + 10) {
          if (i == 1) st64_sc1(ysb + kq * 512, l8, u32x2{rx.x, rx.y});
          else { const u32 o = owner_of(b > 1 ? b - 1 : 1); st128_sc1(ws + W_YP + (((grp * 4 + o) * 8 + w) * 3 + ((kq - o - 1) & 3u)) * 1024, lane16, rx); }
        }
      }
      if (e == 1) {
        if (i == 0 && k < 5) issue_u(1, k);
        if (i == 0 && k >= 13) issue_u(2, k - 13);
        if (i == 1 && k == 3) flags_check<5>(w ^ 1, fv, ws + W_FLAGE, fo2, tag);
        if (i == 1 && k == 12) bar_vo<8>();
        if (i == 6 && k == 3) flags_check<1>(w ^ 1, fv, ws + W_FLAGY, fo, tag);
        if (k == (i == 0 ? 12 : 4)) {
          constexpr int vm[9] = {0, 63, 6, 5, 6, 9, 10, 4, 0};
          if (i & 1) { if (i == 1) lds_w64(exw, u32x2{rx.x, rx.y}); else lds_w128(exw, rx); asm volatile("s_waitcnt lgkmcnt(0)" ::: "memory"); }
          asm volatile("s_waitcnt vmcnt(%0)\n\ts_barrier" ::"n"(vm[i]) : "memory");
          if (i == 3 || i == 5 || i == 7) flag_y(b - 2);
          if (i == 8) flag_y(3);
          if (i & 1) rx = lds_r128(exr);
        }
        if (i == 1 && k >= 4 && k < 9) issue_u(3, k - 4);
        if (i == 1 ? (k >= 9 && k < 14) : (k >= 5 && k < 10 && i > 0 && i + 3 < 9)) issue_u(i + 3, i == 1 ? k - 9 : k - 5);
        if (i == 0 && k == 12) flags_request(w ^ 1, fv, ws + W_FLAGE, fo2);
        if (i == 4 && (k == 11 || k == 13 || k == 15)) {
          const u32 c = (k == 15 ? (odd ? 0u : 128u) : odd * 16 + kq * 32 + (k - 11) * 8) + 4 * g;
          ld64_sc1(hres[((k - 11) >> 1) % 3], ws + W_H + Mq * (2 * 2048 * 144), srow * 144 + c * 2 + (c >= 72 ? 2048 * 144 - 144 : 0));
        }
        if (i == 4 && k == 17) ld16(bb, P.b2, srow * 2);
        if (i == 5 && k == 13) { fo = (1024 + grp * 4 + p4) * 128; flags_request(w ^ 1, fv, ws + W_FLAGY, fo); }
        if (i == 6 && k >= 5 && k < 9) ld64_sc1(ps_[(k - 5) & 3], ysb + ((k - 5) & 3) * 512, l8);
        if (i == 8 && k == 6) fo = ((grp * 4 + ((kq + 1 + min(lane, 2u)) & 3u)) * 4 + kq) * 128;
        if (i == 8 && k == 13) flags_request(w ^ 1, fv, ws + W_FLAGY, fo);
      }
      if (i == 6 && g_ == 19) { waitv<10>(); pin(hres[0]); pin(hres[1]); pin(hres[2]); pin(bb); bb <<= 16; pin(bb); }
      if (i == 6 && g_ >= 20 && g_ < 28) { constexpr int j = (g_ - 20) & 7; k8[j] = half2(hres[j >> 2], j & 3) + asf(bb); pin(k8[j]); }
      if (i == 7 && g_ == 25) { waitv<1>(); pin(ps_[0]); pin(ps_[1]); pin(ps_[2]); pin(ps_[3]); }
      if (G >= 277 && G < 281) { constexpr int j = (G - 277) & 3; v8[j] = half2(hres[2], j) + asf(bb); pin(v8[j]); }
      if (G >= 281 && G < 297) { constexpr int j = (G - 281) & 3, q = ((G - 281) >> 2) & 3; v8[j] += half2(ps_[q], j); pin(v8[j]); }
      if (G == 299) {
        asm volatile("s_cmp_lg_u32 %3, %4\n\ts_cbranch_scc1 .Lso%=\n\tglobal_store_dwordx2 %0, %1, %2 sc1\n.Lso%=:"
                     ::"v"(oo), "v"(u32x2{pack2(v8[0], v8[1]), pack2(v8[2], v8[3])}), "s"(P.out + (T0 + 4) * 64 + odd * 32), "s"(w3), "s"(kq) : "memory", "scc");
      }
      SB();
    });
    {
      const u32x2 p2 = pack4(cc[0][0][1]), p3 = pack4(cc[0][1][1]);
      lds_w128(exw, u32x4{p2.x, p2.y, p3.x, p3.y});
    }
    flags_check<0>(w ^ 1, fv, ws + W_FLAGY, fo, tag);
    bar();
    STAMP(17); FS(60);
#pragma unroll
    for (int j = 0; j < 3; ++j) ld128_sc1(pv[j], ws + W_YP + ((grp * 4 + kq) * 8 + w) * 3072, lane16, j * 1024);
    rx = lds_r128(exr);
    f32x4 kv0 = f32x4{k8[0], k8[1], k8[2], k8[3]} + cc[0][0][0] + unpk(rx.x, rx.y), kv1 = f32x4{k8[4], k8[5], k8[6], k8[7]} + cc[0][1][0] + unpk(rx.z, rx.w);
    pin(kv0); pin(kv1);
    waitv<0>();
#pragma unroll
    for (int j = 0; j < 3; ++j) { pin(pv[j]); kv0 += unpk(pv[j].x, pv[j].y); kv1 += unpk(pv[j].z, pv[j].w); }
    const u32x2 o0 = pack4(kv0), o1 = pack4(kv1);
    auto s0 = __builtin_amdgcn_permlane16_swap(o0.x, o1.x, false, false);
    auto s1 = __builtin_amdgcn_permlane16_swap(o0.y, o1.y, false, false);
    asm volatile("global_store_dwordx4 %0, %1, %2 sc1\n\ts_nop 1" ::"v"(oo + (g & 1) * 24), "v"(u32x4{s0[0], s1[0], s0[1], s1[1]}), "s"(P.out + (TF + kq) * 64) : "memory");
  }
  asm volatile(".Lfk_code_end:");
  if (bid == 0 && w == 0) st_flag(ws + W_EPOCH, tag);
}
}

extern "C" void kernel_launch(void* const* d_in, const int* in_sizes, int n_in, void* d_out, int out_size, void* d_ws,
                              size_t ws_size, hipStream_t stream) {
  Params p;
  p.x = (const char*)d_in[0]; p.wqkv = (const char*)d_in[1]; p.wphi = (const char*)d_in[2]; p.wo = (const char*)d_in[3];
  p.w1 = (const char*)d_in[5]; p.b1 = (const char*)d_in[6]; p.w2 = (const char*)d_in[7]; p.b2 = (const char*)d_in[8];
  p.out = (char*)d_out; p.ws = (char*)d_ws;
  (void)hipFuncSetAttribute((const void*)fused_kernel, hipFuncAttributeMaxDynamicSharedMemorySize, LDS_BYTES);
  fused_kernel<<<NWG, NT, LDS_BYTES, stream>>>(p);
}
```

```cpp
#include <hip/hip_runtime.h>
#include <hip/hip_bf16.h>

typedef unsigned u32;
typedef unsigned long long u64;
typedef u32 u32x2 __attribute__((ext_vector_type(2)));
typedef u32 u32x4 __attribute__((ext_vector_type(4)));
typedef float f32x2 __attribute__((ext_vector_type(2)));
typedef float f32x4 __attribute__((ext_vector_type(4)));
typedef float f32x16 __attribute__((ext_vector_type(16)));
typedef short s16x4 __attribute__((ext_vector_type(4)));
typedef __bf16 bf16x2 __attribute__((ext_vector_type(2)));
typedef __bf16 bf16x8 __attribute__((ext_vector_type(8)));
#define LAS __attribute__((address_space(3)))
#define DEVINL __device__ __forceinline__

namespace {
constexpr int S = 2048, D = 576, F = 4608;
constexpr int NWG = 256, NT = 512;
constexpr int LDS_BYTES = 163840;
constexpr u32 SPIN_CAP = 1u << 16;

constexpr u32 W_EPOCH = 0;
constexpr u32 W_FLAGB = 4096;
constexpr u32 W_FLAGD = W_FLAGB + 1024 * 128;
constexpr u32 W_FLAGE = W_FLAGD + 256 * 128;
constexpr u32 W_FLAGH = W_FLAGE + 18 * 36 * 128;
constexpr u32 W_FLAGU = W_FLAGH + 256 * 128;
constexpr u32 W_FLAGY = W_FLAGU + 216 * 8 * 128;
constexpr u32 W_KVFIN = W_FLAGY + 1280 * 128;
constexpr u32 W_STATS = W_KVFIN + 8 * 96 * 128;
constexpr u32 W_KVPART = W_STATS + 32 * 4 * 8 * 256;
constexpr u32 W_O = W_KVPART + 8 * 96 * 32 * 128;
constexpr u32 W_H = W_O + 128 * 18 * 1024;
constexpr u32 W_HN = W_H + S * D * 2;
constexpr u32 W_U = W_HN + 18 * 128 * 1024;
constexpr u32 W_UP = W_U + 18 * 288 * 1024;
constexpr u32 W_YP = W_UP + 18 * 288 * 1024;
constexpr u32 W_YS = W_YP + 64 * 4 * 3 * 8 * 1024;
constexpr u32 W_END = W_YS + 64 * 8 * 4 * 512;
constexpr u32 W_DBG = W_END;

constexpr u32 SLOT = 27648;
constexpr u32 L_XN = 3 * SLOT;
constexpr u32 L_PHI = 4 * SLOT;
constexpr u32 L_V = L_PHI + 64 * 328;
constexpr u32 L_KV = 147456;
constexpr u32 L_MISC = L_XN + 73728;
constexpr u32 L_WPHI = L_MISC;
constexpr u32 L_ZERO = L_MISC + 5760;
constexpr u32 L_CNT = L_MISC + 5824;
constexpr u32 L_HI = 5 * SLOT;
constexpr u32 L_HN = L_HI + 10240;

struct Params {
  const char* x; const char* wqkv; const char* wphi; const char* wo;
  const char* w1; const char* b1; const char* w2; const char* b2;
  char* out; char* ws;
};

DEVINL f32x4 mfma32(u32x4 a, u32x4 b, f32x4 c) {
  return __builtin_amdgcn_mfma_f32_16x16x32_bf16(__builtin_bit_cast(bf16x8, a), __builtin_bit_cast(bf16x8, b), c, 0, 0, 0);
}
DEVINL f32x16 mfmaB(u32x4 a, u32x4 b, f32x16 c) {
  return __builtin_amdgcn_mfma_f32_32x32x16_bf16(__builtin_bit_cast(bf16x8, a), __builtin_bit_cast(bf16x8, b), c, 0, 0, 0);
}
template <int Q> DEVINL f32x4 quad(const f32x16& v) { return f32x4{v[4 * Q], v[4 * Q + 1], v[4 * Q + 2], v[4 * Q + 3]}; }
DEVINL f32x4 mfma16(u32x2 a, u32x2 b, f32x4 c) {
  return mfma32(u32x4{a.x, a.y, 0u, 0u}, u32x4{b.x, b.y, 0u, 0u}, c);
}
DEVINL u32 pack2(float lo, float hi) {
  f32x2 p = {lo, hi};
  return __builtin_bit_cast(u32, __builtin_convertvector(p, bf16x2));
}
DEVINL u32x2 pack4(f32x4 v) { return u32x2{pack2(v.x, v.y), pack2(v.z, v.w)}; }
DEVINL float bflo(u32 v) { return __builtin_bit_cast(float, v << 16); }
DEVINL float bfhi(u32 v) { return __builtin_bit_cast(float, v & 0xffff0000u); }
DEVINL float asf(u32 v) { return __builtin_bit_cast(float, v); }
DEVINL u32 asu(float v) { return __builtin_bit_cast(u32, v); }
DEVINL f32x4 unpk(u32 a, u32 b) { return f32x4{bflo(a), bfhi(a), bflo(b), bfhi(b)}; }
DEVINL float dot2(u32 a, u32 b, float c) {
  return __builtin_amdgcn_fdot2_f32_bf16(__builtin_bit_cast(bf16x2, a), __builtin_bit_cast(bf16x2, b), c, false);
}
DEVINL u32x2 lds_tr(u32 addr) {
  return __builtin_bit_cast(u32x2, __builtin_amdgcn_ds_read_tr16_b64_v4i16((LAS s16x4*)addr));
}
DEVINL u32x4 lds_tr2(u32 a0, u32 a1) {
  u32x2 lo = lds_tr(a0), hi = lds_tr(a1);
  return u32x4{lo.x, lo.y, hi.x, hi.y};
}
DEVINL u32x4 lds_r128(u32 addr) { return *(LAS u32x4*)addr; }
DEVINL u32x2 lds_r64(u32 addr) { return *(LAS u32x2*)addr; }
DEVINL void lds_w128(u32 addr, u32x4 v) { *(LAS u32x4*)addr = v; }
DEVINL void lds_w64(u32 addr, u32x2 v) { *(LAS u32x2*)addr = v; }
template <int CTRL> DEVINL float dpp(float v) { return asf(__builtin_amdgcn_update_dpp(0u, asu(v), CTRL, 0xf, 0xf, true)); }
DEVINL float sum8(float v) { v += dpp<0xB1>(v); v += dpp<0x4E>(v); return v + dpp<0x141>(v); }
DEVINL float sum_g(float v) {
  auto a = __builtin_amdgcn_permlane16_swap(asu(v), asu(v), false, false);
  v = asf(a[0]) + asf(a[1]);
  auto b = __builtin_amdgcn_permlane32_swap(asu(v), asu(v), false, false);
  return asf(b[0]) + asf(b[1]);
}
#define SB() __builtin_amdgcn_sched_barrier(0)
template <int V> struct IC { static constexpr int value = V; };
template <int B, int E, class Fn> DEVINL void sfor(Fn&& f) {
  if constexpr (B < E) { f(IC<B>{}); sfor<B + 1, E>(f); }
}
#define PRIO_K1 2
#define PRIO_K2 2
#define PRIO(v)
DEVINL u32 fresh_lane() {
  u32 z = 0;
  asm volatile("" : "+v"(z));
  const u32 l = __builtin_amdgcn_mbcnt_hi(~0u, __builtin_amdgcn_mbcnt_lo(~0u, z));
  __builtin_assume(l < 64);
  return l;
}
#define LANEVARS const u32 lane = fresh_lane(), l15 = lane & 15, g = lane >> 4, q4 = l15 >> 2, p4 = lane & 3; \
  const u32 trrow = (16 * (g >> 1) + 4 * (g & 1) + q4) * 288; (void)l15; (void)g; (void)q4; (void)p4; (void)trrow
struct LAddr {
  u32 b[3];
  DEVINL explicit LAddr(u32 a) { b[0] = a; b[1] = a + 65536u; b[2] = a + 131072u; asm volatile("" : "+v"(b[0]), "+v"(b[1]), "+v"(b[2])); }
  DEVINL u32 operator()(u32 c) const { return b[c >> 16] + (c & 0xffffu); }
};

#define POL_W ""
#define POL_T ""
#define POL_S ""
#define PRETOUCH 1
#define TLBSTEP 1048576u
#define WO_BEHIND 1
#define SPJ0 4
#define RETOUCH 1
#define THR1 63
#define PRE1 0
#define PRE2 3
#define TG 0
#define FLG 72
#define RQG 93
#define SPW 5
#define T3R 0
#define XS 0
#define XR 2
#define HPOS 0
#define MAP1 0
#define DLY_A1 6
#define DLY_A2 4
#define DLY_B 6
#define DLY_C 4
#define PF_A 0
#define PF_B 0
#define PF_C 0
#define PF_D 0
#define POL_1 ""
#define ROT1 1
#define POL_X ""
template <int NL, bool SC1> DEVINL void dma(const char* base, u32 voff, u32 lbase, u32 loff) {
  if constexpr (NL == 64) {
    if constexpr (SC1) asm volatile("s_add_u32 m0, %2, %3\n\ts_nop 0\n\tglobal_load_lds_dwordx4 %0, %1 sc1" ::"v"(voff), "s"(base), "s"(lbase), "n"(loff) : "memory", "scc");
    else asm volatile("s_add_u32 m0, %2, %3\n\ts_nop 0\n\tglobal_load_lds_dwordx4 %0, %1" POL_W ::"v"(voff), "s"(base), "s"(lbase), "n"(loff) : "memory", "scc");
  } else {
    if constexpr (SC1) asm volatile("s_add_u32 m0, %2, %3\n\ts_mov_b32 exec_hi, %4\n\tglobal_load_lds_dwordx4 %0, %1 sc1\n\ts_mov_b32 exec_hi, -1" ::"v"(voff), "s"(base), "s"(lbase), "n"(loff), "n"((1u << (NL - 32)) - 1u) : "memory", "scc");
    else asm volatile("s_add_u32 m0, %2, %3\n\ts_mov_b32 exec_hi, %4\n\tglobal_load_lds_dwordx4 %0, %1" POL_X "\n\ts_mov_b32 exec_hi, -1" ::"v"(voff), "s"(base), "s"(lbase), "n"(loff), "n"((1u << (NL - 32)) - 1u) : "memory", "scc");
  }
}
DEVINL void dma_w1(const char* base, u32 voff, u32 lds) {
  asm volatile("s_mov_b32 m0, %2\n\ts_nop 0\n\tglobal_load_lds_dwordx4 %0, %1" POL_1 ::"v"(voff), "s"(base), "s"(lds) : "memory");
}
DEVINL void dma_if(u32 a, u32 b, const char* base, u32 voff, u32 lbase, u32 loff) {
  asm volatile("s_cmp_lg_u32 %4, %5\n\ts_cbranch_scc1 .Ldi%=\n\ts_add_u32 m0, %2, %3\n\ts_nop 0\n\tglobal_load_lds_dwordx4 %0, %1 sc1\n.Ldi%=:"
               ::"v"(voff), "s"(base), "s"(lbase), "n"(loff), "s"(a), "n"(b) : "memory", "scc");
}
DEVINL void ld64_sc1_if(u32 a, u32 b, u32x2& d, const char* base, u32 voff) {
  asm volatile("s_cmp_lg_u32 %3, %4\n\ts_cbranch_scc1 .Lli%=\n\tglobal_load_dwordx2 %0, %1, %2 sc1\n.Lli%=:" : "=v"(d) : "v"(voff), "s"(base), "s"(a), "s"(b) : "memory", "scc");
}
DEVINL void st128_sc1(char* base, u32 voff, u32x4 v) {
  asm volatile("global_store_dwordx4 %0, %1, %2 sc1\n\ts_nop 1" ::"v"(voff), "v"(v), "s"(base) : "memory");
}
DEVINL void st128_sc1_m(char* base, u32 voff, u32x4 v, u64 mask) {
  asm volatile("s_mov_b64 exec, %3\n\tglobal_store_dwordx4 %0, %1, %2 sc1\n\ts_mov_b64 exec, -1\n\ts_nop 0" ::"v"(voff), "v"(v), "s"(base), "s"(mask) : "memory");
}
DEVINL void st64_sc1(char* base, u32 voff, u32x2 v) {
  asm volatile("global_store_dwordx2 %0, %1, %2 sc1" ::"v"(voff), "v"(v), "s"(base) : "memory");
}
DEVINL void st64(char* base, u32 voff, u32x2 v) {
  asm volatile("global_store_dwordx2 %0, %1, %2" ::"v"(voff), "v"(v), "s"(base) : "memory");
}
DEVINL void st32_sc1(char* base, u32 voff, u32 v) {
  asm volatile("global_store_dword %0, %1, %2 sc1" ::"v"(voff), "v"(v), "s"(base) : "memory");
}
DEVINL void st_flag(char* addr, u32 tag) {
  asm volatile("s_mov_b64 exec, 1\n\tglobal_store_dword %0, %1, %2 sc1\n\ts_mov_b64 exec, -1" ::"v"(0u), "v"(tag), "s"(addr) : "memory");
}
DEVINL void st_flag_if(u32 zero_to_store, char* addr, u32 tag) {
  asm volatile("s_cmp_lg_u32 %3, 0\n\ts_cbranch_scc1 .Lsf%=\n\ts_mov_b64 exec, 1\n\tglobal_store_dword %0, %1, %2 sc1\n\ts_mov_b64 exec, -1\n.Lsf%=:"
               ::"v"(0u), "v"(tag), "s"(addr), "s"(zero_to_store) : "memory", "scc");
}
DEVINL void flags_request(u32 sel, u32& v, const char* base, u32 voff) {
  asm volatile("s_cmp_lg_u32 %3, 0\n\ts_cbranch_scc1 .Lfq%=\n\tglobal_load_dword %0, %1, %2 sc1\n.Lfq%=:" : "+v"(v) : "v"(voff), "s"(base), "s"(sel) : "memory", "scc");
}
template <int N> DEVINL void flags_check(u32 sel, u32& v, const char* base, u32 voff, u32 tag) {
  u32 n;
  asm volatile(
      "s_cmp_lg_u32 %4, 0\n\ts_cbranch_scc1 .Lfe%=\n\ts_mov_b32 %1, %6\n\ts_waitcnt vmcnt(%7)\n"
      ".Lfl%=:\n\tv_cmp_ne_u32 vcc, %5, %0\n\ts_sub_u32 %1, %1, 1\n\ts_cbranch_vccz .Lfe%=\n\ts_cbranch_scc1 .Lfe%=\n\t"
      "global_load_dword %0, %2, %3 sc1\n\ts_waitcnt vmcnt(0)\n\ts_branch .Lfl%=\n"
      ".Lfe%=:"
      : "+v"(v), "=&s"(n) : "v"(voff), "s"(base), "s"(sel), "v"(tag), "n"(SPIN_CAP), "n"(N) : "memory", "vcc", "scc");
}
DEVINL void ld128(u32x4& d, const char* base, u32 voff, int imm) {
  asm volatile("global_load_dwordx4 %0, %1, %2 offset:%3" POL_X : "=v"(d) : "v"(voff), "s"(base), "n"(imm) : "memory");
}
DEVINL void ld128_sc1(u32x4& d, const char* base, u32 voff, int imm = 0) {
  asm volatile("global_load_dwordx4 %0, %1, %2 offset:%3 sc1" : "=v"(d) : "v"(voff), "s"(base), "n"(imm) : "memory");
}
DEVINL void ld64(u32x2& d, const char* base, u32 voff, int imm = 0) {
  asm volatile("global_load_dwordx2 %0, %1, %2 offset:%3" : "=v"(d) : "v"(voff), "s"(base), "n"(imm) : "memory");
}
DEVINL void ld32(u32& d, const char* base, u32 voff) {
  asm volatile("global_load_dword %0, %1, %2" POL_T : "=v"(d) : "v"(voff), "s"(base) : "memory");
}
DEVINL void touch(u32& d, const char* base, u32 voff) {
  asm volatile("global_load_dword %0, %1, %2" POL_T : "+v"(d) : "v"(voff), "s"(base) : "memory");
}
DEVINL void ld64_sc1(u32x2& d, const char* base, u32 voff) {
  asm volatile("global_load_dwordx2 %0, %1, %2 sc1" : "=v"(d) : "v"(voff), "s"(base) : "memory");
}
DEVINL void ld16(u32& d, const char* base, u32 voff) {
  asm volatile("global_load_ushort %0, %1, %2" : "=v"(d) : "v"(voff), "s"(base) : "memory");
}
template <int N> DEVINL void waitv() { asm volatile("s_waitcnt vmcnt(%0)" ::"n"(N) : "memory"); }
template <typename T> DEVINL void pin(T& v) { asm volatile("" : "+v"(v)); }
template <int N> DEVINL void bar_v() { asm volatile("s_waitcnt vmcnt(%0) lgkmcnt(0)\n\ts_barrier" ::"n"(N) : "memory"); }
template <int N> DEVINL void bar_vo() { asm volatile("s_waitcnt vmcnt(%0)\n\ts_barrier" ::"n"(N) : "memory"); }
DEVINL void bar() { asm volatile("s_waitcnt lgkmcnt(0)\n\ts_barrier" ::: "memory"); }

DEVINL void cnt_add(u32 c) {
  asm volatile("s_mov_b64 exec, 1\n\tds_add_u32 %0, %1\n\ts_mov_b64 exec, -1" ::"v"(L_CNT + 4 * c), "v"(1u) : "memory");
}
DEVINL void cnt_wait(u32 c) {
#pragma unroll 1
  for (u32 spins = 0; spins < (SPIN_CAP << 4); ++spins) {
    const u32 v = *(volatile LAS u32*)(L_CNT + 4 * c);
    if (__builtin_amdgcn_readfirstlane(v) >= 4u) break;
    __builtin_amdgcn_s_sleep(1);
  }
  asm volatile("" ::: "memory");
}
template <int N> DEVINL void sync4(u32 c) {
  asm volatile("s_waitcnt vmcnt(%0) lgkmcnt(0)" ::"n"(N) : "memory");
  cnt_add(c);
  cnt_wait(c);
}
DEVINL void poll_flags(const char* base, u32 voff, u32 tag) {
#pragma unroll 1
  for (u32 spins = 0; spins < SPIN_CAP; ++spins) {
    u32 v;
    asm volatile("global_load_dword %0, %1, %2 sc1\n\ts_waitcnt vmcnt(0)" : "=v"(v) : "v"(voff), "s"(base) : "memory");
    if (__all(v == tag)) break;
  }
}

#define STAMP(i)

#define FS(i)
DEVINL void feature_map2(const u32x4 (&wa)[5], const u32x2 (&wb)[5], const f32x4 (&t0)[2], const f32x4 (&t1)[2], const f32x4 (&t2)[2], u32 (&ph)[2][9]) {
  float p[2][20];
#pragma unroll
  for (int m = 0; m < 2; ++m) {
    const u32x2 p0 = pack4(t0[m]), p1 = pack4(t1[m]), p2 = pack4(t2[m]);
    const u32x4 b01 = {p0.x, p0.y, p1.x, p1.y};
    f32x4 a[5];
#pragma unroll
    for (int t = 0; t < 5; ++t) a[t] = mfma32(wa[t], b01, f32x4{0.f, 0.f, 0.f, 0.f});
#pragma unroll
    for (int t = 0; t < 5; ++t) {
      a[t] = mfma16(wb[t], p2, a[t]);
      p[m][4 * t] = a[t].x; p[m][4 * t + 1] = a[t].y; p[m][4 * t + 2] = a[t].z; p[m][4 * t + 3] = a[t].w;
    }
  }
  const float L2E = 1.44269504088896341f;
#pragma unroll
  for (int m = 0; m < 2; ++m) {
    float mx = p[m][0];
#pragma unroll
    for (int i = 1; i < 18; ++i) mx = fmaxf(mx, p[m][i]);
    const float nm = -mx * L2E;
    f32x2 sum = {0.f, 0.f};
#pragma unroll
    for (int i = 0; i < 18; ++i) p[m][i] = __builtin_amdgcn_exp2f(__builtin_fmaf(p[m][i], L2E, nm));
#pragma unroll
    for (int i = 0; i < 9; ++i) sum += f32x2{p[m][2 * i], p[m][2 * i + 1]};
    const float inv = __builtin_amdgcn_rcpf(sum.x + sum.y);
#pragma unroll
    for (int i = 0; i < 9; ++i) ph[m][i] = pack2(p[m][2 * i] * inv, p[m][2 * i + 1] * inv);
  }
}

DEVINL float gelu_tanh(float x) {
  const float C = -2.f * 0.7978845608028654f * 1.44269504088896341f;
  float y = x * __builtin_fmaf(x * x, 0.044715f * C, C);
  float e = __builtin_amdgcn_exp2f(y);
  return x * __builtin_amdgcn_rcpf(1.f + e);
}

__global__ __launch_bounds__(NT, 2) void fused_kernel(Params P) {
  extern __shared__ __attribute__((aligned(16))) char smem[];
  STAMP(0); FS(0);
  const u32 w = __builtin_amdgcn_readfirstlane(threadIdx.x >> 6);
  const u32 bid = blockIdx.x, xcd = bid & 7, idx = bid >> 3;
  char* const ws = P.ws;
  u64 pc; u32 len;
  asm volatile("s_getpc_b64 %0\n.Lfk_code_begin:\n\ts_mov_b32 %1, .Lfk_code_end-.Lfk_code_begin\n\ts_max_u32 %1, %1, .Lfk_copy_end-.Lfk_code_begin" : "=s"(pc), "=s"(len)::"scc");

  const u32 cg = (xcd & 1) * 4 + (idx & 3), rb = (xcd >> 1) * 8 + (idx >> 2);
  const u32 rot = (idx >> 2) % 6;
  const u32 rb2 = xcd * 4 + (idx >> 3), cj = idx & 7;
  const u32 w3 = w & 3, kh = w >> 2;
  const u32 kq = xcd >> 1, kp = xcd & 1;
  const bool g1 = idx < 27;
  const u32 ni = kq * 9 + (g1 ? idx / 3 : 0), mi = idx % 3;
  u32 tag, tag0;
  u32x4 wf[36];
  u32x4 xr[9];
  const u32 rgp = w ^ ((cg & 1) * 4);
  auto load_x = [&]() __attribute__((always_inline)) {
    LANEVARS;
    const u32 xo = (rb * 64 + rgp * 8 + (lane >> 3)) * 1152 + (lane & 7) * 16;
#pragma unroll
    for (int i = 0; i < 9; ++i) ld128(xr[i], P.x, xo, i * 128);
    asm volatile("s_load_dword %0, %1, 0x0" : "=s"(tag0) : "s"(ws) : "memory");
    FS(1);
  };
  auto layer_norm = [&]() __attribute__((always_inline)) {
    LANEVARS;
    f32x2 c[36];
    f32x2 s1 = {0.f, 0.f}, s2 = {0.f, 0.f};
#pragma unroll
    for (int i = 0; i < 9; ++i) {
#pragma unroll
      for (int j = 0; j < 4; ++j) {
        const f32x2 v = {bflo(xr[i][j]), bfhi(xr[i][j])};
        c[i * 4 + j] = v;
        s1 += v; s2 += v * v;
      }
    }
    const float mean = sum8(s1.x + s1.y) * (1.f / 576.f);
    const float var = fmaxf(sum8(s2.x + s2.y) * (1.f / 576.f) - mean * mean, 0.f);
    const float rs = __builtin_amdgcn_rsqf(var + 1e-5f), nb = -mean * rs;
    const u32 a0 = L_XN + ((rgp >> 1) * 18 + ((lane & 7) >> 2)) * 1024 + (lane & 3) * 256 + (((8 * (rgp & 1) + (lane >> 3)) ^ (lane & 7)) * 16);
#pragma unroll
    for (int i = 0; i < 9; ++i) {
      u32x4 o;
#pragma unroll
      for (int j = 0; j < 4; ++j) { const f32x2 v = c[i * 4 + j] * rs + nb; o[j] = pack2(v.x, v.y); }
      lds_w128(a0 + i * 2048, o);
    }
  };
#define WAIT_X(N) asm volatile("s_waitcnt vmcnt(" #N ")" : "+v"(xr[0]), "+v"(xr[1]), "+v"(xr[2]), "+v"(xr[3]), "+v"(xr[4]), "+v"(xr[5]), "+v"(xr[6]), "+v"(xr[7]), "+v"(xr[8])::"memory")
  const char* pw1 = P.w1 + (size_t)kp * 1024 * 9216 + kq * 2304;
  auto w1_line = [](u32 r0, u32 lane) -> u32 {
    const u32 r = min(r0 + min(lane / 18, 2u), 1023u);
    return (((r >> 5) & 1) * 512 + (r >> 6) * 32 + (r & 31)) * 9216 + (lane % 18) * 128;
  };
  u32x2 bias[2];
  const u32 wl = w3 * 3456;
  const char* wq = P.wqkv + cg * 144;
  auto stage = [&](u32 n) __attribute__((always_inline)) -> u32 { const u32 st = rot + n; return (st >= 6 ? st - 6 : st) * (96 * 3456); };
#define RING_DEF \
  const u32 r3 = lane / 18, c18 = lane % 18, seg = c18 / 9, ch9 = c18 % 9; \
  u32 wrow[4], voff[4]; \
  _Pragma("unroll") for (int i = 0; i < 4; ++i) { \
    const u32 r = 12 * w3 + 3 * i + r3; \
    wrow[i] = (r & ~12u) | ((r & 4u) << 1) | ((r & 8u) >> 1); \
    voff[i] = wrow[i] * 3456 + seg * 1152 + ch9 * 16; \
    pin(voff[i]); \
  } \
  auto issue1 = [&](int n, int j0, int j1) __attribute__((always_inline)) {   \
    constexpr u32 slot[9] = {0, 1, 2, 3, 4, 0, 1, 2, 3}; \
    const char* b = n < 6 ? wq + 1152 + stage(n) : wq; \
    const u32 qo = n < 6 ? 0u : (seg ? stage(2 * (n - 6) + 1) - 1152 : stage(2 * (n - 6))); \
    _Pragma("unroll") for (int j = j0; j < j1; ++j) \
      dma<54, false>(b + (j >> 2) * (48 * 3456), voff[j & 3] + qo, wl, slot[n] * SLOT + (j >> 2) * 13824 + (j & 3) * 864); \
  }

  if (__builtin_expect(w < 4, 1)) {
    const u32 mp = w >> 1, hp = w & 1;
    u32 adp = 0;
    load_x();
    {
      LANEVARS;
      RING_DEF;
#pragma unroll
      for (int a = 0; a < 3; ++a) {
        const u32 ic = a < 2 ? 16 * a + 4 * g + q4 : 32 + q4;
        const bool ok = (ic >= 18 ? 1u : 0u) == (p4 >> 1) && (a < 2 || g == 0);
        adp |= (ok ? (p4 & 1) * 360 + (hp * 36 + ic) * 5 : (L_ZERO - L_WPHI) / 8) << (10 * a);
      }
      pin(adp);
      WAIT_X(0);
      STAMP(1); FS(2);
      layer_norm();
      FS(3);
      issue1(1, 0, 8);
    }
    u32 ta0, ta1, xa[2];
    {
      LANEVARS;
      if (lane < 32) *(LAS u32*)(L_ZERO + lane * 4) = 0u;
      ta0 = trrow + hp * 72 + p4 * 8; ta1 = trrow + hp * 72 + 64 + (p4 & 1) * 144;
#pragma unroll
      for (int i = 0; i < 2; ++i) { xa[i] = L_XN + mp * 36864 + g * 256 + ((l15 ^ (((i + rot) & 1) * 4 + g)) * 16); pin(xa[i]); }
    }
    const LAddr trA(ta0);
    FS(4);
    asm volatile("s_waitcnt lgkmcnt(0)\n\ts_barrier\n\ts_add_u32 %0, %1, 1" : "=s"(tag) : "s"(tag0) : "memory", "scc");
    STAMP(2); FS(5);
    u32x4 xf[2][18];
    auto rdx = [&](int n) __attribute__((always_inline)) {
      u32 st = rot + n / 3; if (st >= 6) st -= 6;
      const u32 ad = xa[(n / 3 + n % 3) & 1] + st * 3072;
      xf[0][n] = lds_r128(ad + (n % 3) * 1024);
      xf[1][n] = lds_r128(ad + (n % 3) * 1024 + 18432);
    };
    f32x4 acc[5][2];
#pragma unroll
    for (int i = 0; i < 5; ++i) { acc[i][0] = f32x4{0.f, 0.f, 0.f, 0.f}; acc[i][1] = acc[i][0]; }
    u32x4 wa[5]; u32x2 wb[5];
    {
      const LAddr trM(ta1);
      u32x4 F[6];
      u32 ad[3];
      auto rd = [&](int u) __attribute__((always_inline)) {
        constexpr u32 toff[4] = {0, 32, 144, 176};
        const int n = u / 5, i = u % 5;
        const u32 b = ((n / 3) % 5) * SLOT + (n % 3) * 9216;
        F[u % 6] = i < 4 ? lds_tr2(trA(b + toff[i & 3]), trA(b + toff[i & 3] + 2304)) : lds_tr2(trM(b), trM(b + 2304));
      };
      rdx(0);
#pragma unroll
      for (int u = 0; u < 5; ++u) rd(u);
      rdx(1);
      SB();
#pragma unroll
      for (int n = 0; n < 18; ++n) {
        if (n == 16) {
#pragma unroll
          for (int a = 0; a < 3; ++a) { ad[a] = L_WPHI + ((adp >> (10 * a)) & 1023u) * 8; pin(ad[a]); }
        }
        if (n % 3 == 2 && n < 17) { FS(6 + 2 * (n / 3)); bar_v<0>(); FS(7 + 2 * (n / 3)); }
#pragma unroll
        for (int i = 0; i < 5; ++i) {
          const int u = n * 5 + i;
          if (u + 5 < 90) rd(u + 5);
          else { wa[i] = lds_tr2(ad[0] + 8 * i, ad[1] + 8 * i); wb[i] = lds_tr(ad[2] + 8 * i); }
          if (u < 8) { rdx(2 + 2 * u); rdx(3 + 2 * u); }
          acc[i][0] = mfma32(F[u % 6], xf[0][n], acc[i][0]);
          acc[i][1] = mfma32(F[u % 6], xf[1][n], acc[i][1]);
          SB();
        }
      }
    }
    STAMP(3); FS(16);
    {
      LANEVARS;
      const u32 va = L_V + (mp * 32 + l15) * 152 + hp * 72 + g * 8;
#pragma unroll
      for (int m = 0; m < 2; ++m) {
        lds_w64(va + m * (16 * 152), pack4(acc[2][m]));
        lds_w64(va + m * (16 * 152) + 32, pack4(acc[3][m]));
        if (g == 1) lds_w64(va + m * (16 * 152) + 56, pack4(acc[4][m]));
      }
      u32 ph[2][9];
      feature_map2(wa, wb, acc[0], acc[1], acc[4], ph);
      const u32 pa = L_PHI + (mp * 32 + l15) * 328 + (hp * 4 + g) * 40;
#pragma unroll
      for (int m = 0; m < 2; ++m)
#pragma unroll
        for (int i = 0; i < 5; ++i) lds_w64(pa + m * (16 * 328) + 8 * i, u32x2{ph[m][2 * i], i < 4 ? ph[m][2 * i + 1] : 0u});
    }
    FS(17);
    bar();
    FS(18);
    bar();
    STAMP(4); FS(20);
#pragma unroll
    for (int i = 0; i < 3; ++i) { acc[i][0] = f32x4{0.f, 0.f, 0.f, 0.f}; acc[i][1] = acc[i][0]; }
    {
      u32 tq;
      { LANEVARS; tq = trrow + hp * 72 + 64; }
      const LAddr trQ(tq);
      u32x4 F[6];
      auto rd = [&](int u) __attribute__((always_inline)) {
        const int n = u / 3, i = u % 3;
        const u32 b = (1 + n / 6) * SLOT + (n % 3) * 9216 + ((n % 6) / 3) * 144;
        F[u % 6] = i < 2 ? lds_tr2(trA(b + 32 * i), trA(b + 32 * i + 2304)) : lds_tr2(trQ(b), trQ(b + 2304));
      };
#pragma unroll
      for (int u = 0; u < 5; ++u) rd(u);
      SB();
#pragma unroll
      for (int u = 0; u < 54; ++u) {
        if (u + 5 < 54) rd(u + 5);
        acc[u % 3][0] = mfma32(F[u % 6], xf[0][u / 3], acc[u % 3][0]);
        acc[u % 3][1] = mfma32(F[u % 6], xf[1][u / 3], acc[u % 3][1]);
        SB();
      }
    }
    STAMP(5); FS(21);
    u32 cpf = 0;

    u32 pq[2][9];
    feature_map2(wa, wb, acc[0], acc[1], acc[2], pq);
    LANEVARS;
    u32 oa[3];
    {
      const bool same = (q4 >> 1) == (g >> 1);
#pragma unroll
      for (int t = 0; t < 3; ++t) {
        const u32 slot = 4 * t + p4;
        const u32 cc = slot < 9 ? 9 * (q4 & 1) + slot : 18;
        oa[t] = same ? L_KV + ((hp * 2 + (g >> 1)) * 19 + cc) * 96 + (g & 1) * 48 : L_ZERO;
        pin(oa[t]);
      }
    }
    const char* w1b = P.w1 + (size_t)(kp * 1024 + w * 4) * 9216 + ni * 256;
    u32 w1off = g * 9216 + ((((l15 >> 1) ^ (g | ((w & 2) << 1))) << 1) | (l15 & 1)) * 16, lane16 = lane * 16;
    pin(w1off); pin(lane16);
#pragma unroll
    for (int m = 0; m < 2; ++m)
#pragma unroll
      for (int i = 0; i < 9; ++i) pin(pq[m][i]);
    if (XR) touch(cpf, pw1, w1_line(384 + (idx * 4 + w) * XR, min(lane, XR * 18u - 1)));
    u32 po2 = w1_line(384 + XR * 128 + (idx * 4 + w) * XS, min(lane, XS * 18u - 1));
    pin(po2);
    if (RETOUCH & 1) touch(cpf, (const char*)pc, min(((idx * 4 + w) * 4 + p4) * 128u, len));
    STAMP(6); FS(22);
    bar();
    STAMP(7); FS(23);
    {
      u32x4 k0[3], k1[3]; u32x2 k2[3];
#pragma unroll
      for (int t = 2; t >= 0; --t) { k0[t] = lds_r128(oa[t]); k1[t] = lds_r128(oa[t] + 16); k2[t] = lds_r64(oa[t] + 32); }
      SB();
      f32x4 nu[2][3];
#pragma unroll
      for (int t = 2; t >= 0; --t)
#pragma unroll
        for (int m = 0; m < 2; ++m) {
          f32x4 c = mfma32(k0[t], u32x4{pq[m][0], pq[m][1], pq[m][2], pq[m][3]}, f32x4{0.f, 0.f, 0.f, 0.f});
          c = mfma32(k1[t], u32x4{pq[m][4], pq[m][5], pq[m][6], pq[m][7]}, c);
          nu[m][t] = mfma16(k2[t], u32x2{pq[m][8], 0u}, c);
        }
      char* ob = ws + W_O + (rb * 4 + mp * 2) * 18432;
#pragma unroll
      for (int m = 0; m < 2; ++m) {
        const float inv = __builtin_amdgcn_rcpf(nu[m][2].y + 1e-6f);
        const u32x2 a = pack4(nu[m][0] * inv), b = pack4(nu[m][1] * inv);
        st128_sc1(ob + m * 18432 + (cg * 2 + hp) * 1024, lane16, u32x4{a.x, a.y, b.x, b.y});
        const u32 e0 = asu(nu[m][2].x * inv);
        auto t16 = __builtin_amdgcn_permlane16_swap(e0, e0, false, false);
        const u32 e01 = pack2(asf(t16[0]), asf(t16[1]));
        auto t32 = __builtin_amdgcn_permlane32_swap(e01, e01, false, false);
        asm volatile("s_mov_b64 exec, 0xffff\n\tglobal_store_dwordx2 %0, %1, %2 sc1\n\ts_mov_b64 exec, -1" ::"v"(lane16), "v"(u32x2{t32[0], t32[1]}), "s"(ob + m * 18432 + 16384 + cg * 256 + hp * 8) : "memory");
      }
    }
    FS(24);
    if (XS) { touch(cpf, pw1, po2); waitv<1>(); } else waitv<0>();
    st_flag(ws + W_FLAGB + ((rb * 2 + mp) * 16 + cg * 2 + hp) * 128, tag);
    cnt_add(11);
    STAMP(8); FS(25);
    u32 po = w1_line((idx * 4 + w) * 3, lane);
    pin(po);
    ld64(bias[0], P.b1 + (ni * 4 + w3) * 64, g * 8);
    ld64(bias[1], P.b1 + (ni * 4 + w3) * 64, g * 8, 32);
    cnt_wait(6);
    FS(42);
    touch(cpf, pw1, po);
    asm volatile("s_waitcnt vmcnt(0)" : "+v"(cpf), "+v"(bias[0]), "+v"(bias[1])::"memory");
    FS(44);
    auto prefill = [&](int j0, int j1) __attribute__((always_inline)) {
      if (g1) {
#pragma unroll
        for (int j = j0; j < j1; ++j)
          dma<64, false>(w1b + (size_t)(((j >> 1) & 1) * 512 + (j >> 2) * 32 + (j & 1) * 16) * 9216, w1off, w * 1024, (((j >> 2) + 1) % 5) * 16384 + (j & 3) * 4096);
      }
    };
    prefill(0, 4 * PRE1);
    cnt_wait(7);
    prefill(4 * PRE1, 4 * PRE2);
    if (PRE2 < 5) { cnt_wait(13); prefill(4 * PRE2, 20); }
    FS(38);
    asm volatile("s_barrier" ::: "memory");
  } else {
    LANEVARS;
    const u32 lane16 = lane * 16;
    u32 pfd = 0;
    asm volatile("global_load_dword %0, %1, %2" : "+v"(pfd) : "v"(min(((idx * 4 + w3) * 4 + p4) * 128u, len)), "s"(pc) : "memory");
    load_x();
    RING_DEF;
    u32 wp[3];
    {
      const u32 so = (min(lane, 59u) / 10) * 36 + min(lane % 10, 8u) * 4;
#pragma unroll
      for (int j = 0; j < 3; ++j) ld32(wp[j], P.wphi + cg * 2592 + w3 * 648 + j * 216, so);
    }
    issue1(0, 0, 8);
    WAIT_X(8);
    FS(2);
    pin(wp[0]); pin(wp[1]); pin(wp[2]);
    layer_norm();
    if (lane < 60) {
#pragma unroll
      for (int j = 0; j < 3; ++j) {
        *(LAS u32*)(L_WPHI + w3 * 720 + j * 240 + lane * 4) = wp[j];
        *(LAS u32*)(L_WPHI + 2880 + w3 * 720 + j * 240 + lane * 4) = wp[j] ^ 0x80008000u;
      }
    }
    FS(4);
    asm volatile("s_waitcnt vmcnt(0) lgkmcnt(0)\n\ts_barrier\n\ts_add_u32 %0, %1, 1" : "=s"(tag) : "s"(tag0) : "memory", "scc");
    __builtin_amdgcn_s_setprio(1);
    issue1(2, 0, 8);
    const u32 srow = 8 * g + q4;
    u32 e_vb0 = L_V + srow * 152 + (w3 >> 1) * 72 + ((w3 & 1) ? 40 : 0) + p4 * 8, e_vb1 = L_V + srow * 152 + (w3 >> 1) * 72 + 32;
    u32 e_pb = L_PHI + srow * 328 + w3 * 80 + p4 * 8, e_pb2 = L_PHI + srow * 328 + w3 * 80 + 64 + (p4 & 1) * 8;
    u32 e_s0 = (lane >> 3) * 4096 + (lane & 7) * 16, e_s1[3];
    const u32 colp = l15 == 4 ? 2u : l15 - 2 * (w3 & 1);
#pragma unroll
    for (int i = 0; i < 3; ++i) {
      const u32 sidx = (160 + min(colp, 2u) * 10 + 4 * i + g);
      e_s1[i] = (sidx >> 3) * 4096 + (sidx & 7) * 16;
      pin(e_s1[i]);
    }
    const u64 e_m01 = __ballot(l15 == 4 || colp < 2), e_m2 = __ballot(l15 == 4 || (colp < 2 && g < 2));
    pin(e_vb0); pin(e_vb1); pin(e_pb); pin(e_pb2); pin(e_s0);
    u32 vow[4];
#pragma unroll
    for (int i = 0; i < 4; ++i) { vow[i] = (9 * (wrow[i] >> 3) + (wrow[i] & 7) + seg * 324) * 1152 + ch9 * 16; pin(vow[i]); }
    FS(6);
    bar_vo<8>();
    issue1(3, 0, 8);
    FS(8);
    bar_vo<8>();
    issue1(4, 0, 8);
    FS(10);
    bar_vo<8>();
    issue1(5, 0, 8);
    FS(12);
    bar_vo<8>();
    {
      touch(pfd, P.wo, min((idx * 4 + w3) * 41 + min(lane, 40u), 5183u) * 128);
    }
    {
      u32 pt = W_STATS + (rb2 * 4 + w3) * 2048 + cj * 256 + (lane & 1) * 128;
      if (lane == 2) pt = W_KVFIN + (cg * 96 + rb * 3 + min(w3, 2u)) * 128;
      if (lane == 3) pt = W_FLAGB + ((rb * 2 + (w3 >> 1)) * 16 + cg * 2 + (w3 & 1)) * 128;
      if (lane == 4) pt = W_FLAGD + (cj * 32 + rb2) * 128;
      if (lane >= 5 && lane < 9) pt = W_FLAGE + min((bid * 4 + w3) * 4 + lane - 5, (W_KVFIN - W_FLAGE) / 128 - 1) * 128;
      touch(pfd, ws, pt);
    }
    FS(14);
    bar_vo<2>();
    issue1(6, 0, 8);
    issue1(7, 0, 8);
    issue1(8, 0, 8);
    const char* wob = P.wo + cj * 144;
    __builtin_amdgcn_s_setprio(3);
    FS(17);
    bar();
    FS(18);
    {
      u32x4 vf[2][2], pf_[2][3];
#pragma unroll
      for (int ks = 0; ks < 2; ++ks) {
        vf[ks][0] = lds_tr2(e_vb0 + ks * (32 * 152), e_vb0 + ks * (32 * 152) + 4 * 152);
        pf_[ks][0] = lds_tr2(e_pb + ks * (32 * 328), e_pb + ks * (32 * 328) + 4 * 328);
        pf_[ks][1] = lds_tr2(e_pb + ks * (32 * 328) + 32, e_pb + ks * (32 * 328) + 32 + 4 * 328);
        pf_[ks][2] = lds_tr2(e_pb2 + ks * (32 * 328), e_pb2 + ks * (32 * 328) + 4 * 328);
      }
#pragma unroll
      for (int ks = 0; ks < 2; ++ks) vf[ks][1] = lds_tr2(e_vb1 + ks * (32 * 152), e_vb1 + ks * (32 * 152) + 4 * 152);
      SB();
      f32x4 kv[2][3];
#pragma unroll
      for (int vt = 0; vt < 2; ++vt)
#pragma unroll
        for (int ks = 0; ks < 2; ++ks) {
          if (vt && l15 >= 4) vf[ks][1] = u32x4{0x3f803f80u, 0x3f803f80u, 0x3f803f80u, 0x3f803f80u};
#pragma unroll
          for (int i = 0; i < 3; ++i) kv[vt][i] = mfma32(pf_[ks][i], vf[ks][vt], ks ? kv[vt][i] : f32x4{0.f, 0.f, 0.f, 0.f});
        }
      char* kb = ws + W_KVPART + ((cg * 96 + w3 * 24) * 32 + rb) * 128;
#pragma unroll
      for (int i = 0; i < 3; ++i) {
        const u32x2 pk = pack4(kv[0][i]);
        st128_sc1_m(kb + i * 32768, e_s0, u32x4{pk.x, tag, pk.y, tag}, i < 2 ? ~0ull : 0xffffffffull);
      }
#pragma unroll
      for (int i = 0; i < 3; ++i) {
        const u32x2 pk = pack4(kv[1][i]);
        st128_sc1_m(kb, e_s1[i], u32x4{pk.x, tag, pk.y, tag}, i < 2 ? e_m01 : e_m2);
      }
    }
    FS(19);
    bar_v<6>();
    u32x2 xres[5];
    auto issue_wo = [&](int part) __attribute__((always_inline)) {
#pragma unroll
      for (int j = 0; j < 8; ++j) dma<54, false>(wob + (part * 108 + (j >> 2) * 54) * 1152, vow[j & 3], wl, part * 4 * SLOT + (j >> 2) * 13824 + (j & 3) * 864);
    };
    issue_wo(0);
    u32 la[3], vo2[8];
    if (DLY_A1) __builtin_amdgcn_s_sleep(DLY_A1);
    FS(26);
    {
      const u32 line = cg * 96 + rb * 3 + min(w3, 2u);
      const char* pb = ws + W_KVPART + line * 4096;
      u32x4 v[4];
#pragma unroll
      for (int i = 0; i < 4; ++i) ld128_sc1(v[i], pb, lane16, i * 1024);
    {
#pragma unroll
      for (int a = 0; a < 3; ++a) {
        const u32 r = a * 64 + lane;
        const u32 rr = r - 160, cp = (rr * 205) >> 11;
        const u32 cc = r < 160 ? (r & 15) + 2 * (w3 & 1) : (cp >= 2 ? 18 : ((w3 & 1) ? cp : 16 + cp));
        const u32 piece = r < 160 ? (r >> 4) : rr - cp * 10;
        la[a] = r < 190 ? L_KV + (w3 * 19 + cc) * 96 + piece * 8 + (piece >= 5 ? 8 : 0) : L_KV + 40;
        pin(la[a]);
      }
    }
    {
#pragma unroll
      for (int j = 0; j < 8; ++j) {
        const u32 kap = 192 + (j >> 2) * 48 + wrow[j & 3] + seg * 288;
        vo2[j] = (kap < 512 ? 9 * (kap >> 3) + (kap & 7) : 9 * (kap - 512) + 8) * 1152 + ch9 * 16;
        pin(vo2[j]);
      }
    }
      waitv<0>();
#pragma unroll 1
      for (u32 spins = 0; spins < SPIN_CAP; ++spins) {
        u32 bad = 0;
#pragma unroll
        for (int i = 0; i < 4; ++i) { pin(v[i]); bad |= (v[i].y ^ tag) | (v[i].w ^ tag); }
        pin(bad);
        if (__all(bad == 0)) break;
#pragma unroll
        for (int i = 0; i < 4; ++i) ld128_sc1(v[i], pb, lane16, i * 1024);
        waitv<0>();
      }
#pragma unroll
      for (int i = 0; i < 4; ++i) pin(v[i]);
      f32x4 sum = (unpk(v[0].x, v[0].z) + unpk(v[1].x, v[1].z)) + (unpk(v[2].x, v[2].z) + unpk(v[3].x, v[3].z));
#pragma unroll
      for (int j = 0; j < 4; ++j) sum[j] = sum_g(sum[j] + dpp<0x128>(sum[j]));
      const u32x2 pk = pack4(sum);
      if (w3 < 3) st128_sc1_m(ws + W_KVFIN + line * 128, lane16, u32x4{pk.x, tag, pk.y, tag}, 0xffull);
    }
    FS(27);
    {
      issue_wo(1);
      if (DLY_A2) __builtin_amdgcn_s_sleep(DLY_A2);
      FS(28);
      const char* fb = ws + W_KVFIN + (cg * 4 + w3) * 3072;
      u32x4 v[3];
#pragma unroll
      for (int i = 0; i < 3; ++i) ld128_sc1(v[i], fb, lane16, i * 1024);
      {
        const u32 xo = (rb2 * 64 + w3 * 16 + l15) * 1152 + cj * 144;
#pragma unroll
        for (int n = 0; n < 4; ++n) ld64(xres[n], P.x, xo + g * 8, n * 32);
        ld64(xres[4], P.x, xo + (g & 1) * 8, 128);
      }
      waitv<5>();
#pragma unroll 1
      for (u32 spins = 0; spins < SPIN_CAP; ++spins) {
        u32 bad = 0;
#pragma unroll
        for (int i = 0; i < 3; ++i) { pin(v[i]); bad |= (v[i].y ^ tag) | (v[i].w ^ tag); }
        pin(bad);
        if (__all(bad == 0)) break;
#pragma unroll
        for (int i = 0; i < 3; ++i) ld128_sc1(v[i], fb, lane16, i * 1024);
        waitv<0>();
      }
#pragma unroll
      for (int i = 0; i < 3; ++i) { pin(v[i]); lds_w64(la[i], u32x2{v[i].x, v[i].z}); }
    }
    FS(22);
    pin(pfd);
    __builtin_amdgcn_s_setprio(0);
    bar();
#pragma unroll
    for (int j = 0; j < 8; ++j) dma<54, false>(wob, vo2[j], wl, 3 * SLOT + (j >> 2) * 13824 + (j & 3) * 864);
    const LAddr wo0(trrow + p4 * 8), wo4(trrow + 128 + (p4 & 1) * 8);
    auto rdw = [&](u32 b, int n) __attribute__((always_inline)) -> u32x4 {
      return n < 4 ? lds_tr2(wo0(b + 32 * n), wo0(b + 32 * n + 2304)) : lds_tr2(wo4(b), wo4(b + 2304));
    };
    u32x4 wm[30];
#pragma unroll
    for (int u = 0; u < 30; ++u) wm[u] = rdw(((u / 5) % 3) * 9216 + (u / 15) * 144, u % 5);
    sync4<0>(5);
    f32x4 hc[5];
#pragma unroll
    for (int n = 0; n < 5; ++n) { pin(xres[n]); hc[n] = unpk(xres[n].x, xres[n].y); pin(hc[n]); }
    {
      u32x4 F[6];
      auto rd = [&](int u) __attribute__((always_inline)) {
        const int v = u / 5;
        F[u % 6] = rdw((v < 6 ? 4 * SLOT : 3 * SLOT) + (v % 3) * 9216 + ((v / 3) & 1) * 144, u % 5);
      };
#pragma unroll
      for (int u = 0; u < 5; ++u) rd(u);
      FS(29);
      __builtin_amdgcn_s_setprio(3);
      cnt_wait(11);
      if (DLY_B) __builtin_amdgcn_s_sleep(DLY_B);
      poll_flags(ws + W_FLAGB + (rb2 * 2 + (w3 >> 1)) * 2048, l15 * 128, tag);
      STAMP(9); FS(30);
      constexpr int ord[18] = {3, 4, 5, 12, 13, 14, 6, 7, 8, 15, 16, 17, 0, 1, 2, 9, 10, 11};
      u32x4 of[18];
      const char* ob = ws + W_O + (rb2 * 4 + w3) * 18432;
#pragma unroll
      for (int j = 0; j < 18; ++j) ld128_sc1(of[j], ob + (ord[j] >> 2) * 4096, lane16, (ord[j] & 3) * 1024);
      cnt_add(6);
      FS(31);
#pragma unroll
      for (int j = 0; j < 18; ++j) {
        asm volatile("s_waitcnt vmcnt(%1)" : "+v"(of[j]) : "n"(17 - j) : "memory");
#pragma unroll
        for (int n = 0; n < 5; ++n) {
          const int u = j * 5 + n;
          if (u + 5 < 60) rd(u + 5);
          hc[n] = mfma32(u < 60 ? F[u % 6] : wm[u - 60], of[j], hc[n]);
          SB();
        }
      }
    }
    STAMP(10); FS(32);
    {
    LANEVARS;
    const u32 lane16 = lane * 16;
    float s1 = 0.f, s2 = 0.f;
    u32x2 hr[5];
#pragma unroll
    for (int n = 0; n < 5; ++n) {
      hr[n] = pack4(hc[n]);
      if (n == 4 && g >= 2) hr[n] = u32x2{0u, 0u};
      s1 = dot2(hr[n].x, 0x3f803f80u, s1); s1 = dot2(hr[n].y, 0x3f803f80u, s1);
      s2 = dot2(hr[n].x, hr[n].x, s2); s2 = dot2(hr[n].y, hr[n].y, s2);
    }
    s1 = sum_g(s1); s2 = sum_g(s2);
    char* sb = ws + W_STATS + (rb2 * 4 + w3) * 2048;
    st128_sc1_m(sb + cj * 256, lane16, u32x4{asu(s1), tag, asu(s2), tag}, 0xffffull);
    FS(34);
    u32 hw = L_HI + (w3 * 16 + l15) * 160 + g * 8;
#pragma unroll
    for (int n = 0; n < 5; ++n) {
      lds_w64(hw + n * 32, hr[n]);
      hc[n] = unpk(hr[n].x, hr[n].y);
      pin(hc[n]);
    }
    u32 hrd = L_HN + (w3 * 16 + 8 * (g & 1) + q4) * 160 + ((g >> 1) * 16 + p4 * 4) * 2;
    u32 hrx = L_HN + (w3 * 16 + 8 * (g & 1) + q4) * 160 + (64 + (p4 & 1) * 4) * 2;
    u32 ho[2];
#pragma unroll
    for (int j = 0; j < 2; ++j) {
      const u32 d = cj * 72 + (j ? 64 + (l15 & 7) : (g >> 1) * 16 + l15);
      ho[j] = (d >> 5) * 131072 + (g & 1) * 512 + (d & 31) * 16;
      pin(ho[j]);
    }
    u32x4 hrow[3];
    u32 hso[3];
#pragma unroll
    for (int j = 0; j < 3; ++j) {
      const u32 it = min(j * 64 + lane, 143u);
      hrow[j] = lds_r128(L_HI + (w3 * 16 + it / 9) * 160 + (it % 9) * 16);
      hso[j] = it * 16;
      pin(hso[j]);
    }
    pin(hw); pin(hrd); pin(hrx);
    char* hb = ws + W_HN + (rb2 * 4 + w3) * 1024;
    u32x4 v0, v1;
    if (HPOS == 1) {
#pragma unroll
    for (int j = 0; j < 3; ++j) st128_sc1(ws + W_H + (cj * 2048 + rb2 * 64 + w3 * 16) * 144, hso[j], hrow[j]);
    }
    if (DLY_C) __builtin_amdgcn_s_sleep(DLY_C);
    FS(35);
#pragma unroll 1
    for (u32 spins = 0; spins < SPIN_CAP; ++spins) {
      ld128_sc1(v0, sb, lane16);
      ld128_sc1(v1, sb, lane16, 1024);
      waitv<0>();
      pin(v0); pin(v1);
      u32 bad = (v0.y ^ tag) | (v0.w ^ tag) | (v1.y ^ tag) | (v1.w ^ tag);
      pin(bad);
      if (__all(bad == 0)) break;
    }
    FS(36);
    s1 = sum_g(asf(v0.x) + asf(v1.x));
    s2 = sum_g(asf(v0.z) + asf(v1.z));
    const float mean = s1 * (1.f / 576.f);
    const float var = fmaxf(s2 * (1.f / 576.f) - mean * mean, 0.f);
    const float r1 = __builtin_amdgcn_rsqf(var + 1e-5f);
    const float r2 = r1 * __builtin_amdgcn_rsqf(var * r1 * r1 + 1e-5f);
    const float nb = -mean * r2;
#pragma unroll
    for (int n = 0; n < 5; ++n) lds_w64(hw + 10240 + n * 32, pack4(hc[n] * r2 + nb));
    const u32x4 f0 = lds_tr2(hrd, hrd + 4 * 160), f1 = lds_tr2(hrd + 64, hrd + 64 + 4 * 160), f2 = lds_tr2(hrx, hrx + 4 * 160);
    STAMP(11); FS(37);
    st128_sc1(hb, ho[0], f0);
    st128_sc1(hb + 131072, ho[0], f1);
    st128_sc1_m(hb, ho[1], f2, 0x00ff00ffull);
    ld64(bias[0], P.b1 + (ni * 4 + w3) * 64, g * 8);
    ld64(bias[1], P.b1 + (ni * 4 + w3) * 64, g * 8, 32);
    cnt_add(7);
    sync4<0>(10);
    pin(bias[0]); pin(bias[1]);
    st_flag_if(w3, ws + W_FLAGD + (cj * 32 + rb2) * 128, tag);
    if (PRE2 < 5) cnt_add(13);
    if (HPOS == 0) {
#pragma unroll
    for (int j = 0; j < 3; ++j) st128_sc1(ws + W_H + (cj * 2048 + rb2 * 64 + w3 * 16) * 144, hso[j], hrow[j]);
    }
    __builtin_amdgcn_s_setprio(0);
    FS(38);
    asm volatile("s_barrier" ::: "memory");
    asm volatile(".Lfk_copy_end:");
    }
  }
  STAMP(12); FS(39);
  LANEVARS;
  const u32 lane16 = lane * 16;
  const u32 wcol = ((((l15 >> 1) ^ ((w * 4 + g) & 7)) << 1) | (l15 & 1)) * 16;

  const u32 Mq = idx & 3, Nq = kp * 8 + (idx >> 2), odd = Mq & 1;
  const char* w2b = P.w2 + (size_t)(kq * 1152 + w * 4) * 4096 + Nq * 256;
  const u32 w2off = g * 4096 + wcol;
  u32 w1k = w * 1024;
  asm volatile("" : "+s"(w1k));
  auto issue_w2 = [&](int ps, u32 unit) __attribute__((always_inline)) {
    const char* b = w2b + ps * (32 * 4096);
    dma<64, false>(b, w2off, w1k, unit * 16384);
    dma<64, false>(b + 18 * (32 * 4096), w2off, w1k, unit * 16384 + 8192);
  };
  static constexpr u32 un2[18] = {8, 6, 7, 0, 1, 2, 3, 8, 6, 7, 0, 1, 4, 5, 9, 2, 3, 0};
  u32 tw2;
  { LANEVARS; tw2 = kh * 8192 + (4 * g + q4) * 256 + (((2 * w3 + kh) ^ (4 * (g & 1) + q4)) * 32) + p4 * 8; }
#define GRAB2_DEF \
  const LAddr t2w(tw2), t2x(tw2 ^ 32); \
  auto grab = [&](int ps, u32 unit) __attribute__((always_inline)) { \
    const u32 sl = unit * 16384; \
    wf[2 * ps] = lds_tr2(t2w(sl), t2w(sl + 4096)); \
    wf[2 * ps + 1] = lds_tr2(t2x(sl), t2x(sl + 4096)); \
  }
  const f32x4 zero4 = {0.f, 0.f, 0.f, 0.f};
  const u32 ord = mi == 0 ? 0x321504u : (mi == 1 ? 0x251403u : 0x504321u);
  auto tile_of = [&](int t) __attribute__((always_inline)) -> u32 {
    const u32 pos = t < 3 ? 2 * t + (kp ^ 1) : 2 * (t - 3) + kp;
    return mi * 6 + ((ord >> (4 * pos)) & 15u);
  };
  const u32 f16 = ni * 4 + w3 + kh * 144;
  u32 usel = w | ((idx + 5) >> 5);
  asm volatile("" : "+s"(usel));
  auto flag_u = [&](int t) __attribute__((always_inline)) {
    st_flag_if(usel, ws + W_FLAGE + (tile_of(t) * 36 + ni) * 128, tag);
  };
  float pre[3][8];
  auto finish_u = [&](int q) __attribute__((always_inline)) {
    u32x4 uo;
#pragma unroll
    for (int j = 0; j < 4; ++j) uo[j] = pack2(gelu_tanh(pre[q][2 * j]), gelu_tanh(pre[q][2 * j + 1]));
    st128_sc1(ws + W_U + (tile_of(3 + q) * 288 + f16) * 1024, lane16, uo);
  };
  const char* pw2 = P.w2 + (size_t)kq * 1152 * 4096 + kp * 2048;
  if (g1) {
    const char* hb = ws + W_HN + kp * 65536;
    u32 la24 = kh * 12288 + w3 * 1024;
    asm volatile("" : "+s"(la24));
    auto issue_a = [&](int d, int j) __attribute__((always_inline)) {
      dma<64, true>(hb + tile_of(j) * 131072 + (kh * 32 + w3 + d * 4) * 1024, lane16, la24, 81920 + ((d + 1) % 3) * 24576 + j * 4096);
    };
    const char* w1s = P.w1 + (size_t)(kp * 1024 + w * 4) * 9216 + ni * 256;
    const u32 w1o = g * 9216 + ((((l15 >> 1) ^ (g | ((w & 2) << 1))) << 1) | (l15 & 1)) * 16;
    auto issue_w = [&](int st, int j) __attribute__((always_inline)) {
      dma<64, false>(w1s + (size_t)(j * 512 + st * 32) * 9216, w1o, w1k, ((st + 1) % 5) * 16384 + j * 8192);
    };
    static constexpr u32 regb[6] = {1, 2, 3, 0, 1, 2};
    auto issue_b = [&](int i, int j) __attribute__((always_inline)) {
      dma<64, true>(hb + tile_of(3 + (i >> 1)) * 131072 + w1k + ((j >> 1) * 32 + (i & 1) * 16 + (j & 1) * 8) * 1024, lane16, w1k, regb[i] * 32768 + j * 8192);
    };
    u32 tw0 = kh * 8192 + (8 * g + q4) * 256 + (((2 * w3) ^ (4 * (g & 1) + q4)) * 32) + p4 * 8, tw1 = tw0 ^ 32;
    u32 tw0h = tw0 + 65536, tw1h = tw1 + 65536;
    const u32 aa = kh * 256 + (g >> 1) * 1024 + (g & 1) * 512 + l15 * 16;
    u32 pa0 = 81920 + 24576 + kh * 12288 + aa, pa1 = pa0 ^ 256, pa0l = pa0 - 24576, pa1l = pa1 - 24576;
    pin(tw0); pin(tw1); pin(tw0h); pin(tw1h); pin(pa0); pin(pa1); pin(pa0l); pin(pa1l);
    {
      const u32 cp = min((mi * 192) / 72 + q4, 7u);
      poll_flags(ws + W_FLAGD, (cp * 32 + kp * 16 + kh * 8 + (p4 >> 1) * 2 * w3 + (p4 & 1)) * 128, tag);
      FS(52);
#pragma unroll
      for (int j = 0; j < 6; ++j) issue_a(j / 3, j % 3);
    }
    STAMP(13); FS(54);
    bar_vo<3>();
    FS(55);
    st_flag_if(w ^ 2, ws + W_FLAGH + (rb2 * 8 + cj) * 128, tag);
    const LAddr ar0(kh * 16384 + aa), ar1((kh * 16384 + aa) ^ 256);
    u32 exw = 131072 + w * 1024 + lane16, exr = 131072 + (w ^ 4) * 1024 + lane16;
    f32x4 ca[3][2][2], cc[2][2][2], ci[2];
    u32x4 a[6], rx, r3[3];
    u32 pfd = 0, fv = 0, zo = 0;
    pin(zo);
    auto rdw = [&](int st, int e) __attribute__((always_inline)) {
      const u32 o = ((st + 1) % 5) * 16384;
      const u32 b = o < 65536 ? (e ? tw1 : tw0) + o : (e ? tw1h : tw0h);
      wf[2 * st + e] = lds_tr2(b, b + 1024);
    };
    auto rd = [&](int n) __attribute__((always_inline)) {
      if (n < 96) {
        const int st = n / 6, j = n % 6, sl = (st / 2 + 1) % 3;
        const u32 o = (j >> 1) * 4096 + (st & 1) * 2048;
        a[n % 6] = lds_r128(sl ? ((j & 1) ? pa1 : pa0) + (sl - 1) * 24576 + o : ((j & 1) ? pa1l : pa0l) + o);
      } else {
        const u32 o = regb[((n - 96) >> 4) % 6] * 32768 + (n & 14) * 1024;
        a[n % 6] = lds_r128((n & 1) ? ar1(o) : ar0(o));
      }
    };
    char* fl_own = ws + W_FLAGU + (((ni * 3 + mi) * 2 + kp) * 8 + w) * 128;
    const char* fl_par = ws + W_FLAGU + (((ni * 3 + mi) * 2 + (kp ^ 1)) * 8 + w) * 128;
    auto pack8 = [&](f32x4 lo, f32x4 hi) __attribute__((always_inline)) -> u32x4 {
      const u32x2 p0 = pack4(lo), p1 = pack4(hi);
      return u32x4{p0.x, p0.y, p1.x, p1.y};
    };
    rdw(0, 0); rdw(0, 1);
#pragma unroll
    for (int n = 0; n < 5; ++n) rd(n);
    SB();
    sfor<0, 96>([&](auto NN) __attribute__((always_inline)) {
      constexpr int n = decltype(NN)::value, st = n / 6, j = n % 6;
      rd(n + 5);
      if (st < 15 && (j == 1 || j == 2)) rdw(st + 1, j - 1);
#pragma unroll
      for (int e = 0; e < 2; ++e) ca[j >> 1][j & 1][e] = mfma32(wf[2 * st + e], a[j], st ? ca[j >> 1][j & 1][e] : zero4);
      SB();
      if (j == 0) {
        constexpr int vm[16] = {63, 5, 63, 7, 63, 7, 63, 7, 63, 7, 63, 7, 63, 4, 63, 8};
        if (st == 15) asm volatile("s_waitcnt lgkmcnt(0)" ::: "memory");
        asm volatile("s_waitcnt vmcnt(%0)\n\ts_barrier" ::"n"(vm[st]) : "memory");
      }
      if (st <= 10) {
        if (st & 1) { if (j == 1 || j == 3) issue_w(st + 5, j >> 1); }
        else if (j < 2) issue_w(st + 5, j);
        else if (j < 5) issue_a(st / 2 + 2, j - 2);
      } else if (st >= 12 && j < 4) issue_b(st - 12, j);
      if (st == 15 && j == 3) { ci[0] = unpk(bias[0].x, bias[0].y); ci[1] = unpk(bias[1].x, bias[1].y); pin(ci[0]); pin(ci[1]); pin(exw); pin(exr); }
      SB();
    });
    FS(56);
    auto grab_h = [&](int f) __attribute__((always_inline)) {
      const u32 la = (tw2 ^ ((f & 1) * 32)) + un2[f >> 1] * 16384;
      wf[f] = lds_tr2(la, la + 4096);
    };
    float v8[8];
    u32x4 uo;
    auto gelu_op = [&](int q) __attribute__((always_inline)) {
      const float C = -2.f * 0.7978845608028654f * 1.44269504088896341f;
      if (q < 12 || (q >= 20 && q < 24) || (q >= 32 && q < 36)) {
#pragma unroll
        for (int h = 0; h < 2; ++h) {
          const int j = (q & 3) * 2 + h;
          v8[j] = q < 4 ? pre[0][j] * pre[0][j] : (q < 8 ? __builtin_fmaf(v8[j], 0.044715f * C, C) : (q >= 20 && q < 24 ? v8[j] + 1.f : pre[0][j] * v8[j]));
          pin(v8[j]);
        }
      } else if (q < 20) { v8[q - 12] = __builtin_amdgcn_exp2f(v8[q - 12]); pin(v8[q - 12]); }
      else if (q < 32) { v8[q - 24] = __builtin_amdgcn_rcpf(v8[q - 24]); pin(v8[q - 24]); }
      else {
        const int j = (q - 36) * 4;
        u32 x = pack2(v8[j], v8[j + 1]), y = pack2(v8[j + 2], v8[j + 3]);
        pin(x); pin(y);
        uo[j >> 1] = x; uo[(j >> 1) + 1] = y;
      }
    };
    auto acc8 = [&](const f32x4 (&c)[2], int j) __attribute__((always_inline)) -> float { return c[j >> 2][j & 3]; };
    auto half = [&](const u32x4& p, int j) __attribute__((always_inline)) -> float { return (j & 1) ? bfhi(p[j >> 1]) : bflo(p[j >> 1]); };
    auto pack_op = [&](u32x4& d, const f32x4 (&c)[2], int q) __attribute__((always_inline)) {
      u32 x = pack2(c[q][0], c[q][1]), y = pack2(c[q][2], c[q][3]);
      pin(x); pin(y);
      d[2 * q] = x; d[2 * q + 1] = y;
    };
    sfor<0, 192>([&](auto NN) __attribute__((always_inline)) {
      constexpr int G = decltype(NN)::value, m = G / 2, e = G & 1, n = 96 + m, i = m / 16, k = m % 16, t = 3 + i / 2, x = (i & 1) * 16 + (k & 14);
      if (e == 0 && n + 5 < 192) rd(n + 5);
      cc[t & 1][k & 1][e] = mfma32(wf[x + e], a[n % 6], x ? cc[t & 1][k & 1][e] : ((k & 1) ? zero4 : ci[e]));
      SB();
      if (G >= 2 && G < 8) pack_op(r3[((G - 2) >> 1) % 3], ca[((G - 2) >> 1) % 3][1], G & 1);
      if (G >= 13 && G < 13 + 33) {
        constexpr int q = ((G - 13) / 11) % 3, o = (G - 13) % 11;
        if (o < 8) { v8[o] = acc8(ca[q][0], o) + half(r3[q], o); pin(v8[o]); }
        else if (o < 10) { u32 px = pack2(v8[(o - 8) * 4], v8[(o - 8) * 4 + 1]), py = pack2(v8[(o - 8) * 4 + 2], v8[(o - 8) * 4 + 3]); pin(px); pin(py); r3[q][(o - 8) * 2] = px; r3[q][(o - 8) * 2 + 1] = py; }
        else st128_sc1(ws + W_UP + (tile_of(q) * 288 + f16) * 1024, lane16, r3[q]);
      }
      if ((i == 2 || i == 4) && k == 2) pack_op(rx, cc[(t - 1) & 1][1], e);
      if (G == TG) touch(pfd, pw2, ((T3R && g >= 2) ? (w & 1) * 576 + 216 + idx * 4 + (w >> 1) : (g & 1) * 576 + idx * 8 + w) * 4096 + (lane16 & 0xf0) * 8);
      if (G == FLG) { waitv<(TG > 45 && TG < FLG) ? 5 : 4>(); st_flag(fl_own, tag); }
      if (G >= 92 && G < 100) { constexpr int j = (G - 92) & 7; pre[0][j] = acc8(cc[1][0], j) + half(rx, j); pin(pre[0][j]); }
      if (G == RQG) flags_request(0u, fv, fl_par, zo);
      if (G == 109) flags_check<0>(0u, fv, fl_par, zo, tag);
      if (G >= 109 && G < 112) ld128_sc1(r3[(G - 109) % 3], ws + W_UP + (tile_of(3 + (G - 109) % 3) * 288 + f16) * 1024, lane16);
      if (G == 126) { waitv<6>(); pin(r3[0]); }
      if (G >= 126 && G < 136 && (G < 132 || G >= 134)) { constexpr int j = (G < 132 ? G - 126 : G - 128) & 7; pre[0][j] += half(r3[0], j); pin(pre[0][j]); }
      if (G >= 136 && G < 174) gelu_op(G - 136);
      if (G == 175) st128_sc1(ws + W_U + (tile_of(3) * 288 + f16) * 1024, lane16, uo);
      if (G >= 176 && G < 184) { constexpr int j = (G - 176) & 7; pre[1][j] = acc8(cc[0][0], j) + half(rx, j); pin(pre[1][j]); }
      if (G >= 184) { constexpr int j = (G - 184) & 7; pre[1][j] += half(r3[1], j); pin(pre[1][j]); }
      if (e == 1) {
        if (k == (i == 0 ? 4 : 8)) {
          constexpr int vm[6] = {TG < 9 ? 9 : 8, TG < 50 ? 8 : 7, 63, 63, 0, 1};
          if (i == 0) { lds_w128(exw, r3[0]); lds_w128(exw + 8192, r3[1]); lds_w128(exw + 16384, r3[2]); exw += 16384; }
          if (i == 2 || i == 4) lds_w128(exw, rx);
          if (!(i & 1)) asm volatile("s_waitcnt lgkmcnt(0)" ::: "memory");
          asm volatile("s_waitcnt vmcnt(%0)\n\ts_barrier" ::"n"(vm[i]) : "memory");
          if (i == 0) { r3[0] = lds_r128(exr); r3[1] = lds_r128(exr + 8192); r3[2] = lds_r128(exr + 16384); exr += 16384; }
          if (i == 4) { pin(pfd); pin(r3[1]); pin(r3[2]); }
          if (i == 2 || i == 4) rx = lds_r128(exr);
        }
        if ((i == 1 || i == 2) && k >= 9 && k < 13) issue_b(i + 3, k - 9);
        if (i == 2 && k == 13) issue_w2(0, 8);
        if ((i == 3 || i == 4) && (k == 9 || k == 11)) issue_w2(2 * (i - 3) + 1 + ((k >> 1) & 1), un2[(2 * (i - 3) + 1 + ((k >> 1) & 1)) % 18]);
        if (i == 5 && k >= 9 && k < 14) issue_w2(k - 4, un2[(k - 4) % 18]);
      } else {
        if (i == 4 && k >= 10) grab_h((k - 10) % 10);
        if (i == 5 && k >= 9 && k < 13) grab_h((k - 3) % 10);
      }
      SB();
    });
    STAMP(14); FS(57);
    lds_w128(exw, pack8(cc[1][1][0], cc[1][1][1]));
    finish_u(1);
    bar_v<11>();
    flag_u(3);
    rx = lds_r128(exr);
#pragma unroll
    for (int j = 0; j < 8; ++j) pre[2][j] = acc8(cc[1][0], j) + half(r3[2], j) + half(rx, j);
    finish_u(2);
#pragma unroll
    for (int ps = 10; ps < 14; ++ps) issue_w2(ps, un2[ps]);
  } else {
    {
      const u32 part = (idx - 27) * 8 + w;
      u32 d = 0;
#pragma unroll 1
      for (u32 r = 384 + (XR + XS) * 128; r < 1024; r += 120) {
        touch(d, pw1, w1_line(r + part * 3, lane));
        asm volatile("s_barrier" ::: "memory");
      }
#pragma unroll
      for (int j = 0; j < SPW; ++j) {
        touch(d, pw2, ((g & 1) * 576 + min((TG < 192 ? 216 : 0) + T3R * 108 + (j * 40 + part) * 2 + (g >> 1), 575u)) * 4096 + l15 * 128);
        asm volatile("s_barrier" ::: "memory");
      }
      asm volatile("s_waitcnt vmcnt(0)" : "+v"(d)::"memory");
    }
    GRAB2_DEF;
    bar();
    if (w == 0) st_flag(ws + W_FLAGH + (rb2 * 8 + cj) * 128, tag);
#pragma unroll
    for (int ps = 0; ps < 7; ++ps) issue_w2(ps, un2[ps]);
    bar_v<4>();
#pragma unroll
    for (int ps = 0; ps < 5; ++ps) grab(ps, un2[ps]);
    bar();
#pragma unroll
    for (int ps = 7; ps < 14; ++ps) issue_w2(ps, un2[ps]);
  }
  STAMP(15); FS(58);

  {
    const u32 T0 = 9 * (Mq >> 1), TF = T0 + 5 * odd;
    u32 owners = (0x210310320321ull >> (12 * kq)) & 0xfffu;
    asm volatile("" : "+s"(owners));
    auto owner_of = [&](int b) __attribute__((always_inline)) -> u32 { return b == 4 ? kq : (owners >> (4 * (b - 1))) & 15u; };
    GRAB2_DEF;
    bar_v<8>();
    flag_u(4); flag_u(5);
    issue_w2(14, un2[14]);
#pragma unroll
    for (int ps = 5; ps < 10; ++ps) grab(ps, un2[ps]);
    u32 fo, fo2, fv = 0;
    {
      const u32 b = min(lane / 9, 4u);
      fo = ((T0 + 4) * 36 + kq * 9 + lane % 9) * 128;
      fo2 = lane < 60 ? ((TF + min(b, 3u)) * 36 + kq * 9 + lane % 9) * 128 : (W_FLAGH - W_FLAGE) + ((Nq * 2 + (lane & 1)) * 8 + Mq * 2 + ((lane >> 1) & 1)) * 128;
    }
    flags_request(w ^ 1, fv, ws + W_FLAGE, fo);
    bar_v<2>();
    issue_w2(15, un2[15]); issue_w2(16, un2[16]);
#pragma unroll
    for (int ps = 10; ps < 14; ++ps) grab(ps, un2[ps]);
    flags_check<4>(w ^ 1, fv, ws + W_FLAGE, fo, tag);
    const char* ub = ws + W_U + kq * (36 * 1024);
    u32 uo_[5], lx = (32 + w3) * 1024;
#pragma unroll
    for (int j = 0; j < 5; ++j) {
      const u32 b = j < 4 ? 8 * j + w : 32 + w3;
      uo_[j] = (((b / 9) & 1) * 144 + (b / 18) * 18 + b % 9) * 1024;
      asm volatile("" : "+s"(uo_[j]));
    }
    asm volatile("" : "+s"(lx));
    auto issue_u = [&](int i, int j) __attribute__((always_inline)) {
      const char* b = i == 0 ? ub + ((T0 + 4) * 2 + odd) * (144 * 1024) + (j < 4 ? w1k + j * 8192 : lx)
                             : ub + ((TF + owner_of((i + 1) >> 1)) * 288 + ((i + 1) & 1) * 9) * 1024 + uo_[j];
      if (j < 4) dma<64, true>(b, lane16, w1k, ((11 - i) & 3) * 36864 + j * 8192);
      else dma_if(kh, i & 1, b, lane16, lx, ((11 - i) & 3) * 36864);
    };
    bar();
#pragma unroll
    for (int j = 0; j < 5; ++j) issue_u(0, j);
    issue_w2(17, un2[17]);
    fo += (TF + owner_of(1) - (T0 + 4)) * (36 * 128);
    flags_request(w ^ 1, fv, ws + W_FLAGE, fo);
    STAMP(16); FS(59);
    const u32 grp = Mq * 16 + Nq;
    u32 exw = 147456 + w * 1024 + lane16, exr = 147456 + (w ^ 4) * 1024 + lane16;
    pin(exw); pin(exr);
    const LAddr ar(kh * 18432 + lane16);
    f32x4 cc[2][2][2];
    float v8[8], k8[8];
    u32x4 a[6], rx, pv[3];
    u32x2 hres[3], ps_[4]; u32 bb;
    char* ysb = ws + W_YS + (grp * 8 + w) * 2048;
    auto flag_y = [&](int b) __attribute__((always_inline)) {
      st_flag_if(w, ws + W_FLAGY + (b == 0 ? 1024 + grp * 4 + kq : (grp * 4 + kq) * 4 + owner_of(b ? b : 1)) * 128, tag);
    };
    auto rd = [&](int n) __attribute__((always_inline)) {
      a[n % 6] = lds_r128(ar(((11 - n / 18) & 3) * 36864 + (n < 18 ? n * 1024 : (n & 1) * 9216 + ((n % 18) >> 1) * 1024)));
    };
    auto half = [&](const u32x4& p, int j) __attribute__((always_inline)) -> float { return (j & 1) ? bfhi(p[j >> 1]) : bflo(p[j >> 1]); };
    auto half2 = [&](const u32x2& p, int j) __attribute__((always_inline)) -> float { return (j & 1) ? bfhi(p[j >> 1]) : bflo(p[j >> 1]); };
    const u32 srow = Nq * 128 + w3 * 32 + kh * 16 + l15;
    u32 oo = srow * 1152 + g * 8, l8 = lane * 8;
    pin(oo); pin(l8);
    flags_check<0>(w ^ 1, fv, ws + W_FLAGE, fo, tag);
    bar_vo<0>();
#pragma unroll
    for (int n = 0; n < 5; ++n) rd(n);
    SB();
    sfor<0, 324>([&](auto NN) __attribute__((always_inline)) {
      constexpr int G = decltype(NN)::value, n = G / 2, e = G & 1, i = n / 18, k = n % 18, g_ = G % 36, b = (i + 1) >> 1, x = (i & 1) ? k : 18 + k, pb = (b - 1) & 1;
      if (e == 0 && n + 5 < 162) rd(n + 5);
      if (i == 0 && e == 0 && k < 4) grab(14 + (k & 3), un2[14 + (k & 3)]);
      if (i == 0) cc[0][0][e] = mfma32(a[n % 6], wf[2 * k + e], k ? cc[0][0][e] : zero4);
      else cc[b & 1][x & 1][e] = mfma32(a[n % 6], wf[(x & ~1) + e], x > 1 ? cc[b & 1][x & 1][e] : zero4);
      SB();
      if (i & 1) {
        if (g_ == 4 || (g_ == 5 && i > 1)) { u32 px = pack2(cc[pb][e][1][0], cc[pb][e][1][1]), py = pack2(cc[pb][e][1][2], cc[pb][e][1][3]); pin(px); pin(py); rx[2 * e] = px; rx[2 * e + 1] = py; }
        constexpr int s0 = i == 7 ? 13 : 21, nv = i == 1 ? 4 : 8;
        if (g_ >= s0 && g_ < s0 + nv) { constexpr int j = (g_ - s0) & 7; v8[j] = cc[pb][j >> 2][0][j & 3] + half(rx, j); pin(v8[j]); }
        if (g_ >= s0 + nv && g_ < s0 + nv + nv / 4) { constexpr int q = (g_ - s0 - nv) & 1; u32 px = pack2(v8[4 * q], v8[4 * q + 1]), py = pack2(v8[4 * q + 2], v8[4 * q + 3]); pin(px); pin(py); rx[2 * q] = px; rx[2 * q + 1] = py; }
        if (g_ == s0 + 10) {
          if (i == 1) st64_sc1(ysb + kq * 512, l8, u32x2{rx.x, rx.y});
          else { const u32 o = owner_of(b > 1 ? b - 1 : 1); st128_sc1(ws + W_YP + (((grp * 4 + o) * 8 + w) * 3 + ((kq - o - 1) & 3u)) * 1024, lane16, rx); }
        }
      }
      if (e == 1) {
        if (i == 0 && k < 5) issue_u(1, k);
        if (i == 0 && k >= 13) issue_u(2, k - 13);
        if (i == 1 && k == 3) flags_check<5>(w ^ 1, fv, ws + W_FLAGE, fo2, tag);
        if (i == 1 && k == 12) bar_vo<7>();
        if (i == 6 && k == 3) flags_check<1>(w ^ 1, fv, ws + W_FLAGY, fo, tag);
        if (k == (i == 0 ? 12 : 4)) {
          constexpr int vm[9] = {0, 63, 5, 4, 5, 7, 8, 0, 0};
          if (i & 1) { if (i == 1) lds_w64(exw, u32x2{rx.x, rx.y}); else lds_w128(exw, rx); asm volatile("s_waitcnt lgkmcnt(0)" ::: "memory"); }
          asm volatile("s_waitcnt vmcnt(%0)\n\ts_barrier" ::"n"(vm[i]) : "memory");
          if (i == 3 || i == 5 || i == 7) flag_y(b - 2);
          if (i == 8) flag_y(3);
          if (i & 1) rx = lds_r128(exr);
        }
        if (i == 1 && k >= 4 && k < 9) issue_u(3, k - 4);
        if (i == 1 ? (k >= 9 && k < 14) : (k >= 5 && k < 10 && i > 0 && i + 3 < 9)) issue_u(i + 3, i == 1 ? k - 9 : k - 5);
        if (i == 0 && k == 12) flags_request(w ^ 1, fv, ws + W_FLAGE, fo2);
        if (i == 4 && (k == 11 || k == 13 || k == 15)) {
          const u32 c = (k == 15 ? (odd ? 0u : 128u) : odd * 16 + kq * 32 + (k - 11) * 8) + 4 * g;
          const u32 ho = srow * 144 + c * 2 + (c >= 72 ? 2048 * 144 - 144 : 0);
          if (k == 15) ld64_sc1_if(w3, kq, hres[2], ws + W_H + Mq * (2 * 2048 * 144), ho);
          else ld64_sc1(hres[((k - 11) >> 1) % 3], ws + W_H + Mq * (2 * 2048 * 144), ho);
        }
        if (i == 4 && k == 17) ld16(bb, P.b2, srow * 2);
        if (i == 5 && k == 13) { fo = (1024 + grp * 4 + p4) * 128; flags_request(w ^ 1, fv, ws + W_FLAGY, fo); }
        if (i == 6 && k >= 5 && k < 9) ld64_sc1_if(w3, kq, ps_[(k - 5) & 3], ysb + ((k - 5) & 3) * 512, l8);
        if (i == 8 && k == 6) fo = ((grp * 4 + ((kq + 1 + min(lane, 2u)) & 3u)) * 4 + kq) * 128;
        if (i == 8 && k == 13) flags_request(w ^ 1, fv, ws + W_FLAGY, fo);
      }
      if (i == 6 && g_ == 9) { waitv<5>(); pin(hres[0]); pin(hres[1]); pin(hres[2]); pin(bb); bb <<= 16; pin(bb); }
      if (i == 6 && g_ >= 20 && g_ < 28) { constexpr int j = (g_ - 20) & 7; k8[j] = half2(hres[j >> 2], j & 3) + asf(bb); pin(k8[j]); }
      if (i == 7 && g_ == 25) { waitv<1>(); pin(ps_[0]); pin(ps_[1]); pin(ps_[2]); pin(ps_[3]); }
      if (G >= 277 && G < 281) { constexpr int j = (G - 277) & 3; v8[j] = half2(hres[2], j) + asf(bb); pin(v8[j]); }
      if (G >= 281 && G < 297) { constexpr int j = (G - 281) & 3, q = ((G - 281) >> 2) & 3; v8[j] += half2(ps_[q], j); pin(v8[j]); }
      if (G == 299) {
        asm volatile("s_cmp_lg_u32 %3, %4\n\ts_cbranch_scc1 .Lso%=\n\tglobal_store_dwordx2 %0, %1, %2 sc1\n.Lso%=:"
                     ::"v"(oo), "v"(u32x2{pack2(v8[0], v8[1]), pack2(v8[2], v8[3])}), "s"(P.out + (T0 + 4) * 64 + odd * 32), "s"(w3), "s"(kq) : "memory", "scc");
      }
      SB();
    });
    {
      const u32x2 p2 = pack4(cc[0][0][1]), p3 = pack4(cc[0][1][1]);
      lds_w128(exw, u32x4{p2.x, p2.y, p3.x, p3.y});
    }
    flags_check<0>(w ^ 1, fv, ws + W_FLAGY, fo, tag);
    bar();
    STAMP(17); FS(60);
#pragma unroll
    for (int j = 0; j < 3; ++j) ld128_sc1(pv[j], ws + W_YP + ((grp * 4 + kq) * 8 + w) * 3072, lane16, j * 1024);
    rx = lds_r128(exr);
    f32x4 kv0 = f32x4{k8[0], k8[1], k8[2], k8[3]} + cc[0][0][0] + unpk(rx.x, rx.y), kv1 = f32x4{k8[4], k8[5], k8[6], k8[7]} + cc[0][1][0] + unpk(rx.z, rx.w);
    pin(kv0); pin(kv1);
    waitv<0>();
#pragma unroll
    for (int j = 0; j < 3; ++j) { pin(pv[j]); kv0 += unpk(pv[j].x, pv[j].y); kv1 += unpk(pv[j].z, pv[j].w); }
    const u32x2 o0 = pack4(kv0), o1 = pack4(kv1);
    auto s0 = __builtin_amdgcn_permlane16_swap(o0.x, o1.x, false, false);
    auto s1 = __builtin_amdgcn_permlane16_swap(o0.y, o1.y, false, false);
    asm volatile("global_store_dwordx4 %0, %1, %2 sc1\n\ts_nop 1" ::"v"(oo + (g & 1) * 24), "v"(u32x4{s0[0], s1[0], s0[1], s1[1]}), "s"(P.out + (TF + kq) * 64) : "memory");
  }
  asm volatile(".Lfk_code_end:");
  if (bid == 0 && w == 0) st_flag(ws + W_EPOCH, tag);
}
}

extern "C" void kernel_launch(void* const* d_in, const int* in_sizes, int n_in, void* d_out, int out_size, void* d_ws,
                              size_t ws_size, hipStream_t stream) {
  Params p;
  p.x = (const char*)d_in[0]; p.wqkv = (const char*)d_in[1]; p.wphi = (const char*)d_in[2]; p.wo = (const char*)d_in[3];
  p.w1 = (const char*)d_in[5]; p.b1 = (const char*)d_in[6]; p.w2 = (const char*)d_in[7]; p.b2 = (const char*)d_in[8];
  p.out = (char*)d_out; p.ws = (char*)d_ws;
  (void)hipFuncSetAttribute((const void*)fused_kernel, hipFuncAttributeMaxDynamicSharedMemorySize, LDS_BYTES);
  fused_kernel<<<NWG, NT, LDS_BYTES, stream>>>(p);
}
```
